```python
import torch

def _softmax(x, axis=-1):
    return torch.softmax(x, dim=axis)
def _gelu(x):
    return torch.nn.functional.gelu(x, approximate="tanh")
def _silu(x):
    return torch.nn.functional.silu(x)
def _sigmoid(x):
    return torch.sigmoid(x)
def _layernorm(x, axis=-1, eps=1e-5):
    m = x.mean(dim=axis, keepdim=True)
    v = x.var(dim=axis, keepdim=True, unbiased=False)
    return (x - m) / torch.sqrt(v + eps)
def _scatter_add(tgt, idx, src, axis=0):
    return tgt.clone().index_add_(axis, idx, src)
def _segment_sum(src, seg, n):
    out = torch.zeros((n,) + src.shape[1:], dtype=src.dtype)
    return out.index_add_(0, seg, src)
def _pad(x, pw):
    flat = tuple(v for pair in reversed(pw) for v in pair)
    return torch.nn.functional.pad(x, flat)

kernel_name = 'sequential_preLN__mha_block__dense_activation_mish__prompt'

def setup_inputs():

    _w = (lambda a: (lambda t: t.to(torch.bfloat16) if t.is_floating_point() else t)(torch.as_tensor(a)))
    def _A_setup():
        B, S, D, H = 1, 2048, 512, 32
        return {
            "x": _w((lambda *s: torch.randn(*s, dtype=torch.bfloat16) * 0.05)(B, S, D)),
            "wqkv": _w((lambda *s: torch.randn(*s, dtype=torch.bfloat16) * 0.05)(D, 3 * D)),
            "wo": _w((lambda *s: torch.randn(*s, dtype=torch.bfloat16) * 0.05)(D, D)),
        }
    _A_in = _A_setup()

    def _B_setup():
        B, S, D, F = 1, 2048, 512, 1376
        return {
            "x": _w((lambda *s: torch.randn(*s, dtype=torch.bfloat16) * 0.05)(B, S, D)),
            "w1": _w((lambda *s: torch.randn(*s, dtype=torch.bfloat16) * 0.8)(D, F) * (D ** -0.5)),
            "b1": _w((lambda *s: torch.randn(*s, dtype=torch.bfloat16) * 0.8)(F)),
            "w2": _w((lambda *s: torch.randn(*s, dtype=torch.bfloat16) * 0.2)(F, D) * (F ** -0.5)),
        }
    _B_in = _B_setup()

    return {
        "A_x": _A_in["x"],
        "A_wqkv": _A_in["wqkv"],
        "A_wo": _A_in["wo"],
        "B_x": _B_in["x"],
        "B_w1": _B_in["w1"],
        "B_b1": _B_in["b1"],
        "B_w2": _B_in["w2"],
    }

def reference(A_x, A_wqkv, A_wo, B_x, B_w1, B_b1, B_w2):
    def _A_ref(x, wqkv, wo):
        B, S, D = x.shape
        H = 32
        Dh = D // H
        qkv = (lambda x, *d: x.permute(*d))((x @ wqkv).reshape(B, S, 3, H, Dh), 2, 0, 3, 1, 4)
        q, k, v = qkv[0], qkv[1], qkv[2]
        scores = torch.einsum("bhqd,bhkd->bhqk", q, k) * (Dh ** -0.5)
        p = _softmax(scores, axis=-1)
        o = torch.einsum("bhqk,bhkd->bhqd", p, v)
        return (lambda x, *d: x.permute(*d))(o, 0, 2, 1, 3).reshape(B, S, D) @ wo

    def _B_ref(x, w1, b1, w2):
        h = torch.einsum("bsd,df->bsf", x, w1) + b1
        sp = torch.log1p(torch.exp(-torch.abs(h))) + torch.where(h > 0, h, torch.zeros_like(h))
        act = h * torch.tanh(sp)
        return torch.einsum("bsf,fd->bsd", act, w2)

    x = A_x
    h = x + _A_ref(_layernorm(x, axis=-1), A_wqkv, A_wo)
    h2 = h + _B_ref(_layernorm(h, axis=-1), B_w1, B_b1, B_w2)
    return h2
```

```cpp
#include <hip/hip_runtime.h>
#include <hip/hip_bf16.h>
#include <stdint.h>

typedef __attribute__((ext_vector_type(16))) float f32x16;
typedef __attribute__((ext_vector_type(8))) float f32x8;
typedef __attribute__((ext_vector_type(4))) float f32x4;
typedef __attribute__((ext_vector_type(2))) float f32x2;
typedef __attribute__((ext_vector_type(8))) __bf16 bf16x8;
typedef __attribute__((ext_vector_type(16))) __bf16 bf16x16;
typedef __attribute__((ext_vector_type(2))) __bf16 bf16x2;
typedef __attribute__((ext_vector_type(4))) short s16x4;
typedef __attribute__((ext_vector_type(4))) unsigned u32x4;
typedef __attribute__((ext_vector_type(2))) unsigned u32x2;
typedef unsigned short u16;
#define LAS __attribute__((address_space(3)))
#define GAS __attribute__((address_space(1)))
#define DEV __device__ __forceinline__

constexpr int S_ = 2048, D_ = 512, F_ = 1376;
constexpr int NSLOT = 5, SLOT = 32768;
constexpr float LOG2E = 1.4426950408889634f;

constexpr size_t WS_Q = 0, WS_K = 2u << 20, WS_V = 4u << 20, WS_O = 6u << 20, WS_H = 8u << 20, WS_ACT = 10u << 20;

DEV unsigned pack2(float a, float b) { f32x2 p = {a, b}; return __builtin_bit_cast(unsigned, __builtin_convertvector(p, bf16x2)); }
DEV float bflo(unsigned u) { return __builtin_bit_cast(float, u << 16); }
DEV float bfhi(unsigned u) { return __builtin_bit_cast(float, u & 0xffff0000u); }
DEV float dot2(unsigned a, unsigned b, float c) { return __builtin_amdgcn_fdot2_f32_bf16(__builtin_bit_cast(bf16x2, a), __builtin_bit_cast(bf16x2, b), c, false); }
DEV f32x16 mfma(u32x4 a, u32x4 b, f32x16 c) { return __builtin_amdgcn_mfma_f32_32x32x16_bf16(__builtin_bit_cast(bf16x8, a), __builtin_bit_cast(bf16x8, b), c, 0, 0, 0); }
DEV u32x2 tr_read(LAS char* p) { return __builtin_bit_cast(u32x2, __builtin_amdgcn_ds_read_tr16_b64_v4i16((LAS s16x4*)p)); }
DEV void glds16(const void* g, LAS char* l) { __builtin_amdgcn_global_load_lds((const GAS void*)g, (LAS void*)l, 16, 0, 0); }
DEV float xhalf_sum(float v) { return v + __shfl_xor(v, 32); }
#define BARRIER() asm volatile("s_waitcnt lgkmcnt(0)\n\ts_barrier" ::: "memory")
template <int N> DEV void wait_vm() { asm volatile("s_waitcnt vmcnt(%0)" ::"n"(N) : "memory"); }

template <int I, int N, class Fn> DEV void static_for(Fn&& f) {
  if constexpr (I < N) { f(std::integral_constant<int, I>{}); static_for<I + 1, N>(f); }
}

template <int NC, class Issue> DEV void ring_loader(Issue&& issue) {
  static_for<0, (NC < NSLOT ? NC : NSLOT)>([&](auto c) { issue(c.value); });
  static_for<0, NC>([&](auto cc) {
    constexpr int c = cc.value;
    constexpr int hi0 = (c < 2 ? NSLOT - 1 : c - 2 + NSLOT);
    constexpr int hi = hi0 < NC - 1 ? hi0 : NC - 1;
    wait_vm<8 * (hi - c)>();
    BARRIER();
    if constexpr (c >= 1 && c - 1 + NSLOT < NC) issue(c - 1 + NSLOT);
  });
}

template <int MODE>
__global__ __launch_bounds__(512, 2) void gemm_wide(const u16* __restrict__ A, const u16* __restrict__ W, const u16* __restrict__ bias, char* __restrict__ ws) {
  __shared__ __attribute__((aligned(16))) char smem_[NSLOT * SLOT];
  LAS char* smem = (LAS char*)smem_;
  const int tid = threadIdx.x, lane = tid & 63, wave = __builtin_amdgcn_readfirstlane(tid >> 6);
  const int b = blockIdx.x;
  int rg, cs, unit0 = 0, nunit = 6;
  if (MODE == 0) { cs = b & 7; rg = b >> 3; }
  else { rg = (b & 7) * 4 + (b >> 6); cs = (b >> 3) & 7; unit0 = cs <= 5 ? 5 * cs : 6 * cs - 5; nunit = cs < 5 ? 5 : 6; }
  constexpr int LDW = MODE == 0 ? 3 * D_ : F_;
  constexpr int NC = 8;
  if (wave >= 4) {
    const int lw = wave - 4;
    if (lw == 0) {
      if (MODE == 0) {
        const u16* p0 = A + (size_t)(rg * 64 + (lane >> 3)) * D_ + 8 * ((lane & 7) ^ (lane >> 4));
        const u16* p1 = A + (size_t)(rg * 64 + (lane >> 3)) * D_ + 8 * ((lane & 7) ^ (lane >> 4) ^ 4);
        ring_loader<NC>([&](int c) {
#pragma unroll
          for (int i = 0; i < 8; i++) glds16(((i & 1) ? p1 : p0) + i * 8 * D_ + c * 64, smem + (c % NSLOT) * SLOT + i * 1024);
        });
      } else {
        const u16* p0 = A + (size_t)rg * 2 * 32 * 512 + lane * 8;
        ring_loader<NC>([&](int c) {
#pragma unroll
          for (int i = 0; i < 8; i++) glds16(p0 + ((i >> 2) * 32 + 4 * c + (i & 3)) * 512, smem + (c % NSLOT) * SLOT + i * 1024);
        });
      }
    } else {
      const int sub = lw - 1;
      const int col0 = MODE == 0 ? sub * D_ + cs * 64 : unit0 * 32 + sub * 64;
      const u16* p0 = W + (size_t)(lane >> 3) * LDW + col0 + 8 * ((lane & 7) ^ (((lane >> 4) & 1) << 2));
      LAS char* l0 = smem + 8192 + sub * 8192;
      ring_loader<NC>([&](int c) {
#pragma unroll
        for (int i = 0; i < 8; i++) glds16(p0 + (size_t)(c * 64 + i * 8) * LDW, l0 + (c % NSLOT) * SLOT + i * 1024);
      });
    }
    return;
  }
  const int rb = wave >> 1, g = wave & 1;
  const int r32 = lane & 31, h = lane >> 5;
  float bv[3] = {0.f, 0.f, 0.f};
  if (MODE == 1) {
#pragma unroll
    for (int u = 0; u < 3; u++) { int n = (unit0 + 2 * u + g) * 32 + r32; n = n < F_ ? n : F_ - 1; bv[u] = bflo(bias[n]); }
  }
  int aoff[4];
  if (MODE == 0) {
    const int row = rb * 32 + r32;
#pragma unroll
    for (int ks = 0; ks < 4; ks++) aoff[ks] = row * 128 + (((2 * ks + h) ^ ((row >> 1) & 7)) << 4);
  } else {
#pragma unroll
    for (int ks = 0; ks < 4; ks++) aoff[ks] = (rb * 4 + ks) * 1024 + lane * 16;
  }
  const int q = (lane & 15) >> 2, p = lane & 3, blk = (lane >> 4) & 1;
  const int boff = 8192 + (8 * h + q) * 128 + (((4 * g + 2 * blk + (p >> 1)) ^ ((q >> 1) << 2)) << 4) + 8 * (p & 1);
  f32x16 acc[3];
#pragma unroll
  for (int u = 0; u < 3; u++) for (int i = 0; i < 16; i++) acc[u][i] = 0.f;
  float sx = 0.f, sxx = 0.f, cw[3] = {0.f, 0.f, 0.f};
#pragma unroll 1
  for (int c = 0; c < NC; c++) {
    BARRIER();
    LAS char* sl = smem + (c % NSLOT) * SLOT;
#pragma unroll
    for (int ks = 0; ks < 4; ks++) {
      const u32x4 a = *(LAS u32x4*)(sl + aoff[ks]);
#pragma unroll
      for (int u = 0; u < 3; u++) {
        const u32x2 w0 = tr_read(sl + boff + u * 8192 + ks * 2048), w1 = tr_read(sl + boff + u * 8192 + ks * 2048 + 512);
        const u32x4 w = {w0[0], w0[1], w1[0], w1[1]};
        if (MODE == 0 && u == 2) acc[u] = mfma(a, w, acc[u]); else acc[u] = mfma(w, a, acc[u]);
#pragma unroll
        for (int j = 0; j < 4; j++) cw[u] = dot2(w[j], 0x3f803f80u, cw[u]);
      }
#pragma unroll
      for (int j = 0; j < 4; j++) { sx = dot2(a[j], 0x3f803f80u, sx); sxx = dot2(a[j], a[j], sxx); }
    }
  }
  LAS float* scr = (LAS float*)(smem + ((NC - 2) % NSLOT) * SLOT + wave * 2048);
  sx = xhalf_sum(sx); sxx = xhalf_sum(sxx);
  const float mean = sx * (1.f / D_);
  const float var = fmaxf(sxx * (1.f / D_) - mean * mean, 0.f);
  const float rstd = __builtin_amdgcn_rsqf(var + 1e-5f);
#pragma unroll
  for (int u = 0; u < 3; u++) cw[u] = xhalf_sum(cw[u]);
  if (h == 0) {
#pragma unroll
    for (int u = 0; u < 3; u++) { scr[u * 32 + r32] = cw[u]; scr[128 + u * 32 + r32] = bv[u]; }
    scr[256 + r32] = mean; scr[288 + r32] = rstd;
  }
  const int tile = rg * 2 + rb;
  if (MODE == 0) {
#pragma unroll
    for (int u = 0; u < 2; u++) {
      const float sc = u == 0 ? rstd * (0.25f * LOG2E) : rstd;
      const float nm = -mean;
      unsigned o[8];
#pragma unroll
      for (int i = 0; i < 4; i++) {
        const f32x4 c4 = *(LAS f32x4*)(scr + u * 32 + 8 * i + 4 * h);
        o[2 * i] = pack2(fmaf(nm, c4[0], acc[u][4 * i]) * sc, fmaf(nm, c4[1], acc[u][4 * i + 1]) * sc);
        o[2 * i + 1] = pack2(fmaf(nm, c4[2], acc[u][4 * i + 2]) * sc, fmaf(nm, c4[3], acc[u][4 * i + 3]) * sc);
      }
      const int pos = u == 0 ? lane : (h * 32 + ((r32 & 19) | ((r32 & 4) << 1) | ((r32 & 8) >> 1)));
      char* dst = ws + (u == 0 ? WS_Q : WS_K) + (size_t)((cs * 4 + 2 * g) * 64 + tile) * 1024 + pos * 16;
      *(u32x4*)dst = u32x4{o[0], o[1], o[2], o[3]};
      *(u32x4*)(dst + 65536) = u32x4{o[4], o[5], o[6], o[7]};
    }
    {
      unsigned o[8];
      const float ncw = -cw[2];
#pragma unroll
      for (int i = 0; i < 4; i++) {
        const f32x4 m4 = *(LAS f32x4*)(scr + 256 + 8 * i + 4 * h), r4 = *(LAS f32x4*)(scr + 288 + 8 * i + 4 * h);
        o[2 * i] = pack2(fmaf(ncw, m4[0], acc[2][4 * i]) * r4[0], fmaf(ncw, m4[1], acc[2][4 * i + 1]) * r4[1]);
        o[2 * i + 1] = pack2(fmaf(ncw, m4[2], acc[2][4 * i + 2]) * r4[2], fmaf(ncw, m4[3], acc[2][4 * i + 3]) * r4[3]);
      }
      char* dst = ws + WS_V + (size_t)((cs * 4 + 2 * g + (r32 >> 4)) * 64 + tile) * 1024 + h * 256 + (r32 & 15) * 16;
      *(u32x4*)dst = u32x4{o[0], o[1], o[2], o[3]};
      *(u32x4*)(dst + 512) = u32x4{o[4], o[5], o[6], o[7]};
    }
  } else {
    const float nmr = -mean * rstd;
#pragma unroll
    for (int u = 0; u < 3; u++) {
      if (2 * u + g < nunit) {
        unsigned o[8];
#pragma unroll
        for (int i = 0; i < 4; i++) {
          const f32x4 c4 = *(LAS f32x4*)(scr + u * 32 + 8 * i + 4 * h), b4 = *(LAS f32x4*)(scr + 128 + u * 32 + 8 * i + 4 * h);
          float y[4];
#pragma unroll
          for (int j = 0; j < 4; j++) {
            const float v = fmaf(acc[u][4 * i + j], rstd, fmaf(nmr, c4[j], b4[j]));
            const float t = __builtin_amdgcn_exp2f(fminf(v * LOG2E, 40.f));
            const float n = t * (t + 2.f);
            y[j] = v * n * __builtin_amdgcn_rcpf(n + 2.f);
          }
          o[2 * i] = pack2(y[0], y[1]); o[2 * i + 1] = pack2(y[2], y[3]);
        }
        char* dst = ws + WS_ACT + (size_t)(tile * 86 + (unit0 + 2 * u + g) * 2) * 1024 + lane * 16;
        *(u32x4*)dst = u32x4{o[0], o[1], o[2], o[3]};
        *(u32x4*)(dst + 1024) = u32x4{o[4], o[5], o[6], o[7]};
      }
    }
  }
}

template <int MODE>
__global__ __launch_bounds__(512, 2) void gemm_sq(const u16* __restrict__ W, const u16* __restrict__ x, char* __restrict__ ws, u16* __restrict__ out) {
  __shared__ __attribute__((aligned(16))) char smem_[NSLOT * SLOT];
  LAS char* smem = (LAS char*)smem_;
  const int tid = threadIdx.x, lane = tid & 63, wave = __builtin_amdgcn_readfirstlane(tid >> 6);
  const int b = blockIdx.x;
  const int rg = (b & 7) * 4 + (b >> 6), cs = (b >> 3) & 7;
  constexpr int KS = MODE == 0 ? 32 : 86;
  constexpr int NC = (KS + 7) / 8;
  if (wave >= 4) {
    const int lw = wave - 4;
    if (lw < 2) {
      const char* p0 = ws + (MODE == 0 ? WS_O : WS_ACT) + (size_t)(rg * 2 + lw) * KS * 1024 + lane * 16;
      ring_loader<NC>([&](int c) {
#pragma unroll
        for (int i = 0; i < 8; i++) { const int ks = (c * 8 + i < KS) ? c * 8 + i : KS - 1; glds16(p0 + ks * 1024, smem + (c % NSLOT) * SLOT + lw * 8192 + i * 1024); }
      });
    } else {
      const u16* p0 = W + (size_t)((lw - 2) * 64 + (lane >> 3)) * D_ + cs * 64 + 8 * ((lane & 7) ^ (((lane >> 4) & 1) << 2));
      const int lim = KS * 16 - 8 - (lw - 2) * 64;
      ring_loader<NC>([&](int c) {
#pragma unroll
        for (int i = 0; i < 8; i++) { int k = c * 128 + i * 8; if (c == NC - 1) k = k < lim ? k : lim; glds16(p0 + (size_t)k * D_, smem + (c % NSLOT) * SLOT + lw * 8192 + i * 1024); }
      });
    }
    return;
  }
  const int rb = wave >> 1, cb = wave & 1;
  const int r32 = lane & 31, h = lane >> 5;
  const int tile = rg * 2 + rb;
  u32x4 res[2];
  char* hp = ws + WS_H + (size_t)(tile * 32 + cs * 4 + cb * 2) * 1024 + lane * 16;
  const size_t rowoff = (size_t)(tile * 32 + r32) * D_ + cs * 64 + cb * 32 + 8 * h;
  if (MODE == 0) { res[0] = *(const u32x4*)(x + rowoff); res[1] = *(const u32x4*)(x + rowoff + 16); }
  else { res[0] = *(const u32x4*)hp; res[1] = *(const u32x4*)(hp + 1024); }
  const int q = (lane & 15) >> 2, p = lane & 3, blk = (lane >> 4) & 1;
  const int aoff = rb * 8192 + lane * 16;
  const int boff = 16384 + (4 * h + q) * 128 + (((4 * cb + 2 * blk + (p & 1)) ^ ((q >> 1) << 2)) << 4) + 8 * (p >> 1);
  f32x16 acc;
#pragma unroll
  for (int i = 0; i < 16; i++) acc[i] = 0.f;
#pragma unroll 1
  for (int c = 0; c < NC; c++) {
    BARRIER();
    LAS char* sl = smem + (c % NSLOT) * SLOT;
#pragma unroll
    for (int ks = 0; ks < 8; ks++) {
      if (ks < 6 || KS % 8 == 0 || c < NC - 1) {
        const u32x4 a = *(LAS u32x4*)(sl + aoff + ks * 1024);
        const u32x2 w0 = tr_read(sl + boff + ks * 2048), w1 = tr_read(sl + boff + ks * 2048 + 1024);
        acc = mfma(u32x4{w0[0], w0[1], w1[0], w1[1]}, a, acc);
      }
    }
  }
#pragma unroll
  for (int s = 0; s < 2; s++) {
    u32x4 o;
#pragma unroll
    for (int j = 0; j < 4; j++) o[j] = pack2(acc[8 * s + 2 * j] + bflo(res[s][j]), acc[8 * s + 2 * j + 1] + bfhi(res[s][j]));
    if (MODE == 0) *(u32x4*)(hp + s * 1024) = o; else *(u32x4*)(out + rowoff + 16 * s) = o;
  }
}

DEV f32x16 pv(u32x4 v, f32x8 pr, f32x16 o) {
  return __builtin_amdgcn_smfmac_f32_32x32x32_bf16(__builtin_bit_cast(bf16x8, v), __builtin_bit_cast(bf16x16, pr), o, 0xDDDD, 0, 0);
}
DEV f32x8 exp8(f32x16 s, int o) { f32x8 r; for (int i = 0; i < 8; i++) r[i] = __builtin_amdgcn_exp2f(s[o + i]); return r; }

__global__ __launch_bounds__(512, 2) void attn(char* __restrict__ ws) {
  __shared__ __attribute__((aligned(16))) float red[8 * 9 * 64];
  const int tid = threadIdx.x, lane = tid & 63, wave = __builtin_amdgcn_readfirstlane(tid >> 6);
  const int b = blockIdx.x, hg = b & 7, rg = b >> 3;
  const int head = hg * 4 + (wave >> 1), kh = wave & 1;
  const char* qp = ws + WS_Q + (size_t)(head * 64 + rg * 2) * 1024 + lane * 16;
  const char* kp = ws + WS_K + (size_t)head * 65536 + lane * 16;
  const char* vp = ws + WS_V + (size_t)head * 65536 + (lane >> 5) * 256 + (lane & 15) * 16;
  const bool vl = (lane & 16) == 0;
  const u32x4 q0 = *(const u32x4*)qp, q1 = *(const u32x4*)(qp + 1024);
  const u32x4 ones = {0x3f803f80u, 0x3f803f80u, 0x3f803f80u, 0x3f803f80u};
  constexpr int PD = 4;
  u32x4 kf[PD], va[PD], vb[PD];
  const char* kp0 = kp + kh * 32768; const char* vp0 = vp + kh * 32768;
#pragma unroll
  for (int i = 0; i < PD; i++) {
    va[i] = ones; vb[i] = ones;
    kf[i] = *(const u32x4*)(kp0 + i * 1024);
    if (vl) { va[i] = *(const u32x4*)(vp0 + i * 1024); vb[i] = *(const u32x4*)(vp0 + i * 1024 + 512); }
  }
  f32x16 o0, o1, z;
#pragma unroll
  for (int i = 0; i < 16; i++) { o0[i] = 0.f; o1[i] = 0.f; z[i] = 0.f; }
  f32x16 s0 = mfma(kf[0], q0, z), s1 = mfma(kf[0], q1, z);
#pragma unroll 1
  for (int t0 = 0; t0 < 32; t0 += PD) {
#pragma unroll
    for (int i = 0; i < PD; i++) {
      const int tn = (t0 + i + PD) & 31;
      kf[i] = *(const u32x4*)(kp0 + tn * 1024);
      const u32x4 kn = kf[(i + 1) % PD];
      f32x8 pa = exp8(s0, 0), pb = exp8(s0, 8);
      s0 = mfma(kn, q0, z);
      o0 = pv(va[i], pa, o0); o0 = pv(vb[i], pb, o0);
      pa = exp8(s1, 0); pb = exp8(s1, 8);
      s1 = mfma(kn, q1, z);
      o1 = pv(va[i], pa, o1); o1 = pv(vb[i], pb, o1);
      if (vl) { va[i] = *(const u32x4*)(vp0 + tn * 1024); vb[i] = *(const u32x4*)(vp0 + tn * 1024 + 512); }
    }
  }
  f32x16 mine = kh ? o1 : o0, other = kh ? o0 : o1;
#pragma unroll
  for (int i = 0; i < 9; i++) red[(wave * 9 + i) * 64 + lane] = other[i];
  __syncthreads();
  float o[9];
#pragma unroll
  for (int i = 0; i < 9; i++) o[i] = mine[i] + red[((wave ^ 1) * 9 + i) * 64 + lane];
  const bool ok = o[8] >= 0x1p-40f && o[8] <= 0x1p40f;
  if (!__all(ok)) {
    const u32x4 qq = kh ? q1 : q0;
    f32x16 oc = z; float m = -__builtin_inff();
    u32x4 wa = ones, wb = ones;
#pragma unroll 1
    for (int t = 0; t < 64; t++) {
      const u32x4 kk = *(const u32x4*)(kp + t * 1024);
      if (vl) { wa = *(const u32x4*)(vp + t * 1024); wb = *(const u32x4*)(vp + t * 1024 + 512); }
      f32x16 s = mfma(kk, qq, z);
      float tm = s[0];
#pragma unroll
      for (int i = 1; i < 16; i++) tm = fmaxf(tm, s[i]);
      tm = fmaxf(tm, __shfl_xor(tm, 32));
      const float mn = fmaxf(m, tm), al = __builtin_amdgcn_exp2f(m - mn);
      m = mn;
#pragma unroll
      for (int i = 0; i < 16; i++) { oc[i] *= al; s[i] -= mn; }
      oc = pv(wa, exp8(s, 0), oc); oc = pv(wb, exp8(s, 8), oc);
    }
#pragma unroll
    for (int i = 0; i < 9; i++) o[i] = oc[i];
  }
  const float rl = 1.0f / o[8];
  u32x4 r;
#pragma unroll
  for (int j = 0; j < 4; j++) r[j] = pack2(o[2 * j] * rl, o[2 * j + 1] * rl);
  *(u32x4*)(ws + WS_O + (size_t)((rg * 2 + kh) * 32 + head) * 1024 + lane * 16) = r;
}

extern "C" void kernel_launch(void* const* d_in, const int* in_sizes, int n_in, void* d_out, int out_size, void* d_ws, size_t ws_size, hipStream_t stream) {
  const u16* x = (const u16*)d_in[0];
  const u16* wqkv = (const u16*)d_in[1];
  const u16* wo = (const u16*)d_in[2];
  const u16* w1 = (const u16*)d_in[4];
  const u16* b1 = (const u16*)d_in[5];
  const u16* w2 = (const u16*)d_in[6];
  char* ws = (char*)d_ws;
  gemm_wide<0><<<256, 512, 0, stream>>>(x, wqkv, b1, ws);
  attn<<<256, 512, 0, stream>>>(ws);
  gemm_sq<0><<<256, 512, 0, stream>>>(wo, x, ws, (u16*)d_out);
  gemm_wide<1><<<256, 512, 0, stream>>>((const u16*)(ws + WS_H), w1, b1, ws);
  gemm_sq<1><<<256, 512, 0, stream>>>(w2, x, ws, (u16*)d_out);
}
```

```cpp
#include <hip/hip_runtime.h>
#include <hip/hip_bf16.h>
#include <stdint.h>

typedef __attribute__((ext_vector_type(16))) float f32x16;
typedef __attribute__((ext_vector_type(8))) float f32x8;
typedef __attribute__((ext_vector_type(4))) float f32x4;
typedef __attribute__((ext_vector_type(2))) float f32x2;
typedef __attribute__((ext_vector_type(8))) __bf16 bf16x8;
typedef __attribute__((ext_vector_type(16))) __bf16 bf16x16;
typedef __attribute__((ext_vector_type(2))) __bf16 bf16x2;
typedef __attribute__((ext_vector_type(4))) short s16x4;
typedef __attribute__((ext_vector_type(4))) unsigned u32x4;
typedef __attribute__((ext_vector_type(2))) unsigned u32x2;
typedef unsigned short u16;
#define LAS __attribute__((address_space(3)))
#define GAS __attribute__((address_space(1)))
#define DEV __device__ __forceinline__

constexpr int S_ = 2048, D_ = 512, F_ = 1376;
constexpr int NSLOT = 5, SLOT = 32768;
constexpr float LOG2E = 1.4426950408889634f;

constexpr size_t WS_Q = 0, WS_K = 2u << 20, WS_V = 4u << 20, WS_O = 6u << 20, WS_H = 8u << 20, WS_ACT = 10u << 20;

DEV unsigned pack2(float a, float b) { f32x2 p = {a, b}; return __builtin_bit_cast(unsigned, __builtin_convertvector(p, bf16x2)); }
DEV float bflo(unsigned u) { return __builtin_bit_cast(float, u << 16); }
DEV float bfhi(unsigned u) { return __builtin_bit_cast(float, u & 0xffff0000u); }
DEV float dot2(unsigned a, unsigned b, float c) { return __builtin_amdgcn_fdot2_f32_bf16(__builtin_bit_cast(bf16x2, a), __builtin_bit_cast(bf16x2, b), c, false); }
DEV f32x16 mfma(u32x4 a, u32x4 b, f32x16 c) { return __builtin_amdgcn_mfma_f32_32x32x16_bf16(__builtin_bit_cast(bf16x8, a), __builtin_bit_cast(bf16x8, b), c, 0, 0, 0); }
DEV u32x2 tr_read(LAS char* p) { return __builtin_bit_cast(u32x2, __builtin_amdgcn_ds_read_tr16_b64_v4i16((LAS s16x4*)p)); }
DEV void glds16(const void* g, LAS char* l) { __builtin_amdgcn_global_load_lds((const GAS void*)g, (LAS void*)l, 16, 0, 0); }
DEV float xhalf_sum(float v) { return v + __shfl_xor(v, 32); }
#define BARRIER() asm volatile("s_waitcnt lgkmcnt(0)\n\ts_barrier" ::: "memory")
template <int N> DEV void wait_vm() { asm volatile("s_waitcnt vmcnt(%0)" ::"n"(N) : "memory"); }

#define NKID 5

#define STAMP(k, i)

template <int I, int N, class Fn> DEV void static_for(Fn&& f) {
  if constexpr (I < N) { f(std::integral_constant<int, I>{}); static_for<I + 1, N>(f); }
}

template <int NC, class Issue> DEV void ring_loader(Issue&& issue) {
  static_for<0, (NC < NSLOT ? NC : NSLOT)>([&](auto c) { issue(c.value); });
  static_for<0, NC>([&](auto cc) {
    constexpr int c = cc.value;
    constexpr int hi0 = (c < 2 ? NSLOT - 1 : c - 2 + NSLOT);
    constexpr int hi = hi0 < NC - 1 ? hi0 : NC - 1;
    wait_vm<8 * (hi - c)>();
    BARRIER();
    if constexpr (c >= 1 && c - 1 + NSLOT < NC) issue(c - 1 + NSLOT);
  });
}

template <int MODE>
__global__ __launch_bounds__(512, 2) void gemm_wide(const u16* __restrict__ A, const u16* __restrict__ W, const u16* __restrict__ bias, char* __restrict__ ws) {
  __shared__ __attribute__((aligned(16))) char smem_[NSLOT * SLOT];
  LAS char* smem = (LAS char*)smem_;
  const int tid = threadIdx.x, lane = tid & 63, wave = __builtin_amdgcn_readfirstlane(tid >> 6);
  const int b = blockIdx.x;
  int rg, cs, unit0 = 0, nunit = 6;
  if (MODE == 0) { cs = b & 7; rg = b >> 3; }
  else { rg = (b & 7) * 4 + (b >> 6); cs = (b >> 3) & 7; unit0 = cs <= 5 ? 5 * cs : 6 * cs - 5; nunit = cs < 5 ? 5 : 6; }
  constexpr int LDW = MODE == 0 ? 3 * D_ : F_;
  constexpr int NC = 8;
  STAMP(MODE ? 3 : 0, 0);
  if (wave >= 4) {
    const int lw = wave - 4;
    if (lw == 0) {
      if (MODE == 0) {
        const u16* p0 = A + (size_t)(rg * 64 + (lane >> 3)) * D_ + 8 * ((lane & 7) ^ (lane >> 4));
        const u16* p1 = A + (size_t)(rg * 64 + (lane >> 3)) * D_ + 8 * ((lane & 7) ^ (lane >> 4) ^ 4);
        ring_loader<NC>([&](int c) {
#pragma unroll
          for (int i = 0; i < 8; i++) glds16(((i & 1) ? p1 : p0) + i * 8 * D_ + c * 64, smem + (c % NSLOT) * SLOT + i * 1024);
        });
      } else {
        const u16* p0 = A + (size_t)rg * 2 * 32 * 512 + lane * 8;
        ring_loader<NC>([&](int c) {
#pragma unroll
          for (int i = 0; i < 8; i++) glds16(p0 + ((i >> 2) * 32 + 4 * c + (i & 3)) * 512, smem + (c % NSLOT) * SLOT + i * 1024);
        });
      }
    } else {
      const int sub = lw - 1;
      const int col0 = MODE == 0 ? sub * D_ + cs * 64 : unit0 * 32 + sub * 64;
      const u16* p0 = W + (size_t)(lane >> 3) * LDW + col0 + 8 * ((lane & 7) ^ (((lane >> 4) & 1) << 2));
      LAS char* l0 = smem + 8192 + sub * 8192;
      ring_loader<NC>([&](int c) {
#pragma unroll
        for (int i = 0; i < 8; i++) glds16(p0 + (size_t)(c * 64 + i * 8) * LDW, l0 + (c % NSLOT) * SLOT + i * 1024);
      });
    }
    STAMP(MODE ? 3 : 0, 2);
    return;
  }
  const int rb = wave >> 1, g = wave & 1;
  const int r32 = lane & 31, h = lane >> 5;
  float bv[3] = {0.f, 0.f, 0.f};
  if (MODE == 1) {
#pragma unroll
    for (int u = 0; u < 3; u++) { int n = (unit0 + 2 * u + g) * 32 + r32; n = n < F_ ? n : F_ - 1; bv[u] = bflo(bias[n]); }
  }
  int aoff[4];
  if (MODE == 0) {
    const int row = rb * 32 + r32;
#pragma unroll
    for (int ks = 0; ks < 4; ks++) aoff[ks] = row * 128 + (((2 * ks + h) ^ ((row >> 1) & 7)) << 4);
  } else {
#pragma unroll
    for (int ks = 0; ks < 4; ks++) aoff[ks] = (rb * 4 + ks) * 1024 + lane * 16;
  }
  const int q = (lane & 15) >> 2, p = lane & 3, blk = (lane >> 4) & 1;
  const int boff = 8192 + (8 * h + q) * 128 + (((4 * g + 2 * blk + (p >> 1)) ^ ((q >> 1) << 2)) << 4) + 8 * (p & 1);
  f32x16 acc[3];
#pragma unroll
  for (int u = 0; u < 3; u++) for (int i = 0; i < 16; i++) acc[u][i] = 0.f;
  float sx = 0.f, sxx = 0.f, cw[3] = {0.f, 0.f, 0.f};
#pragma unroll 1
  for (int c = 0; c < NC; c++) {
    BARRIER();
    STAMP(MODE ? 3 : 0, 4 + c);
    LAS char* sl = smem + (c % NSLOT) * SLOT;
#pragma unroll
    for (int ks = 0; ks < 4; ks++) {
      const u32x4 a = *(LAS u32x4*)(sl + aoff[ks]);
#pragma unroll
      for (int u = 0; u < 3; u++) {
        const u32x2 w0 = tr_read(sl + boff + u * 8192 + ks * 2048), w1 = tr_read(sl + boff + u * 8192 + ks * 2048 + 512);
        const u32x4 w = {w0[0], w0[1], w1[0], w1[1]};
        if (MODE == 0 && u == 2) acc[u] = mfma(a, w, acc[u]); else acc[u] = mfma(w, a, acc[u]);
#pragma unroll
        for (int j = 0; j < 4; j++) cw[u] = dot2(w[j], 0x3f803f80u, cw[u]);
      }
#pragma unroll
      for (int j = 0; j < 4; j++) { sx = dot2(a[j], 0x3f803f80u, sx); sxx = dot2(a[j], a[j], sxx); }
    }
  }
  STAMP(MODE ? 3 : 0, 2);
  LAS float* scr = (LAS float*)(smem + ((NC - 2) % NSLOT) * SLOT + wave * 2048);
  sx = xhalf_sum(sx); sxx = xhalf_sum(sxx);
  const float mean = sx * (1.f / D_);
  const float var = fmaxf(sxx * (1.f / D_) - mean * mean, 0.f);
  const float rstd = __builtin_amdgcn_rsqf(var + 1e-5f);
#pragma unroll
  for (int u = 0; u < 3; u++) cw[u] = xhalf_sum(cw[u]);
  if (h == 0) {
#pragma unroll
    for (int u = 0; u < 3; u++) { scr[u * 32 + r32] = cw[u]; scr[128 + u * 32 + r32] = bv[u]; }
    scr[256 + r32] = mean; scr[288 + r32] = rstd;
  }
  const int tile = rg * 2 + rb;
  if (MODE == 0) {
#pragma unroll
    for (int u = 0; u < 2; u++) {
      const float sc = u == 0 ? rstd * (0.25f * LOG2E) : rstd;
      const float nm = -mean;
      unsigned o[8];
#pragma unroll
      for (int i = 0; i < 4; i++) {
        const f32x4 c4 = *(LAS f32x4*)(scr + u * 32 + 8 * i + 4 * h);
        o[2 * i] = pack2(fmaf(nm, c4[0], acc[u][4 * i]) * sc, fmaf(nm, c4[1], acc[u][4 * i + 1]) * sc);
        o[2 * i + 1] = pack2(fmaf(nm, c4[2], acc[u][4 * i + 2]) * sc, fmaf(nm, c4[3], acc[u][4 * i + 3]) * sc);
      }
      const int pos = u == 0 ? lane : (h * 32 + ((r32 & 19) | ((r32 & 4) << 1) | ((r32 & 8) >> 1)));
      char* dst = ws + (u == 0 ? WS_Q : WS_K) + (size_t)((cs * 4 + 2 * g) * 64 + tile) * 1024 + pos * 16;
      *(u32x4*)dst = u32x4{o[0], o[1], o[2], o[3]};
      *(u32x4*)(dst + 65536) = u32x4{o[4], o[5], o[6], o[7]};
    }
    {
      unsigned o[8];
      const float ncw = -cw[2];
#pragma unroll
      for (int i = 0; i < 4; i++) {
        const f32x4 m4 = *(LAS f32x4*)(scr + 256 + 8 * i + 4 * h), r4 = *(LAS f32x4*)(scr + 288 + 8 * i + 4 * h);
        o[2 * i] = pack2(fmaf(ncw, m4[0], acc[2][4 * i]) * r4[0], fmaf(ncw, m4[1], acc[2][4 * i + 1]) * r4[1]);
        o[2 * i + 1] = pack2(fmaf(ncw, m4[2], acc[2][4 * i + 2]) * r4[2], fmaf(ncw, m4[3], acc[2][4 * i + 3]) * r4[3]);
      }
      char* dst = ws + WS_V + (size_t)((cs * 4 + 2 * g + (r32 >> 4)) * 64 + tile) * 1024 + h * 256 + (r32 & 15) * 16;
      *(u32x4*)dst = u32x4{o[0], o[1], o[2], o[3]};
      *(u32x4*)(dst + 512) = u32x4{o[4], o[5], o[6], o[7]};
    }
  } else {
    const float nmr = -mean * rstd;
#pragma unroll
    for (int u = 0; u < 3; u++) {
      if (2 * u + g < nunit) {
        unsigned o[8];
#pragma unroll
        for (int i = 0; i < 4; i++) {
          const f32x4 c4 = *(LAS f32x4*)(scr + u * 32 + 8 * i + 4 * h), b4 = *(LAS f32x4*)(scr + 128 + u * 32 + 8 * i + 4 * h);
          float y[4];
#pragma unroll
          for (int j = 0; j < 4; j++) {
            const float v = fmaf(acc[u][4 * i + j], rstd, fmaf(nmr, c4[j], b4[j]));
            const float t = __builtin_amdgcn_exp2f(fminf(v * LOG2E, 40.f));
            const float n = t * (t + 2.f);
            y[j] = v * n * __builtin_amdgcn_rcpf(n + 2.f);
          }
          o[2 * i] = pack2(y[0], y[1]); o[2 * i + 1] = pack2(y[2], y[3]);
        }
        char* dst = ws + WS_ACT + (size_t)(tile * 86 + (unit0 + 2 * u + g) * 2) * 1024 + lane * 16;
        *(u32x4*)dst = u32x4{o[0], o[1], o[2], o[3]};
        *(u32x4*)(dst + 1024) = u32x4{o[4], o[5], o[6], o[7]};
      }
    }
  }
  STAMP(MODE ? 3 : 0, 3);
}

template <int MODE>
__global__ __launch_bounds__(512, 2) void gemm_sq(const u16* __restrict__ W, const u16* __restrict__ x, char* __restrict__ ws, u16* __restrict__ out) {
  __shared__ __attribute__((aligned(16))) char smem_[NSLOT * SLOT];
  LAS char* smem = (LAS char*)smem_;
  const int tid = threadIdx.x, lane = tid & 63, wave = __builtin_amdgcn_readfirstlane(tid >> 6);
  const int b = blockIdx.x;
  const int rg = (b & 7) * 4 + (b >> 6), cs = (b >> 3) & 7;
  constexpr int KS = MODE == 0 ? 32 : 86;
  constexpr int NC = (KS + 7) / 8;
  STAMP(MODE ? 4 : 2, 0);
  if (wave >= 4) {
    const int lw = wave - 4;
    if (lw < 2) {
      const char* p0 = ws + (MODE == 0 ? WS_O : WS_ACT) + (size_t)(rg * 2 + lw) * KS * 1024 + lane * 16;
      ring_loader<NC>([&](int c) {
#pragma unroll
        for (int i = 0; i < 8; i++) { const int ks = (c * 8 + i < KS) ? c * 8 + i : KS - 1; glds16(p0 + ks * 1024, smem + (c % NSLOT) * SLOT + lw * 8192 + i * 1024); }
      });
    } else {
      const u16* p0 = W + (size_t)((lw - 2) * 64 + (lane >> 3)) * D_ + cs * 64 + 8 * ((lane & 7) ^ (((lane >> 4) & 1) << 2));
      const int lim = KS * 16 - 8 - (lw - 2) * 64;
      ring_loader<NC>([&](int c) {
#pragma unroll
        for (int i = 0; i < 8; i++) { int k = c * 128 + i * 8; if (c == NC - 1) k = k < lim ? k : lim; glds16(p0 + (size_t)k * D_, smem + (c % NSLOT) * SLOT + lw * 8192 + i * 1024); }
      });
    }
    STAMP(MODE ? 4 : 2, 2);
    return;
  }
  const int rb = wave >> 1, cb = wave & 1;
  const int r32 = lane & 31, h = lane >> 5;
  const int tile = rg * 2 + rb;
  u32x4 res[2];
  char* hp = ws + WS_H + (size_t)(tile * 32 + cs * 4 + cb * 2) * 1024 + lane * 16;
  const size_t rowoff = (size_t)(tile * 32 + r32) * D_ + cs * 64 + cb * 32 + 8 * h;
  if (MODE == 0) { res[0] = *(const u32x4*)(x + rowoff); res[1] = *(const u32x4*)(x + rowoff + 16); }
  else { res[0] = *(const u32x4*)hp; res[1] = *(const u32x4*)(hp + 1024); }
  const int q = (lane & 15) >> 2, p = lane & 3, blk = (lane >> 4) & 1;
  const int aoff = rb * 8192 + lane * 16;
  const int boff = 16384 + (4 * h + q) * 128 + (((4 * cb + 2 * blk + (p & 1)) ^ ((q >> 1) << 2)) << 4) + 8 * (p >> 1);
  f32x16 acc;
#pragma unroll
  for (int i = 0; i < 16; i++) acc[i] = 0.f;
#pragma unroll 1
  for (int c = 0; c < NC; c++) {
    BARRIER();
    STAMP(MODE ? 4 : 2, 4 + c);
    LAS char* sl = smem + (c % NSLOT) * SLOT;
#pragma unroll
    for (int ks = 0; ks < 8; ks++) {
      if (ks < 6 || KS % 8 == 0 || c < NC - 1) {
        const u32x4 a = *(LAS u32x4*)(sl + aoff + ks * 1024);
        const u32x2 w0 = tr_read(sl + boff + ks * 2048), w1 = tr_read(sl + boff + ks * 2048 + 1024);
        acc = mfma(u32x4{w0[0], w0[1], w1[0], w1[1]}, a, acc);
      }
    }
  }
  STAMP(MODE ? 4 : 2, 2);
#pragma unroll
  for (int s = 0; s < 2; s++) {
    u32x4 o;
#pragma unroll
    for (int j = 0; j < 4; j++) o[j] = pack2(acc[8 * s + 2 * j] + bflo(res[s][j]), acc[8 * s + 2 * j + 1] + bfhi(res[s][j]));
    if (MODE == 0) *(u32x4*)(hp + s * 1024) = o; else *(u32x4*)(out + rowoff + 16 * s) = o;
  }
  STAMP(MODE ? 4 : 2, 3);
}

DEV f32x16 pv(u32x4 v, f32x8 pr, f32x16 o) {
  return __builtin_amdgcn_smfmac_f32_32x32x32_bf16(__builtin_bit_cast(bf16x8, v), __builtin_bit_cast(bf16x16, pr), o, 0xDDDD, 0, 0);
}
DEV f32x8 exp8(f32x16 s, int o) { f32x8 r; for (int i = 0; i < 8; i++) r[i] = __builtin_amdgcn_exp2f(s[o + i]); return r; }

__global__ __launch_bounds__(512, 2) void attn(char* __restrict__ ws) {
  __shared__ __attribute__((aligned(16))) float red[8 * 9 * 64];
  const int tid = threadIdx.x, lane = tid & 63, wave = __builtin_amdgcn_readfirstlane(tid >> 6);
  const int b = blockIdx.x, hg = b & 7, rg = b >> 3;
  const int head = hg * 4 + (wave >> 1), kh = wave & 1;
  STAMP(1, 0);
  const char* qb = ws + WS_Q + (size_t)(head * 64 + rg * 2) * 1024;
  const char* kp = ws + WS_K + (size_t)head * 65536 + lane * 16;
  const char* vp = ws + WS_V + (size_t)head * 65536 + (lane >> 5) * 256 + (lane & 15) * 16;
  const bool vl = (lane & 16) == 0;
  const u32x4 ones = {0x3f803f80u, 0x3f803f80u, 0x3f803f80u, 0x3f803f80u};
  float oa[9], ob[9];
  {
    const unsigned koff = lane * 16, voff = (lane >> 5) * 256 + (lane & 15) * 16;
    const char* kb = ws + WS_K + (size_t)head * 65536 + kh * 32768;
    const char* vb = ws + WS_V + (size_t)head * 65536 + kh * 32768;
    const unsigned vinit = ((lane & 31) == 16 || (lane & 31) == 20) ? 0x3f803f80u : 0u;
    int cnt = 8;
#define E1(r) "v_exp_f32 v" #r ", v" #r "\n\t"
#define SM(o, a, p) "v_smfmac_f32_32x32x32_bf16 v[" o "], v[" a "], v[" p "], v176\n\t"
#define QK(s, k, q) "v_mfma_f32_32x32x16_bf16 v[" s "], v[" k "], " q ", 0\n\t"
#define LDK(k, off) "global_load_dwordx4 v[" k "], %[koff], s[40:41] offset:" #off "\n\t"
#define LDV(a, b, offa, offb) "s_mov_b64 exec, %[vm]\n\tglobal_load_dwordx4 v[" a "], %[voff], s[42:43] offset:" #offa "\n\tglobal_load_dwordx4 v[" b "], %[voff], s[42:43] offset:" #offb "\n\ts_mov_b64 exec, -1\n\t"
#define ITER(kc, kn, vap, vbp, vac, vbc, ok, oa_, ob_)                                                        \
      E1(96) E1(97) SM("80:95", vbp, "120:127") E1(98) E1(99) E1(100) QK("112:127", kc, "v[184:187]")              \
      LDK(kc, ok) LDV(vap, vbp, oa_, ob_) E1(101) E1(102) E1(103) E1(104) E1(105) "s_waitcnt vmcnt(9)\n\t"    \
      SM("64:79", vac, "96:103") E1(106) E1(107) E1(108) E1(109) E1(110) E1(111)                              \
      E1(112) E1(113) SM("64:79", vbc, "104:111") E1(114) E1(115) E1(116) QK("96:111", kn, "v[180:183]")           \
      E1(117) E1(118) E1(119) E1(120) E1(121) SM("80:95", vac, "112:119") E1(122) E1(123) E1(124) E1(125) E1(126) E1(127)
#define Z(r) "v_mov_b32 v" #r ", 0\n\t"
#define VI(r) "v_mov_b32 v" #r ", %[vinit]\n\t"
#define OUT(n, r) "v_mov_b32 %" #n ", v" #r "\n\t"
    asm volatile(
      "s_mov_b64 s[40:41], %[kin]\n\ts_mov_b64 s[42:43], %[vin]\n\ts_nop 4\n\t"
      "global_load_dwordx4 v[180:183], %[koff], %[qin]\n\tglobal_load_dwordx4 v[184:187], %[koff], %[qin] offset:1024\n\t"
      LDK("128:131", 0)
      VI(144) VI(145) VI(146) VI(147) VI(160) VI(161) VI(162) VI(163)
      LDK("132:135", 1024) LDV("144:147", "160:163", 0, 512)
      VI(148) VI(149) VI(150) VI(151) VI(164) VI(165) VI(166) VI(167)
      LDK("136:139", 2048) LDV("148:151", "164:167", 1024, 1536)
      VI(152) VI(153) VI(154) VI(155) VI(168) VI(169) VI(170) VI(171)
      LDK("140:143", 3072) LDV("152:155", "168:171", 2048, 2560)
      VI(156) VI(157) VI(158) VI(159) VI(172) VI(173) VI(174) VI(175)
      Z(64) Z(65) Z(66) Z(67) Z(68) Z(69) Z(70) Z(71) Z(72) Z(73) Z(74) Z(75) Z(76) Z(77) Z(78) Z(79)
      Z(80) Z(81) Z(82) Z(83) Z(84) Z(85) Z(86) Z(87) Z(88) Z(89) Z(90) Z(91) Z(92) Z(93) Z(94) Z(95)
      Z(120) Z(121) Z(122) Z(123) Z(124) Z(125) Z(126) Z(127)
      "v_mov_b32 v176, 0xdddd\n\t"
      "s_add_u32 s40, s40, 4096\n\ts_addc_u32 s41, s41, 0\n\t"
      "s_add_u32 s42, s42, 3072\n\ts_addc_u32 s43, s43, 0\n\t"
      "s_waitcnt vmcnt(9)\n\t"
      QK("96:111", "128:131", "v[180:183]")
      "s_nop 15\n\t"
      "1:\n\t"
      ITER("128:131", "132:135", "156:159", "172:175", "144:147", "160:163", 0, 0, 512)
      ITER("132:135", "136:139", "144:147", "160:163", "148:151", "164:167", 1024, 1024, 1536)
      ITER("136:139", "140:143", "148:151", "164:167", "152:155", "168:171", 2048, 2048, 2560)
      ITER("140:143", "128:131", "152:155", "168:171", "156:159", "172:175", 3072, 3072, 3584)
      "s_add_u32 s40, s40, 4096\n\ts_addc_u32 s41, s41, 0\n\t"
      "s_add_u32 s42, s42, 4096\n\ts_addc_u32 s43, s43, 0\n\t"
      "s_sub_u32 %[cnt], %[cnt], 1\n\ts_cmp_lg_u32 %[cnt], 0\n\ts_cbranch_scc1 1b\n\t"
      SM("80:95", "172:175", "120:127")
      "s_waitcnt vmcnt(0)\n\ts_nop 15\n\t"
      OUT(0, 64) OUT(1, 65) OUT(2, 66) OUT(3, 67) OUT(4, 68) OUT(5, 69) OUT(6, 70) OUT(7, 71) OUT(8, 72)
      OUT(9, 80) OUT(10, 81) OUT(11, 82) OUT(12, 83) OUT(13, 84) OUT(14, 85) OUT(15, 86) OUT(16, 87) OUT(17, 88)
      : "=&v"(oa[0]), "=&v"(oa[1]), "=&v"(oa[2]), "=&v"(oa[3]), "=&v"(oa[4]), "=&v"(oa[5]), "=&v"(oa[6]), "=&v"(oa[7]), "=&v"(oa[8]),
        "=&v"(ob[0]), "=&v"(ob[1]), "=&v"(ob[2]), "=&v"(ob[3]), "=&v"(ob[4]), "=&v"(ob[5]), "=&v"(ob[6]), "=&v"(ob[7]), "=&v"(ob[8]),
        [cnt] "+s"(cnt)
      : [qin] "s"(qb), [koff] "v"(koff), [voff] "v"(voff), [vinit] "v"(vinit), [vm] "s"(0x0000ffff0000ffffull),
        [kin] "s"(kb), [vin] "s"(vb)
      : "memory", "scc", "s40", "s41", "s42", "s43",
        "v64","v65","v66","v67","v68","v69","v70","v71","v72","v73","v74","v75","v76","v77","v78","v79","v80","v81","v82","v83","v84","v85","v86","v87","v88","v89","v90","v91","v92","v93","v94","v95",
        "v96","v97","v98","v99","v100","v101","v102","v103","v104","v105","v106","v107","v108","v109","v110","v111","v112","v113","v114","v115","v116","v117","v118","v119","v120","v121","v122","v123","v124","v125","v126","v127",
        "v128","v129","v130","v131","v132","v133","v134","v135","v136","v137","v138","v139","v140","v141","v142","v143","v144","v145","v146","v147","v148","v149","v150","v151","v152","v153","v154","v155","v156","v157","v158","v159",
        "v160","v161","v162","v163","v164","v165","v166","v167","v168","v169","v170","v171","v172","v173","v174","v175","v176","v180","v181","v182","v183","v184","v185","v186","v187");
  }
  f32x16 z;
#pragma unroll
  for (int i = 0; i < 16; i++) z[i] = 0.f;
  STAMP(1, 2);
#pragma unroll
  for (int i = 0; i < 9; i++) red[(wave * 9 + i) * 64 + lane] = kh ? oa[i] : ob[i];
  __syncthreads();
  float o[9];
#pragma unroll
  for (int i = 0; i < 9; i++) o[i] = (kh ? ob[i] : oa[i]) + red[((wave ^ 1) * 9 + i) * 64 + lane];
  const bool ok = o[8] >= 0x1p-40f && o[8] <= 0x1p40f;
  if (!__all(ok)) {
    const u32x4 qq = *(const u32x4*)(qb + kh * 1024 + lane * 16);
    f32x16 oc = z; float m = -__builtin_inff();
    u32x4 wa = ones, wb = ones;
#pragma unroll 1
    for (int t = 0; t < 64; t++) {
      const u32x4 kk = *(const u32x4*)(kp + t * 1024);
      if (vl) { wa = *(const u32x4*)(vp + t * 1024); wb = *(const u32x4*)(vp + t * 1024 + 512); }
      f32x16 s = mfma(kk, qq, z);
      float tm = s[0];
#pragma unroll
      for (int i = 1; i < 16; i++) tm = fmaxf(tm, s[i]);
      tm = fmaxf(tm, __shfl_xor(tm, 32));
      const float mn = fmaxf(m, tm), al = __builtin_amdgcn_exp2f(m - mn);
      m = mn;
#pragma unroll
      for (int i = 0; i < 16; i++) { oc[i] *= al; s[i] -= mn; }
      oc = pv(wa, exp8(s, 0), oc); oc = pv(wb, exp8(s, 8), oc);
    }
#pragma unroll
    for (int i = 0; i < 9; i++) o[i] = oc[i];
  }
  const float rl = 1.0f / o[8];
  u32x4 r;
#pragma unroll
  for (int j = 0; j < 4; j++) r[j] = pack2(o[2 * j] * rl, o[2 * j + 1] * rl);
  *(u32x4*)(ws + WS_O + (size_t)((rg * 2 + kh) * 32 + head) * 1024 + lane * 16) = r;
  STAMP(1, 3);
}

extern "C" void kernel_launch(void* const* d_in, const int* in_sizes, int n_in, void* d_out, int out_size, void* d_ws, size_t ws_size, hipStream_t stream) {
  const u16* x = (const u16*)d_in[0];
  const u16* wqkv = (const u16*)d_in[1];
  const u16* wo = (const u16*)d_in[2];
  const u16* w1 = (const u16*)d_in[4];
  const u16* b1 = (const u16*)d_in[5];
  const u16* w2 = (const u16*)d_in[6];
  char* ws = (char*)d_ws;
  gemm_wide<0><<<256, 512, 0, stream>>>(x, wqkv, b1, ws);
  attn<<<256, 512, 0, stream>>>(ws);
  gemm_sq<0><<<256, 512, 0, stream>>>(wo, x, ws, (u16*)d_out);
  gemm_wide<1><<<256, 512, 0, stream>>>((const u16*)(ws + WS_H), w1, b1, ws);
  gemm_sq<1><<<256, 512, 0, stream>>>(w2, x, ws, (u16*)d_out);
}
```

```cpp
#include <hip/hip_runtime.h>
#include <hip/hip_bf16.h>
#include <stdint.h>

typedef __attribute__((ext_vector_type(16))) float f32x16;
typedef __attribute__((ext_vector_type(8))) float f32x8;
typedef __attribute__((ext_vector_type(4))) float f32x4;
typedef __attribute__((ext_vector_type(2))) float f32x2;
typedef __attribute__((ext_vector_type(8))) __bf16 bf16x8;
typedef __attribute__((ext_vector_type(16))) __bf16 bf16x16;
typedef __attribute__((ext_vector_type(2))) __bf16 bf16x2;
typedef __attribute__((ext_vector_type(4))) short s16x4;
typedef __attribute__((ext_vector_type(4))) unsigned u32x4;
typedef __attribute__((ext_vector_type(2))) unsigned u32x2;
typedef unsigned short u16;
typedef unsigned long long u64;
#define LAS __attribute__((address_space(3)))
#define GAS __attribute__((address_space(1)))
#define DEV __device__ __forceinline__

constexpr int D_ = 512, F_ = 1376;
constexpr float LOG2E = 1.4426950408889634f;

constexpr unsigned WS_K = 0, WS_V = 2u << 20, WS_O = 4u << 20, WS_H = 6u << 20, WS_ACT = 8u << 20, WS_ST = 14u << 20;
constexpr unsigned WS_EP = 15u << 20, WS_F1 = WS_EP + 4096, WS_F2 = WS_F1 + 8192, WS_F3 = WS_F2 + 16384, WS_F4 = WS_F3 + 8192, WS_END = 16u << 20;

DEV unsigned pack2(float a, float b) { f32x2 p = {a, b}; return __builtin_bit_cast(unsigned, __builtin_convertvector(p, bf16x2)); }
DEV float asf(unsigned u) { return __builtin_bit_cast(float, u); }
DEV unsigned asu(float f) { return __builtin_bit_cast(unsigned, f); }
DEV float bflo(unsigned u) { return __builtin_bit_cast(float, u << 16); }
DEV float bfhi(unsigned u) { return __builtin_bit_cast(float, u & 0xffff0000u); }
DEV f32x16 mfma(u32x4 a, u32x4 b, f32x16 c) { return __builtin_amdgcn_mfma_f32_32x32x16_bf16(__builtin_bit_cast(bf16x8, a), __builtin_bit_cast(bf16x8, b), c, 0, 0, 0); }
DEV f32x4 mfma16(u32x4 a, u32x4 b, f32x4 c) { return __builtin_amdgcn_mfma_f32_16x16x32_bf16(__builtin_bit_cast(bf16x8, a), __builtin_bit_cast(bf16x8, b), c, 0, 0, 0); }
DEV u32x2 tr_read(LAS char* p) { return __builtin_bit_cast(u32x2, __builtin_amdgcn_ds_read_tr16_b64_v4i16((LAS s16x4*)p)); }
DEV u32x4 tr_frag(LAS char* p, int step) { const u32x2 w0 = tr_read(p), w1 = tr_read(p + step); return u32x4{w0[0], w0[1], w1[0], w1[1]}; }
DEV float xhalf_sum(float v) {
  const unsigned u = __builtin_bit_cast(unsigned, v);
  const auto r = __builtin_amdgcn_permlane32_swap(u, u, false, false);
  return asf(r[0]) + asf(r[1]);
}
#define BARRIER() do { __builtin_amdgcn_sched_barrier(0); __builtin_amdgcn_s_waitcnt(0xC07F); asm volatile("s_barrier" ::: "memory"); __builtin_amdgcn_sched_barrier(0); } while (0)
template <int N> DEV void wait_vm() { asm volatile("s_waitcnt vmcnt(%0)" ::"n"(N) : "memory"); }
#define SGB(m, n) __builtin_amdgcn_sched_group_barrier(m, n, 0)
template <int I, int N, class Fn> DEV void static_for(Fn&& f) {
  if constexpr (I < N) { f(std::integral_constant<int, I>{}); static_for<I + 1, N>(f); }
}

#define STAMP(i)
#define STAMPC(i)
#define STAMP_DECL
#define STAMP_FLUSH()

template <int NP, bool SC1> DEV void dma(u64 sbase, unsigned v0, unsigned v1, int ps, int lds, int ls) {
  int n = NP / 2;
#define DMA_BODY(SC)                                                                   \
  asm volatile(                                                                        \
      "s_mov_b64 s[44:45], %[sb]\n\t"                                                  \
      "1:\n\t"                                                                         \
      "s_mov_b32 m0, %[m]\n\ts_add_u32 %[m], %[m], %[ls]\n\t"                          \
      "global_load_lds_dwordx4 %[v0], s[44:45]" SC "\n\t"                              \
      "s_add_u32 s44, s44, %[ps]\n\ts_addc_u32 s45, s45, 0\n\t"                        \
      "s_mov_b32 m0, %[m]\n\ts_add_u32 %[m], %[m], %[ls]\n\t"                          \
      "global_load_lds_dwordx4 %[v1], s[44:45]" SC "\n\t"                              \
      "s_add_u32 s44, s44, %[ps]\n\ts_addc_u32 s45, s45, 0\n\t"                        \
      "s_sub_u32 %[n], %[n], 1\n\ts_cmp_lg_u32 %[n], 0\n\ts_cbranch_scc1 1b\n\t"       \
      : [m] "+s"(lds), [n] "+s"(n)                                                     \
      : [sb] "s"(sbase), [v0] "v"(v0), [v1] "v"(v1), [ps] "s"(ps), [ls] "s"(ls)        \
      : "memory", "scc", "s44", "s45")
  if constexpr (SC1) DMA_BODY(" sc1"); else DMA_BODY("");
}
template <int NI> DEV void dma3(u64 sbase, unsigned v0, unsigned v1, unsigned v2, int ps, int lds) {
  int n = NI;
  asm volatile(
      "s_mov_b64 s[44:45], %[sb]\n\t"
      "1:\n\t"
      "s_mov_b32 m0, %[m]\n\ts_add_u32 %[m], %[m], 0x400\n\t"
      "global_load_lds_dwordx4 %[v0], s[44:45]\n\t"
      "s_mov_b32 m0, %[m]\n\ts_add_u32 %[m], %[m], 0x400\n\t"
      "global_load_lds_dwordx4 %[v1], s[44:45]\n\t"
      "s_mov_b32 m0, %[m]\n\ts_add_u32 %[m], %[m], 0x400\n\t"
      "global_load_lds_dwordx4 %[v2], s[44:45]\n\t"
      "s_add_u32 s44, s44, %[ps]\n\ts_addc_u32 s45, s45, 0\n\t"
      "s_sub_u32 %[n], %[n], 1\n\ts_cmp_lg_u32 %[n], 0\n\ts_cbranch_scc1 1b\n\t"
      : [m] "+s"(lds), [n] "+s"(n)
      : [sb] "s"(sbase), [v0] "v"(v0), [v1] "v"(v1), [v2] "v"(v2), [ps] "s"(ps)
      : "memory", "scc", "s44", "s45");
}

#define RLX_AGENT __ATOMIC_RELAXED, __HIP_MEMORY_SCOPE_AGENT
DEV void publish(char* ws, unsigned off, u64 tag, int lane) {
  asm volatile("s_waitcnt vmcnt(0)" ::: "memory");
  if (lane == 0) __hip_atomic_store((GAS u64*)(ws + off), tag, RLX_AGENT);
}
DEV void wait_flags(char* ws, unsigned off, u64 tag) {
#pragma unroll 1
  for (int spin = 0; spin < (1 << 18); spin++) {
    const u64 v = __hip_atomic_load((GAS u64*)(ws + off), RLX_AGENT);
    if (__all(v == tag)) break;
    __builtin_amdgcn_s_sleep(1);
  }
  asm volatile("" ::: "memory");
}
typedef __amdgpu_buffer_rsrc_t rsrc_t;
DEV void st16(rsrc_t r, unsigned voff, unsigned soff, u32x4 v) { __builtin_amdgcn_raw_buffer_store_b128(v, r, voff, soff, 16); }
DEV u32x4 ld16(rsrc_t r, unsigned voff, unsigned soff) { return __builtin_amdgcn_raw_buffer_load_b128(r, voff, soff, 16); }

DEV f32x16 pv(u32x4 v, f32x8 pr, f32x16 o) {
  return __builtin_amdgcn_smfmac_f32_32x32x32_bf16(__builtin_bit_cast(bf16x8, v), __builtin_bit_cast(bf16x16, pr), o, 0xDDDD, 0, 0);
}
DEV f32x8 exp8(f32x16 s, int o) { f32x8 r; for (int i = 0; i < 8; i++) r[i] = __builtin_amdgcn_exp2f(s[o + i]); return r; }

struct WFrag { u32x4 a[4]; u32x4 w[4][3]; };
struct SFrag { u32x4 a[8]; u32x4 w[8]; };
#define LA1 3
constexpr int L_SCR = 131072, L_Q = 155648;
constexpr int L_B1 = 65536, L_SCR4 = 49152;
constexpr int L_A5 = 114688;
constexpr int NB5 = 6;
DEV constexpr int b5slot(int c) { return (c % NB5) < 3 ? (c % NB5) * 16384 : 65536 + ((c % NB5) - 3) * 16384; }

__global__ __launch_bounds__(512, 2) void fused(const u16* __restrict__ x, const u16* __restrict__ wqkv, const u16* __restrict__ wo, const u16* __restrict__ w1,
                                                const u16* __restrict__ b1, const u16* __restrict__ w2, char* __restrict__ ws, u16* __restrict__ out) {
  __shared__ __attribute__((aligned(16))) char smem_[163840];
  LAS char* smem = (LAS char*)smem_;
  const int tid = threadIdx.x, lane = tid & 63, wave = __builtin_amdgcn_readfirstlane(tid >> 6);
  const int b = blockIdx.x, xcd = b & 7, bi = b >> 3;
  STAMP_DECL; STAMP(0);
  const u64 tag = *(const __attribute__((address_space(4))) u64*)(ws + WS_EP) + 1;
  const rsrc_t rs = __builtin_amdgcn_make_buffer_rsrc(ws, 0, WS_END, 0x00020000);
  const int r32 = lane & 31, h = lane >> 5;
  const int q = (lane & 15) >> 2, p = lane & 3, blk = (lane >> 4) & 1;
  const int lw = wave & 3;
  const unsigned vrow128 = 16 * ((lane & 7) ^ (((lane >> 4) & 1) << 2));

  const int hg = (xcd & 1) * 4 + (bi & 3), rg1 = (xcd >> 1) * 8 + (bi >> 2);
  if (wave >= 4) {
    const int c0 = (bi >> 2) + 2 * (bi & 3);
    u64 sb; unsigned v0, v1; int ps, cstr;
    if (lw == 0) {
      sb = (u64)(x + (size_t)rg1 * 64 * D_);
      v0 = (lane >> 3) * (D_ * 2) + 16 * ((lane & 7) ^ (lane >> 4)); v1 = v0 ^ 64; ps = 8 * D_ * 2; cstr = 128;
    } else {
      sb = (u64)(wqkv + (lw - 1) * D_ + hg * 64);
      v0 = v1 = (lane >> 3) * (3 * D_ * 2) + vrow128; ps = 8 * 3 * D_ * 2; cstr = 64 * 3 * D_ * 2;
    }
#pragma unroll 1
    for (int c = 0; c < 8; c++) {
      if (c >= LA1) { wait_vm<8 * (LA1 - 1)>(); BARRIER(); }
      dma<8, false>(sb + (unsigned)(((c + c0) & 7) * cstr), v0, v1, ps, (c & 3) * 32768 + lw * 8192, 1024);
    }
    asm volatile("" ::"s"(tag));
    static_for<0, LA1>([&](auto i) { wait_vm<8 * (LA1 - 1 - i.value)>(); BARRIER(); });
    STAMP(2);
    BARRIER();
  } else {
    const int rb = wave >> 1, g = wave & 1;
    const int rot = g | (rb << 1);
    int aoff[4], boff[4];
#pragma unroll
    for (int i = 0; i < 4; i++) {
      const int ks = i ^ rot, row = rb * 32 + r32;
      aoff[i] = row * 128 + (((2 * ks + h) ^ ((row >> 1) & 7)) << 4);
      boff[i] = 8192 + ks * 2048 + (8 * h + q) * 128 + (((4 * g + 2 * blk + (p >> 1)) ^ ((q >> 1) << 2)) << 4) + 8 * (p & 1);
    }
    f32x16 acc[3];
    f32x4 cs4[3];
#pragma unroll
    for (int u = 0; u < 3; u++) { for (int i = 0; i < 16; i++) acc[u][i] = 0.f; cs4[u] = f32x4{0.f, 0.f, 0.f, 0.f}; }
    float sx = 0.f, sxx = 0.f;
    const unsigned selv = ((lane & 15) == blk) ? 0x3f803f80u : 0u;
    const u32x4 sel = {selv, selv, selv, selv};
    auto rd = [&](WFrag& f, int slot) {
      LAS char* sl = smem + slot * 32768;
#pragma unroll
      for (int i = 0; i < 4; i++) {
        f.a[i] = *(LAS u32x4*)(sl + aoff[i]);
#pragma unroll
        for (int u = 0; u < 3; u++) f.w[i][u] = tr_frag(sl + boff[i] + u * 8192, 512);
      }
    };
    auto mm = [&](const WFrag& f) {
#pragma unroll
      for (int i = 0; i < 4; i++) {
#pragma unroll
        for (int u = 0; u < 3; u++) {
          if (u == 2) acc[u] = mfma(f.a[i], f.w[i][u], acc[u]); else acc[u] = mfma(f.w[i][u], f.a[i], acc[u]);
          if ((i & 2) == 0) cs4[u] = mfma16(sel, f.w[i][u], cs4[u]);
        }
        if ((i & 1) == 0) {
#pragma unroll
          for (int j = 0; j < 4; j++) { const float lo = bflo(f.a[i][j]), hi = bfhi(f.a[i][j]); sx += lo; sxx = fmaf(lo, lo, sxx); sx += hi; sxx = fmaf(hi, hi, sxx); }
        }
      }
    };
    auto sched = [&]() {
#pragma unroll
      for (int i = 0; i < 18; i++) { SGB(0x8, 1); SGB(0x100, 2); SGB(0x2, 3); }
    };
    WFrag fa, fb;
    asm volatile("" ::"s"(tag));
    BARRIER();
    STAMP(1);
    rd(fa, 0);
#pragma unroll 1
    for (int c = 1; c < 7; c += 2) {
      BARRIER();
      rd(fb, c & 3); mm(fa); sched();
      BARRIER();
      rd(fa, (c + 1) & 3); mm(fb); sched();
    }
    BARRIER();
    STAMP(2);
    rd(fb, 3); mm(fa); sched();
    __builtin_amdgcn_sched_barrier(0);
    mm(fb);
    STAMP(3);
    LAS float* scr0 = (LAS float*)(smem + L_SCR);
    LAS float* scr = scr0 + wave * 512;
    sx = xhalf_sum(sx); sxx = xhalf_sum(sxx);
    if (h == 0) { scr[384 + r32] = sx; scr[416 + r32] = sxx; }
    if (lane < 16) {
#pragma unroll
      for (int u = 0; u < 3; u++) { scr[256 + u * 32 + lane] = cs4[u][0]; scr[256 + u * 32 + 16 + lane] = cs4[u][1]; }
    }
    BARRIER();
    sx += scr0[(wave ^ 1) * 512 + 384 + r32]; sxx += scr0[(wave ^ 1) * 512 + 416 + r32];
    const float mean = sx * (1.f / D_);
    const float rstd = __builtin_amdgcn_rsqf(fmaxf(sxx * (1.f / D_) - mean * mean, 0.f) + 1e-5f);
    if (h == 0) { scr[448 + r32] = mean; scr[480 + r32] = rstd; }
    const int tile = rg1 * 2 + rb;
#pragma unroll
    for (int uu = 0; uu < 2; uu++) {
      const int u = 1 - uu;
      const float sc = u == 0 ? rstd * (0.25f * LOG2E) : rstd;
      const float nm = -mean;
      unsigned o[8];
#pragma unroll
      for (int i = 0; i < 4; i++) {
        const f32x4 c4 = *(LAS f32x4*)(scr + 256 + u * 32 + 8 * i + 4 * h) + *(LAS f32x4*)(scr0 + (wave ^ 2) * 512 + 256 + u * 32 + 8 * i + 4 * h);
        o[2 * i] = pack2(fmaf(nm, c4[0], acc[u][4 * i]) * sc, fmaf(nm, c4[1], acc[u][4 * i + 1]) * sc);
        o[2 * i + 1] = pack2(fmaf(nm, c4[2], acc[u][4 * i + 2]) * sc, fmaf(nm, c4[3], acc[u][4 * i + 3]) * sc);
      }
      if (u == 1) {
        const unsigned pos = h * 32 + ((r32 & 19) | ((r32 & 4) << 1) | ((r32 & 8) >> 1));
        const unsigned so = WS_K + ((hg * 4 + 2 * g) * 64 + tile) * 1024;
        st16(rs, pos * 16, so, u32x4{o[0], o[1], o[2], o[3]});
        st16(rs, pos * 16, so + 65536, u32x4{o[4], o[5], o[6], o[7]});
        unsigned ov[8];
        const float ncw = -(scr[256 + 64 + r32] + scr0[(wave ^ 2) * 512 + 256 + 64 + r32]);
#pragma unroll
        for (int i = 0; i < 4; i++) {
          const f32x4 m4 = *(LAS f32x4*)(scr + 448 + 8 * i + 4 * h), r4 = *(LAS f32x4*)(scr + 480 + 8 * i + 4 * h);
          ov[2 * i] = pack2(fmaf(ncw, m4[0], acc[2][4 * i]) * r4[0], fmaf(ncw, m4[1], acc[2][4 * i + 1]) * r4[1]);
          ov[2 * i + 1] = pack2(fmaf(ncw, m4[2], acc[2][4 * i + 2]) * r4[2], fmaf(ncw, m4[3], acc[2][4 * i + 3]) * r4[3]);
        }
        const unsigned vo = (r32 >> 4) * 65536 + h * 256 + (r32 & 15) * 16, sv = WS_V + ((hg * 4 + 2 * g) * 64 + tile) * 1024;
        st16(rs, vo, sv, u32x4{ov[0], ov[1], ov[2], ov[3]});
        st16(rs, vo, sv + 512, u32x4{ov[4], ov[5], ov[6], ov[7]});
      } else {
        LAS char* d = smem + L_Q + (4 * g + rb) * 1024 + lane * 16;
        *(LAS u32x4*)d = u32x4{o[0], o[1], o[2], o[3]};
        *(LAS u32x4*)(d + 2048) = u32x4{o[4], o[5], o[6], o[7]};
      }
    }
    STAMP(4);
    publish(ws, WS_F1 + (((hg * 2 + g) * 32 + rg1) * 2 + rb) * 8, tag, lane);
    STAMP(5);
  }
  BARRIER();

  {
    const int head = hg * 4 + (wave >> 1), kh = wave & 1;
    const u32x4 q0 = *(LAS u32x4*)(smem + L_Q + (wave >> 1) * 2048 + lane * 16), q1 = *(LAS u32x4*)(smem + L_Q + (wave >> 1) * 2048 + 1024 + lane * 16);
    const char* tb = wave == 0 ? (const char*)wo + bi * 16384
                   : wave < 4 ? (const char*)w1 + bi * 44032 + (wave == 3 ? 44032 - 16384 : (wave - 1) * 16384)
                   : wave < 7 ? (const char*)w2 + bi * 44032 + (wave == 6 ? 44032 - 16384 : (wave - 4) * 16384)
                              : (const char*)x + (size_t)(xcd * 256 + (bi >> 1) * 16) * 1024;
    wait_flags(ws, WS_F1 + (((hg * 2 + (wave >> 2)) * 32 + 16 * kh) * 2 + r32) * 8, tag);
    STAMP(6); STAMPC(26);
    float oa[9], ob[9];
    {
      const unsigned koff = lane * 16, voff = h * 256 + (lane & 15) * 16, toff = lane * 32;
      const char* kb = ws + WS_K + (size_t)head * 65536 + kh * 32768;
      const char* vb = ws + WS_V + (size_t)head * 65536 + kh * 32768;
      const unsigned vinit = (r32 == 16 || r32 == 20) ? 0x3f803f80u : 0u;
      int cnt = 8;
#define E1(r) "v_exp_f32 v" #r ", v" #r "\n\t"
#define SM(o, a, p) "v_smfmac_f32_32x32x32_bf16 v[" o "], v[" a "], v[" p "], v144\n\t"
#define QK(s, k, q) "v_mfma_f32_32x32x16_bf16 v[" s "], v[" k "], " q ", 0\n\t"
#define LDK(k, off) "global_load_dwordx4 v[" k "], %[koff], s[40:41] offset:" #off " sc1\n\t"
#define LDV(a, b, offa, offb) "s_mov_b64 exec, %[vm]\n\tglobal_load_dwordx4 v[" a "], %[voff], s[42:43] offset:" #offa " sc1\n\tglobal_load_dwordx4 v[" b "], %[voff], s[42:43] offset:" #offb " sc1\n\ts_mov_b64 exec, -1\n\t"
#define ITER(kc, kn, vap, vbp, vac, vbc, ok, oa_, ob_, X)                                                 \
      E1(64) E1(65) SM("48:63", vbp, "88:95") E1(66) E1(67) E1(68) QK("80:95", kc, "%[q1]")               \
      LDK(kc, ok) LDV(vap, vbp, oa_, ob_) X E1(69) E1(70) E1(71) E1(72) E1(73) "s_waitcnt vmcnt(10)\n\t"  \
      SM("32:47", vac, "64:71") E1(74) E1(75) E1(76) E1(77) E1(78) E1(79)                                 \
      E1(80) E1(81) SM("32:47", vbc, "72:79") E1(82) E1(83) E1(84) QK("64:79", kn, "%[q0]")               \
      E1(85) E1(86) E1(87) E1(88) E1(89) SM("48:63", vac, "80:87") E1(90) E1(91) E1(92) E1(93) E1(94) E1(95)
#define Z(r) "v_mov_b32 v" #r ", 0\n\t"
#define VI(r) "v_mov_b32 v" #r ", %[vinit]\n\t"
#define OUT(n, r) "v_mov_b32 %" #n ", v" #r "\n\t"
      asm volatile(
        "s_mov_b64 s[40:41], %[kin]\n\ts_mov_b64 s[42:43], %[vin]\n\ts_mov_b64 s[46:47], %[tin]\n\t"
        LDK("96:99", 0)
        VI(112) VI(113) VI(114) VI(115) VI(128) VI(129) VI(130) VI(131)
        LDK("100:103", 1024) LDV("112:115", "128:131", 0, 512)
        VI(116) VI(117) VI(118) VI(119) VI(132) VI(133) VI(134) VI(135)
        LDK("104:107", 2048) LDV("116:119", "132:135", 1024, 1536)
        VI(120) VI(121) VI(122) VI(123) VI(136) VI(137) VI(138) VI(139)
        LDK("108:111", 3072) LDV("120:123", "136:139", 2048, 2560)
        VI(124) VI(125) VI(126) VI(127) VI(140) VI(141) VI(142) VI(143)
        Z(32) Z(33) Z(34) Z(35) Z(36) Z(37) Z(38) Z(39) Z(40) Z(41) Z(42) Z(43) Z(44) Z(45) Z(46) Z(47)
        Z(48) Z(49) Z(50) Z(51) Z(52) Z(53) Z(54) Z(55) Z(56) Z(57) Z(58) Z(59) Z(60) Z(61) Z(62) Z(63)
        Z(88) Z(89) Z(90) Z(91) Z(92) Z(93) Z(94) Z(95)
        "v_mov_b32 v144, 0xdddd\n\t"
        "s_add_u32 s40, s40, 4096\n\ts_addc_u32 s41, s41, 0\n\t"
        "s_add_u32 s42, s42, 3072\n\ts_addc_u32 s43, s43, 0\n\t"
        "s_waitcnt vmcnt(9)\n\t"
        QK("64:79", "96:99", "%[q0]")
        "s_nop 15\n\t"
        "1:\n\t"
        ITER("96:99", "100:103", "124:127", "140:143", "112:115", "128:131", 0, 0, 512, "global_load_dword v145, %[toff], s[46:47]\n\t")
        ITER("100:103", "104:107", "112:115", "128:131", "116:119", "132:135", 1024, 1024, 1536, "")
        ITER("104:107", "108:111", "116:119", "132:135", "120:123", "136:139", 2048, 2048, 2560, "")
        ITER("108:111", "96:99", "120:123", "136:139", "124:127", "140:143", 3072, 3072, 3584, "")
        "s_add_u32 s40, s40, 4096\n\ts_addc_u32 s41, s41, 0\n\t"
        "s_add_u32 s42, s42, 4096\n\ts_addc_u32 s43, s43, 0\n\t"
        "s_add_u32 s46, s46, 2048\n\ts_addc_u32 s47, s47, 0\n\t"
        "s_sub_u32 %[cnt], %[cnt], 1\n\ts_cmp_lg_u32 %[cnt], 0\n\ts_cbranch_scc1 1b\n\t"
        SM("48:63", "140:143", "88:95")
        "s_waitcnt vmcnt(0)\n\ts_nop 15\n\t"
        OUT(0, 32) OUT(1, 33) OUT(2, 34) OUT(3, 35) OUT(4, 36) OUT(5, 37) OUT(6, 38) OUT(7, 39) OUT(8, 40)
        OUT(9, 48) OUT(10, 49) OUT(11, 50) OUT(12, 51) OUT(13, 52) OUT(14, 53) OUT(15, 54) OUT(16, 55) OUT(17, 56)
        : "=&v"(oa[0]), "=&v"(oa[1]), "=&v"(oa[2]), "=&v"(oa[3]), "=&v"(oa[4]), "=&v"(oa[5]), "=&v"(oa[6]), "=&v"(oa[7]), "=&v"(oa[8]),
          "=&v"(ob[0]), "=&v"(ob[1]), "=&v"(ob[2]), "=&v"(ob[3]), "=&v"(ob[4]), "=&v"(ob[5]), "=&v"(ob[6]), "=&v"(ob[7]), "=&v"(ob[8]),
          [cnt] "+s"(cnt)
        : [q0] "v"(q0), [q1] "v"(q1), [koff] "v"(koff), [voff] "v"(voff), [toff] "v"(toff), [vinit] "v"(vinit), [vm] "s"(0x0000ffff0000ffffull),
          [kin] "s"(kb), [vin] "s"(vb), [tin] "s"(tb)
        : "memory", "scc", "s40", "s41", "s42", "s43", "s46", "s47",
          "v32","v33","v34","v35","v36","v37","v38","v39","v40","v41","v42","v43","v44","v45","v46","v47","v48","v49","v50","v51","v52","v53","v54","v55","v56","v57","v58","v59","v60","v61","v62","v63",
          "v64","v65","v66","v67","v68","v69","v70","v71","v72","v73","v74","v75","v76","v77","v78","v79","v80","v81","v82","v83","v84","v85","v86","v87","v88","v89","v90","v91","v92","v93","v94","v95",
          "v96","v97","v98","v99","v100","v101","v102","v103","v104","v105","v106","v107","v108","v109","v110","v111","v112","v113","v114","v115","v116","v117","v118","v119","v120","v121","v122","v123","v124","v125","v126","v127",
          "v128","v129","v130","v131","v132","v133","v134","v135","v136","v137","v138","v139","v140","v141","v142","v143","v144","v145");
    }
    STAMP(7); STAMPC(27);
    LAS float* red = (LAS float*)(smem + L_SCR);
#pragma unroll
    for (int i = 0; i < 9; i++) red[(wave * 9 + i) * 64 + lane] = kh ? oa[i] : ob[i];
    BARRIER();
    float o[9];
#pragma unroll
    for (int i = 0; i < 9; i++) o[i] = (kh ? ob[i] : oa[i]) + red[((wave ^ 1) * 9 + i) * 64 + lane];
    const bool ok = o[8] >= 0x1p-40f && o[8] <= 0x1p40f;
    if (!__all(ok)) {
      const u32x4 qq = kh ? q1 : q0;
      f32x16 z, oc;
#pragma unroll
      for (int i = 0; i < 16; i++) { z[i] = 0.f; oc[i] = 0.f; }
      float m = -__builtin_inff();
      const bool vl = (lane & 16) == 0;
      const u32x4 ones = {0x3f803f80u, 0x3f803f80u, 0x3f803f80u, 0x3f803f80u};
#pragma unroll 1
      for (int t = 0; t < 64; t++) {
        const u32x4 kk = ld16(rs, lane * 16, WS_K + head * 65536 + t * 1024);
        u32x4 wa = ld16(rs, h * 256 + (lane & 15) * 16, WS_V + head * 65536 + t * 1024), wb = ld16(rs, h * 256 + (lane & 15) * 16, WS_V + head * 65536 + t * 1024 + 512);
        wa = vl ? wa : ones; wb = vl ? wb : ones;
        f32x16 s = mfma(kk, qq, z);
        float tm = s[0];
#pragma unroll
        for (int i = 1; i < 16; i++) tm = fmaxf(tm, s[i]);
        tm = fmaxf(tm, __shfl_xor(tm, 32));
        const float mn = fmaxf(m, tm), al = __builtin_amdgcn_exp2f(m - mn);
        m = mn;
#pragma unroll
        for (int i = 0; i < 16; i++) { oc[i] *= al; s[i] -= mn; }
        oc = pv(wa, exp8(s, 0), oc); oc = pv(wb, exp8(s, 8), oc);
      }
#pragma unroll
      for (int i = 0; i < 9; i++) o[i] = oc[i];
    }
    const float rl = 1.0f / o[8];
    u32x4 r;
#pragma unroll
    for (int j = 0; j < 4; j++) r[j] = pack2(o[2 * j] * rl, o[2 * j + 1] * rl);
    st16(rs, lane * 16, WS_O + ((rg1 * 2 + kh) * 32 + head) * 1024, r);
    STAMP(8);
    publish(ws, WS_F2 + (rg1 * 64 + hg * 8 + wave) * 8, tag, lane);
    STAMP(9);
  }

  const int rg = xcd * 4 + (bi >> 3), cs = bi & 7;
  const int rp = xcd * 2 + (bi >> 4), sl = bi & 15;
  const int unit0 = sl < 5 ? 2 * sl : 3 * sl - 5, nunit = sl < 5 ? 2 : 3;
  const unsigned vw = (lane >> 3) * (D_ * 2) + vrow128;
  if (wave >= 4) {
    wait_flags(ws, WS_F2 + (rg * 64 + lane) * 8, tag);
    STAMP(10);
    {
      const u64 sa = (u64)(ws + WS_O + ((rg * 2 + (lw >> 1)) * 32 + (lw & 1) * 4) * 1024);
#pragma unroll 1
      for (int c = 0; c < 4; c++) {
        const int cc = (c + cs) & 3;
        if (c >= 2) { wait_vm<4>(); BARRIER(); }
        dma<4, true>(sa + (unsigned)(cc * 8192), lane * 16, lane * 16, 1024, 65536 + cc * 16384 + lw * 4096, 1024);
      }
      wait_vm<4>(); BARRIER();
      wait_vm<0>(); BARRIER();
    }
    STAMP(11);
    BARRIER();
    {
      unsigned v3[3];
#pragma unroll
      for (int j = 0; j < 3; j++) { const int idx = j * 64 + lane, row = idx / 12; v3[j] = row * (F_ * 2) + (idx - row * 12) * 16; }
      dma3<8>((u64)(w1 + (size_t)lw * 128 * F_ + unit0 * 32), v3[0], v3[1], v3[2], 16 * F_ * 2, L_B1 + lw * 24576);
    }
    const u64 sw2 = (u64)(w2 + (size_t)lw * 32 * D_ + cs * 64);
    auto issue_b = [&](int c) { dma<4, false>(sw2 + (unsigned)((c == 10 && lw == 3 ? c * 128 - 32 : c * 128) * D_ * 2), vw, vw, 8 * D_ * 2, b5slot(c) + lw * 4096, 1024); };
    issue_b(0); issue_b(1); issue_b(2);
    wait_vm<12>(); BARRIER();
    STAMP(14);
    BARRIER();
    BARRIER();
    issue_b(3); issue_b(4); issue_b(5);
    wait_flags(ws, WS_F4 + (rg * 32 + r32) * 8, tag);
    STAMP(19);
    const u64 sa = (u64)(ws + WS_ACT + ((rg * 2 + (lw >> 1)) * 86 + (lw & 1) * 4) * 1024);
    auto issue_a = [&](int c) { dma<4, true>(sa + (unsigned)(c * 8192), lane * 16, lane * 16, 1024, L_A5 + (c % 3) * 16384 + lw * 4096, 1024); };
    issue_a(0); issue_a(1);
    static_for<0, 11>([&](auto cc) {
      constexpr int c = cc.value;
      constexpr int younger = (c + 1 < 11 ? 4 : 0) + (c >= 2 && c - 2 + NB5 < 11 ? 4 : 0) + (c >= 3 && c - 3 + NB5 < 11 ? 4 : 0);
      wait_vm<younger>(); BARRIER();
      if constexpr (c + 2 < 11) issue_a(c + 2);
      if constexpr (c >= 1 && c - 1 + NB5 < 11) issue_b(c - 1 + NB5);
    });
    STAMP(21);
    STAMP_FLUSH();
    return;
  }
  const int rb = wave >> 1, cb = wave & 1;
  const int tile = rg * 2 + rb;
  const size_t rowoff = (size_t)(tile * 32 + r32) * D_ + cs * 64 + cb * 32 + 8 * h;
  const int aoff = rb * 8192 + lane * 16;
  const int boff = (4 * h + q) * 128 + (((4 * cb + 2 * blk + (p & 1)) ^ ((q >> 1) << 2)) << 4) + 8 * (p >> 1);
  u32x4 res[2];
  f32x16 acc;
  auto rd = [&](SFrag& f, int la, int lb, auto n) {
#pragma unroll
    for (int ks = 0; ks < n.value; ks++) { f.a[ks] = *(LAS u32x4*)(smem + la + aoff + ks * 1024); f.w[ks] = tr_frag(smem + lb + boff + ks * 2048, 1024); }
  };
  auto mm = [&](const SFrag& f, auto n) {
#pragma unroll
    for (int ks = 0; ks < n.value; ks++) acc = mfma(f.w[ks], f.a[ks], acc);
  };
  auto sched = [&]() {
#pragma unroll
    for (int i = 0; i < 8; i++) { SGB(0x8, 1); SGB(0x100, 3); }
  };
  constexpr std::integral_constant<int, 8> full{};
  constexpr std::integral_constant<int, 6> last{};
  float bv[3];
  {
    dma<16, false>((u64)(wo + (size_t)lw * 128 * D_ + cs * 64), vw, vw, 8 * D_ * 2, lw * 16384, 1024);
    res[0] = *(const u32x4*)(x + rowoff); res[1] = *(const u32x4*)(x + rowoff + 16);
#pragma unroll
    for (int u = 0; u < 3; u++) bv[u] = bflo(b1[(unit0 + u) * 32 + r32]);
#pragma unroll
    for (int i = 0; i < 16; i++) acc[i] = 0.f;
    wait_vm<0>();
    STAMP(10);
    SFrag fa, fb;
    const int c0 = cs & 3, c1 = (cs + 1) & 3, c2 = (cs + 2) & 3, c3 = (cs + 3) & 3;
    BARRIER();
    rd(fa, 65536 + c0 * 16384, c0 * 16384, full);
    BARRIER();
    rd(fb, 65536 + c1 * 16384, c1 * 16384, full); mm(fa, full); sched();
    BARRIER();
    rd(fa, 65536 + c2 * 16384, c2 * 16384, full); mm(fb, full); sched();
    BARRIER();
    STAMP(11);
    rd(fb, 65536 + c3 * 16384, c3 * 16384, full); mm(fa, full); sched();
    __builtin_amdgcn_sched_barrier(0);
    mm(fb, full);
    float sx = 0.f, sxx = 0.f;
#pragma unroll
    for (int s = 0; s < 2; s++) {
#pragma unroll
      for (int j = 0; j < 4; j++) res[s][j] = pack2(acc[8 * s + 2 * j] + bflo(res[s][j]), acc[8 * s + 2 * j + 1] + bfhi(res[s][j]));
      st16(rs, lane * 16, WS_H + (tile * 32 + cs * 4 + cb * 2 + s) * 1024, res[s]);
    }
#pragma unroll
    for (int s = 0; s < 2; s++)
#pragma unroll
      for (int j = 0; j < 4; j++) { const float lo = bflo(res[s][j]), hi = bfhi(res[s][j]); sx += lo + hi; sxx = fmaf(lo, lo, fmaf(hi, hi, sxx)); }
    sx = xhalf_sum(sx); sxx = xhalf_sum(sxx);
    if (h == 0) __builtin_amdgcn_raw_buffer_store_b64(u32x2{__builtin_bit_cast(unsigned, sx), __builtin_bit_cast(unsigned, sxx)}, rs, r32 * 8, WS_ST + (tile * 16 + cs * 2 + cb) * 256, 16);
    STAMP(12);
    BARRIER();
    publish(ws, WS_F3 + (tile * 16 + cs * 2 + cb) * 8, tag, lane);
    STAMP(13);
  }
  {
    const int t4 = rp * 4 + wave;
    LAS char* bb = smem + L_B1 + (8 * h + q) * 192 + blk * 32 + p * 8;
    LAS float* scr0 = (LAS float*)(smem + L_SCR4);
    BARRIER();
    STAMP(14);
    {
      const unsigned selv = ((lane & 15) == blk) ? 0x3f803f80u : 0u;
      const u32x4 sel = {selv, selv, selv, selv};
      f32x4 cs4[3];
#pragma unroll
      for (int u = 0; u < 3; u++) cs4[u] = f32x4{0.f, 0.f, 0.f, 0.f};
      LAS char* bw = bb + wave * 8 * 3072;
#pragma unroll
      for (int ks = 0; ks < 8; ks++)
#pragma unroll
        for (int u = 0; u < 3; u++) cs4[u] = mfma16(sel, tr_frag(bw + ks * 3072 + u * 64, 768), cs4[u]);
      if (lane < 16) {
#pragma unroll
        for (int u = 0; u < 3; u++) { scr0[wave * 96 + u * 32 + lane] = cs4[u][0]; scr0[wave * 96 + u * 32 + 16 + lane] = cs4[u][1]; }
      }
    }
    BARRIER();
    LAS float* scr = scr0 + 512 + wave * 256;
    if (h == 0) {
#pragma unroll
      for (int u = 0; u < 3; u++) {
        scr[u * 32 + r32] = (scr0[u * 32 + r32] + scr0[96 + u * 32 + r32]) + (scr0[192 + u * 32 + r32] + scr0[288 + u * 32 + r32]);
        scr[96 + u * 32 + r32] = bv[u];
      }
    }
    STAMP(15);
    wait_flags(ws, WS_F3 + (t4 * 16 + (lane & 15)) * 8, tag);
    STAMP(16);
    u32x4 a[32];
#pragma unroll
    for (int ks = 0; ks < 32; ks++) a[ks] = ld16(rs, lane * 16, WS_H + (t4 * 32 + ks) * 1024);
    u32x2 st[8];
#pragma unroll
    for (int i = 0; i < 8; i++) st[i] = __builtin_amdgcn_raw_buffer_load_b64(rs, h * 2048 + r32 * 8, WS_ST + (t4 * 16 + i) * 256, 16);
    f32x16 ac[3];
#pragma unroll
    for (int u = 0; u < 3; u++) for (int i = 0; i < 16; i++) ac[u][i] = 0.f;
    __builtin_amdgcn_sched_barrier(0);
    u32x4 wf[2][3];
#pragma unroll
    for (int u = 0; u < 3; u++) wf[0][u] = tr_frag(bb + u * 64, 768);
#pragma unroll
    for (int ks = 0; ks < 32; ks++) {
      if (ks + 1 < 32) {
#pragma unroll
        for (int u = 0; u < 3; u++) wf[(ks + 1) & 1][u] = tr_frag(bb + (ks + 1) * 3072 + u * 64, 768);
      }
#pragma unroll
      for (int u = 0; u < 3; u++) ac[u] = mfma(wf[ks & 1][u], a[ks], ac[u]);
      __builtin_amdgcn_sched_barrier(0);
    }
    BARRIER();
    STAMP(17);
    float sx = 0.f, sxx = 0.f;
#pragma unroll
    for (int i = 0; i < 8; i++) { sx += asf(st[i][0]); sxx += asf(st[i][1]); }
    sx = xhalf_sum(sx); sxx = xhalf_sum(sxx);
    const float mean = sx * (1.f / D_);
    const float rstd = __builtin_amdgcn_rsqf(fmaxf(sxx * (1.f / D_) - mean * mean, 0.f) + 1e-5f);
    const float nmr = -mean * rstd;
#pragma unroll
    for (int u = 0; u < 3; u++) {
      if (u < nunit) {
        unsigned o[8];
#pragma unroll
        for (int i = 0; i < 4; i++) {
          const f32x4 c4 = *(LAS f32x4*)(scr + u * 32 + 8 * i + 4 * h), b4 = *(LAS f32x4*)(scr + 96 + u * 32 + 8 * i + 4 * h);
          float y[4];
#pragma unroll
          for (int j = 0; j < 4; j++) {
            const float v = fmaf(ac[u][4 * i + j], rstd, fmaf(nmr, c4[j], b4[j]));
            const float t = __builtin_amdgcn_exp2f(fminf(v * LOG2E, 40.f));
            const float n = t * (t + 2.f);
            y[j] = v * n * __builtin_amdgcn_rcpf(n + 2.f);
          }
          o[2 * i] = pack2(y[0], y[1]); o[2 * i + 1] = pack2(y[2], y[3]);
        }
        const unsigned so = WS_ACT + (t4 * 86 + (unit0 + u) * 2) * 1024;
        st16(rs, lane * 16, so, u32x4{o[0], o[1], o[2], o[3]});
        st16(rs, lane * 16, so + 1024, u32x4{o[4], o[5], o[6], o[7]});
      }
    }
    STAMP(18);
    publish(ws, WS_F4 + ((t4 >> 1) * 32 + (t4 & 1) * 16 + sl) * 8, tag, lane);
    STAMP(19);
  }
  {
#pragma unroll
    for (int i = 0; i < 16; i++) acc[i] = 0.f;
    SFrag fa, fb;
    BARRIER();
    STAMP(20);
    rd(fa, L_A5, b5slot(0), full);
    static_for<0, 5>([&](auto cc) {
      constexpr int c = 2 * cc.value + 1;
      BARRIER();
      rd(fb, L_A5 + (c % 3) * 16384, b5slot(c), full); mm(fa, full); sched();
      BARRIER();
      if constexpr (c + 1 < 10) rd(fa, L_A5 + ((c + 1) % 3) * 16384, b5slot(c + 1), full); else rd(fa, L_A5 + ((c + 1) % 3) * 16384, b5slot(c + 1), last);
      mm(fb, full); sched();
    });
    STAMP(21);
    __builtin_amdgcn_sched_barrier(0);
    mm(fa, last);
#pragma unroll
    for (int s = 0; s < 2; s++) {
      u32x4 o;
#pragma unroll
      for (int j = 0; j < 4; j++) o[j] = pack2(acc[8 * s + 2 * j] + bflo(res[s][j]), acc[8 * s + 2 * j + 1] + bfhi(res[s][j]));
      *(u32x4*)(out + rowoff + 16 * s) = o;
    }
    if (b == 0 && tid == 0) *(u64*)(ws + WS_EP) = tag;
    STAMP(22);
    STAMP_FLUSH();
  }
}

extern "C" void kernel_launch(void* const* d_in, const int* in_sizes, int n_in, void* d_out, int out_size, void* d_ws, size_t ws_size, hipStream_t stream) {
  fused<<<256, 512, 0, stream>>>((const u16*)d_in[0], (const u16*)d_in[1], (const u16*)d_in[2], (const u16*)d_in[4], (const u16*)d_in[5], (const u16*)d_in[6], (char*)d_ws, (u16*)d_out);
}
```

```cpp
#include <hip/hip_runtime.h>
#include <hip/hip_bf16.h>
#include <stdint.h>

typedef __attribute__((ext_vector_type(16))) float f32x16;
typedef __attribute__((ext_vector_type(8))) float f32x8;
typedef __attribute__((ext_vector_type(4))) float f32x4;
typedef __attribute__((ext_vector_type(2))) float f32x2;
typedef __attribute__((ext_vector_type(8))) __bf16 bf16x8;
typedef __attribute__((ext_vector_type(16))) __bf16 bf16x16;
typedef __attribute__((ext_vector_type(2))) __bf16 bf16x2;
typedef __attribute__((ext_vector_type(4))) short s16x4;
typedef __attribute__((ext_vector_type(4))) unsigned u32x4;
typedef __attribute__((ext_vector_type(2))) unsigned u32x2;
typedef __attribute__((ext_vector_type(16))) unsigned u32x16;
typedef unsigned short u16;
typedef unsigned long long u64;
#define LAS __attribute__((address_space(3)))
#define GAS __attribute__((address_space(1)))
#define DEV __device__ __forceinline__

constexpr int D_ = 512, F_ = 1376;
constexpr float LOG2E = 1.4426950408889634f;

constexpr unsigned WS_K = 0, WS_V = 2u << 20, WS_O = 4u << 20, WS_H = 6u << 20, WS_ACT = 8u << 20, WS_ST = 14u << 20;
constexpr unsigned WS_EP = 15u << 20, WS_F1 = WS_EP + 4096, WS_F2 = WS_F1 + 8192, WS_F3 = WS_F2 + 16384, WS_F4 = WS_F3 + 8192, WS_END = 16u << 20;

DEV unsigned pack2(float a, float b) { f32x2 p = {a, b}; return __builtin_bit_cast(unsigned, __builtin_convertvector(p, bf16x2)); }
DEV float asf(unsigned u) { return __builtin_bit_cast(float, u); }
DEV unsigned asu(float f) { return __builtin_bit_cast(unsigned, f); }
DEV float bflo(unsigned u) { return __builtin_bit_cast(float, u << 16); }
DEV float bfhi(unsigned u) { return __builtin_bit_cast(float, u & 0xffff0000u); }
DEV f32x16 mfma(u32x4 a, u32x4 b, f32x16 c) { return __builtin_amdgcn_mfma_f32_32x32x16_bf16(__builtin_bit_cast(bf16x8, a), __builtin_bit_cast(bf16x8, b), c, 0, 0, 0); }
DEV f32x4 mfma16(u32x4 a, u32x4 b, f32x4 c) { return __builtin_amdgcn_mfma_f32_16x16x32_bf16(__builtin_bit_cast(bf16x8, a), __builtin_bit_cast(bf16x8, b), c, 0, 0, 0); }
DEV u32x2 tr_read(LAS char* p) { return __builtin_bit_cast(u32x2, __builtin_amdgcn_ds_read_tr16_b64_v4i16((LAS s16x4*)p)); }
DEV u32x4 tr_frag(LAS char* p, int step) { const u32x2 w0 = tr_read(p), w1 = tr_read(p + step); return u32x4{w0[0], w0[1], w1[0], w1[1]}; }
DEV float xhalf_sum(float v) {
  const unsigned u = __builtin_bit_cast(unsigned, v);
  const auto r = __builtin_amdgcn_permlane32_swap(u, u, false, false);
  return asf(r[0]) + asf(r[1]);
}
#define BARRIER() do { __builtin_amdgcn_sched_barrier(0); __builtin_amdgcn_s_waitcnt(0xC07F); asm volatile("s_barrier" ::: "memory"); __builtin_amdgcn_sched_barrier(0); } while (0)
template <int N> DEV void wait_vm() { asm volatile("s_waitcnt vmcnt(%0)" ::"n"(N) : "memory"); }
#define SGB(m, n) __builtin_amdgcn_sched_group_barrier(m, n, 0)
template <int I, int N, class Fn> DEV void static_for(Fn&& f) {
  if constexpr (I < N) { f(std::integral_constant<int, I>{}); static_for<I + 1, N>(f); }
}

#define STAMP(i)
#define STAMPC(i)
#define STAMP_DECL
#define STAMP_FLUSH()

DEV void dma_p1(u64 sw, unsigned vw0, unsigned vw1, int lds) {
#define P1(dm, v, off) "s_add_u32 m0, %[m], " #dm "\n\ts_nop 0\n\tglobal_load_lds_dwordx4 %[" #v "], %[sw] offset:" #off "\n\t"
  asm volatile(P1(0, vw0, 0) P1(0x400, vw1, 0) P1(0xc00, vw0, 1024) P1(0x1000, vw1, 1024) P1(0x1800, vw0, 2048) P1(0x1c00, vw1, 2048)
               :: [m] "s"(lds), [sw] "s"(sw), [vw0] "v"(vw0), [vw1] "v"(vw1) : "memory", "scc");
}
DEV void dma_k2(u64 sbase, unsigned v0, unsigned v1, int lds) {
  asm volatile("s_mov_b32 m0, %[m]\n\ts_add_u32 %[m], %[m], 0x400\n\tglobal_load_lds_dwordx4 %[v0], %[sb]\n\t"
               "s_mov_b32 m0, %[m]\n\ts_nop 0\n\tglobal_load_lds_dwordx4 %[v1], %[sb]"
               : [m] "+s"(lds) : [sb] "s"(sbase), [v0] "v"(v0), [v1] "v"(v1) : "memory", "scc");
}
DEV void dma_k3(u64 sbase, unsigned v0, unsigned v1, unsigned v2, int lds) {
  asm volatile("s_mov_b32 m0, %[m]\n\ts_add_u32 %[m], %[m], 0x400\n\tglobal_load_lds_dwordx4 %[v0], %[sb]\n\t"
               "s_mov_b32 m0, %[m]\n\ts_add_u32 %[m], %[m], 0x400\n\tglobal_load_lds_dwordx4 %[v1], %[sb]\n\t"
               "s_mov_b32 m0, %[m]\n\ts_nop 0\n\tglobal_load_lds_dwordx4 %[v2], %[sb]"
               : [m] "+s"(lds) : [sb] "s"(sbase), [v0] "v"(v0), [v1] "v"(v1), [v2] "v"(v2) : "memory", "scc");
}

#define RLX_AGENT __ATOMIC_RELAXED, __HIP_MEMORY_SCOPE_AGENT
DEV void publish(char* ws, unsigned off, u64 tag, int lane) {
  asm volatile("s_waitcnt vmcnt(0)" ::: "memory");
  if (lane == 0) __hip_atomic_store((GAS u64*)(ws + off), tag, RLX_AGENT);
}
#define PSLEEP ""
DEV void wait_flags(char* ws, unsigned off, u64 tag) {
  u64 v; int n = 1 << 18;
  asm volatile("1:\n\tglobal_load_dwordx2 %[v], %[off], %[ws] sc1\n\ts_sub_u32 %[n], %[n], 1\n\ts_cbranch_scc1 2f\n\t"
               "s_waitcnt vmcnt(0)\n\tv_cmp_ne_u64 vcc, %[v], %[tag]\n\t" PSLEEP "s_cbranch_vccnz 1b\n\t2:\n\ts_waitcnt vmcnt(0)"
               : [v] "=&v"(v), [n] "+s"(n) : [off] "v"(off), [ws] "s"(ws), [tag] "v"(tag) : "memory", "vcc", "scc");
}
typedef __amdgpu_buffer_rsrc_t rsrc_t;
DEV void st16(rsrc_t r, unsigned voff, unsigned soff, u32x4 v) { __builtin_amdgcn_raw_buffer_store_b128(v, r, voff, soff, 16); }
DEV u32x4 ld16(rsrc_t r, unsigned voff, unsigned soff) { return __builtin_amdgcn_raw_buffer_load_b128(r, voff, soff, 16); }
#define LSLEEP 1
#define THR 6
#define W2POS 12
#define WDELAY 4
#define DROPI 4
DEV unsigned min4(u32x4 v) { return __builtin_amdgcn_readfirstlane(min(min(v[0], v[1]), min(v[2], v[3]))); }
DEV void lds_wait(volatile LAS u32x4* p, unsigned have, unsigned need) {
#pragma unroll 1
  while (have < need) { have = min4(*p); if (have < need) __builtin_amdgcn_s_sleep(LSLEEP); }
  asm volatile("" ::: "memory");
}

#define LSP ""
DEV void lds_spin(unsigned addr, unsigned need) {
  unsigned a;
  asm volatile("1:\n\tds_read_b32 %[a], %[p]\n\ts_waitcnt lgkmcnt(0)\n\tv_cmp_gt_u32 vcc, %[n], %[a]\n\t" LSP "s_cbranch_vccnz 1b"
               : [a] "=&v"(a) : [p] "v"(addr), [n] "s"(need) : "memory", "vcc");
}
DEV f32x16 pv(u32x4 v, f32x8 pr, f32x16 o) {
  return __builtin_amdgcn_smfmac_f32_32x32x32_bf16(__builtin_bit_cast(bf16x8, v), __builtin_bit_cast(bf16x16, pr), o, 0xDDDD, 0, 0);
}
DEV f32x8 exp8(f32x16 s, int o) { f32x8 r; for (int i = 0; i < 8; i++) r[i] = __builtin_amdgcn_exp2f(s[o + i]); return r; }
DEV float fma1(float a, float b, float c) { float r = __builtin_fmaf(a, b, c); asm("" : "+v"(r)); return r; }
DEV float add1(float a, float b) { float r = a + b; asm("" : "+v"(r)); return r; }
DEV float dot2(unsigned a, unsigned b, float c) { return __builtin_amdgcn_fdot2_f32_bf16(__builtin_bit_cast(bf16x2, a), __builtin_bit_cast(bf16x2, b), c, false); }
DEV f32x2 pk(float a) { return f32x2{a, a}; }
DEV f32x2 pfma(f32x2 a, f32x2 b, f32x2 c) { return __builtin_elementwise_fma(a, b, c); }
DEV unsigned pack2v(f32x2 p) { return __builtin_bit_cast(unsigned, __builtin_convertvector(p, bf16x2)); }
#define FENCE() __builtin_amdgcn_sched_barrier(0)

struct WFrag { u32x4 a[4]; u32x4 w[4][3]; };
constexpr int L_RES = 159744;
constexpr int L_Q = 67584, L_RED = 75776, L_DUM = 94208;
constexpr int L_W1 = 65536, L_Q1 = 151552;
constexpr int L_B1 = 61440;
constexpr int CODE_BYTES = 28672;
#define RD_ 10
constexpr int RD = RD_;

template <int NK, int NU, int NPRE, class ASO, class BRD, class H1, class H2>
DEV void gemm_direct(rsrc_t rs, unsigned voff, ASO&& aso, BRD&& brd, const u32x4 (*pre)[NU], f32x16 (&acc)[NU], H1&& hook, H2&& hook2) {
  u32x4 a[RD], w[3][NU];
  static_for<0, RD>([&](auto i) { a[i.value] = ld16(rs, voff, aso(i.value)); });
  static_for<0, 2>([&](auto i) {
    if constexpr (i.value >= NPRE) { for (int u = 0; u < NU; u++) w[i.value][u] = brd(i.value, u); }
  });
  FENCE();
  static_for<0, NK>([&](auto pp) {
    constexpr int pos = pp.value;
    static_for<0, NU>([&](auto uu) {
      constexpr int u = uu.value;
      if constexpr (pos < NPRE) acc[u] = mfma(pre[pos][u], a[pos % RD], acc[u]); else acc[u] = mfma(w[pos % 3][u], a[pos % RD], acc[u]);
      FENCE();
      if constexpr (u == 0) hook(pp);
      if constexpr (pos + 2 < NK && pos + 2 >= NPRE) w[(pos + 2) % 3][u] = brd(pos + 2, u);
      if constexpr (u == NU - 1) {
        if constexpr (pos + RD < NK) a[pos % RD] = ld16(rs, voff, aso(pos + RD));
        hook2(pp);
      }
      FENCE();
    });
  });
}

__global__ __launch_bounds__(512, 2) void fused(const u16* __restrict__ x, const u16* __restrict__ wqkv, const u16* __restrict__ wo, const u16* __restrict__ w1,
                                                const u16* __restrict__ b1, const u16* __restrict__ w2, char* __restrict__ ws, u16* __restrict__ out) {
  __shared__ __attribute__((aligned(16))) char smem_[163840];
  LAS char* smem = (LAS char*)smem_;
  const int tid = threadIdx.x, lane = tid & 63, wave = __builtin_amdgcn_readfirstlane(tid >> 6);
  const int b = blockIdx.x, xcd = b & 7, bi = b >> 3;
  STAMP_DECL; STAMP(0);
  if (tid < 12) *(volatile LAS unsigned*)(smem + L_RES + tid * 4) = 0;
  BARRIER();
  u64 pc0; asm volatile("s_getpc_b64 %0" : "=s"(pc0));
  asm volatile("" ::"s"(x), "s"(wqkv), "s"(wo), "s"(w1), "s"(b1), "s"(w2), "s"(ws), "s"(out));
  u64 tag;
  auto load_tag = [&]() { asm volatile("s_load_dwordx2 %0, %1, 0x0\n\ts_waitcnt lgkmcnt(0)" : "=s"(tag) : "s"(ws + WS_EP) : "memory"); tag += 1; };
  const rsrc_t rs = __builtin_amdgcn_make_buffer_rsrc(ws, 0, WS_END, 0x00020000);
  const int r32 = lane & 31, h = lane >> 5;
  const int q = (lane & 15) >> 2, p = lane & 3, blk = (lane >> 4) & 1;
  const int lw = wave & 3;
  const unsigned vrow128 = 16 * ((lane & 7) ^ (((lane >> 4) & 1) << 2));
  volatile LAS unsigned* prog = (volatile LAS unsigned*)(smem + L_RES);

#define MAPB 1
  constexpr int HS = 3 - MAPB;
  const int hg = ((xcd & ((1 << MAPB) - 1)) << HS) + (bi & ((1 << HS) - 1)), rg1 = (xcd >> MAPB) * (32 >> HS) + (bi >> HS);
  volatile LAS u32x4* landed = (volatile LAS u32x4*)(prog + 4);
  const int seg = lane & 7, xr = (8 >> HS) * (bi & ((1 << HS) - 1));
  u32x4 xv[2][8];
  if (wave < 4) {
    const char* xp = (const char*)x + (size_t)(rg1 * 64 + wave * 16 + (lane >> 3)) * 1024 + seg * 16;
#pragma unroll
    for (int t = 0; t < 2; t++)
#pragma unroll
      for (int i = 0; i < 8; i++) xv[t][i] = *(const u32x4*)(xp + t * 8192 + ((i + xr) & 7) * 128);
  }
  asm volatile("" ::: "memory");
  if (__builtin_expect(wave >= 4, 1)) {
    const int c0 = bi >> HS;
    const u64 sw0 = (u64)(wqkv + hg * 64);
    const unsigned vw0 = (lane >> 3) * (3 * D_ * 2) + vrow128, vw1 = vw0 + 8 * 3 * D_ * 2;
    prog[wave] = lw;
    __builtin_amdgcn_s_sleep(WDELAY);
    if (lw >= 2) lds_spin(L_RES + 16 + (lane & 1) * 4, 4);
#pragma unroll 1
    for (int c = lw; c < 16; c += 4) {
      if (c >= 7) lds_spin(L_RES + p * 4, c - 6);
      const u64 src = sw0 + (unsigned)(((c + 2 * c0) & 15) * (32 * 3 * D_ * 2));
      const int slot = L_W1 + (c % 7) * 12288;
      dma_p1(src, vw0, vw1, slot);
      dma_p1(src + 16 * 3 * D_ * 2, vw0, vw1, slot + 2048);
      wait_vm<0>(); prog[wave] = c + 4;
    }
    STAMP(2);
  } else {
    const int rb = wave >> 1, g = wave & 1;
    unsigned tch;
    {
      asm volatile("global_load_dword %0, %1, off" : "=v"(tch) : "v"((const char*)(pc0 & ~127ull) + min((wave * 64 + lane) * 128, CODE_BYTES - 128)) : "memory");
      load_tag();
      if (tid == 0) *(LAS u64*)(smem + L_RES + 64) = tag;
      STAMP(1);
#pragma unroll
      for (int t = 0; t < 2; t++) {
        float s4[4] = {0.f, 0.f, 0.f, 0.f}, q4[4] = {0.f, 0.f, 0.f, 0.f};
#pragma unroll
        for (int i = 0; i < 8; i++)
#pragma unroll
          for (int j = 0; j < 4; j++) { s4[j] = dot2(xv[t][i][j], 0x3f803f80u, s4[j]); q4[j] = dot2(xv[t][i][j], xv[t][i][j], q4[j]); }
        float sx = (s4[0] + s4[1]) + (s4[2] + s4[3]), sxx = (q4[0] + q4[1]) + (q4[2] + q4[3]);
        sx += asf(__builtin_amdgcn_mov_dpp(asu(sx), 0xB1, 0xf, 0xf, true)); sxx += asf(__builtin_amdgcn_mov_dpp(asu(sxx), 0xB1, 0xf, 0xf, true));
        sx += asf(__builtin_amdgcn_mov_dpp(asu(sx), 0x4E, 0xf, 0xf, true)); sxx += asf(__builtin_amdgcn_mov_dpp(asu(sxx), 0x4E, 0xf, 0xf, true));
        sx += asf(__builtin_amdgcn_mov_dpp(asu(sx), 0x141, 0xf, 0xf, true)); sxx += asf(__builtin_amdgcn_mov_dpp(asu(sxx), 0x141, 0xf, 0xf, true));
        const float mean = sx * (1.f / D_);
        const float rstd = __builtin_amdgcn_rsqf(fmaxf(sxx * (1.f / D_) - mean * mean, 0.f) + 1e-5f), nmr = -mean * rstd;
        const int r = g * 16 + t * 8 + (lane >> 3);
        LAS char* dst = smem + rb * 32768 + seg * 512 + ((r ^ seg) << 4);
#pragma unroll
        for (int i0 = 0; i0 < 8; i0 += 2) {
          f32x2 y[8];
#pragma unroll
          for (int j = 0; j < 8; j++) { const unsigned u = xv[t][i0 + (j >> 2)][j & 3]; y[j] = f32x2{bflo(u), bfhi(u)}; }
          FENCE();
#pragma unroll
          for (int j = 0; j < 8; j++) y[j] = pfma(y[j], pk(rstd), pk(nmr));
          FENCE();
          const u32x4 o0 = {pack2v(y[0]), pack2v(y[1]), pack2v(y[2]), pack2v(y[3])}, o1 = {pack2v(y[4]), pack2v(y[5]), pack2v(y[6]), pack2v(y[7])};
          *(LAS u32x4*)(dst + ((i0 + xr) & 7) * 4096) = o0;
          *(LAS u32x4*)(dst + ((i0 + 1 + xr) & 7) * 4096) = o1;
          FENCE();
        }
      }
      prog[8 + wave] = 2;
    }
    const int c0 = bi >> HS;
    int aoff[4];
#pragma unroll
    for (int i = 0; i < 4; i++) aoff[i] = rb * 32768 + i * 1024 + h * 512 + ((r32 ^ (2 * i + h)) << 4);
    const int boff = L_W1 + (8 * h + q) * 128 + (((4 * g + 2 * blk + (p >> 1)) ^ ((q >> 1) << 2)) << 4) + 8 * (p & 1);
    f32x16 acc[3];
#pragma unroll
    for (int u = 0; u < 3; u++) for (int i = 0; i < 16; i++) acc[u][i] = 0.f;
    u32x4 fl;
    auto rd = [&](WFrag& f, int c) {
      const int ao = ((c + c0) & 7) * 4096;
#pragma unroll
      for (int i = 0; i < 4; i++) {
        f.a[i] = *(LAS u32x4*)(smem + aoff[i] + ao);
#pragma unroll
        for (int u = 0; u < 3; u++) f.w[i][u] = tr_frag(smem + boff + ((2 * c + (i >> 1)) % 7) * 12288 + (i & 1) * 2048 + u * 4096, 512);
        if (i & 1) prog[wave] = 2 * c + (i >> 1) + 1;
      }
      fl = *landed;
    };
    auto mm = [&](const WFrag& f) {
#pragma unroll
      for (int i = 0; i < 4; i++)
#pragma unroll
        for (int u = 0; u < 3; u++) { if (u == 2) acc[u] = mfma(f.a[i], f.w[i][u], acc[u]); else acc[u] = mfma(f.w[i][u], f.a[i], acc[u]); }
    };
    auto sched = [&]() {
      SGB(0x8, 1); SGB(0x2, 7);
#pragma unroll
      for (int i = 0; i < 11; i++) { SGB(0x80, 3); SGB(0x8, 1); }
    };
    auto step = [&](int c) { FENCE(); lds_wait(landed, min4(fl), 2 * c + 2); FENCE(); };
    WFrag fa, fb;
    STAMP(23);
    lds_spin(L_RES + ((lane & 4) ? 32 + rb * 8 + (lane & 1) * 4 : 16 + p * 4), 2);
    STAMP(24);
    rd(fa, 0);
    static_for<0, 3>([&](auto cc) {
      constexpr int c = 2 * cc.value + 1;
      step(c);
      rd(fb, c); mm(fa); sched();
      step(c + 1);
      rd(fa, c + 1); mm(fb); sched();
    });
    step(7);
    STAMP(2);
    rd(fb, 7); mm(fa); sched();
    FENCE();
    mm(fb);
    STAMP(3);
    const int tile = rg1 * 2 + rb;
    const unsigned so = ((hg * 4 + 2 * g) * 64 + tile) * 1024;
    {
      unsigned o[8];
#pragma unroll
      for (int i = 0; i < 8; i++) o[i] = pack2(acc[1][2 * i], acc[1][2 * i + 1]);
      const unsigned pos = h * 32 + ((r32 & 19) | ((r32 & 4) << 1) | ((r32 & 8) >> 1));
      st16(rs, pos * 16, WS_K + so, u32x4{o[0], o[1], o[2], o[3]});
      st16(rs, pos * 16, WS_K + so + 65536, u32x4{o[4], o[5], o[6], o[7]});
#pragma unroll
      for (int i = 0; i < 8; i++) o[i] = pack2(acc[2][2 * i], acc[2][2 * i + 1]);
      const unsigned vo = (r32 >> 4) * 65536 + h * 256 + (r32 & 15) * 16;
      st16(rs, vo, WS_V + so, u32x4{o[0], o[1], o[2], o[3]});
      st16(rs, vo, WS_V + so + 512, u32x4{o[4], o[5], o[6], o[7]});
      STAMP(4);
#pragma unroll
      for (int i = 0; i < 8; i++) o[i] = pack2v(f32x2{acc[0][2 * i], acc[0][2 * i + 1]} * pk(0.25f * LOG2E));
      LAS char* d = smem + L_Q1 + (4 * g + rb) * 1024 + lane * 16;
      *(LAS u32x4*)d = u32x4{o[0], o[1], o[2], o[3]};
      *(LAS u32x4*)(d + 2048) = u32x4{o[4], o[5], o[6], o[7]};
    }
    publish(ws, WS_F1 + (((hg * 2 + g) * 32 + rg1) * 2 + rb) * 8, tag, lane);
    asm volatile("" ::"v"(tch));
    STAMP(5);
  }
  BARRIER();
  tag = *(LAS u64*)(smem + L_RES + 64);

  const int rg = xcd * 4 + (bi >> 3), cs = bi & 7;
  const int rp = xcd * 2 + (bi >> 4), sl = bi & 15;
  const int unit0 = sl < 5 ? 2 * sl : 3 * sl - 5, nunit = sl < 5 ? 2 : 3;
  const int off3 = (cs & 3) * 4, off4 = (sl & 3) * 8, off5 = (0x080e161c00ull >> (8 * (cs % 5))) & 0xff;
  const unsigned vw = (lane >> 3) * (D_ * 2) + vrow128;
  auto v192 = [&](int j) { const int idx = j * 64 + lane, row = idx / 12; return (unsigned)(row * (F_ * 2) + (idx - row * 12) * 16); };

  const unsigned v30 = v192(0), v31 = v192(1), v32 = v192(2);
  u32x16 sg[7];
  u32x4 res[2];
  auto stg = [&](int i) { return u32x4{sg[i >> 2][(i & 3) * 4], sg[i >> 2][(i & 3) * 4 + 1], sg[i >> 2][(i & 3) * 4 + 2], sg[i >> 2][(i & 3) * 4 + 3]}; };
  auto w2at = [&](int pos) {
    int ks = pos + off5; ks = ks >= 43 ? ks - 43 : ks;
    return (const char*)(w2 + (size_t)((lw & 1) * 43 + ks) * 16 * D_ + cs * 64);
  };
  const size_t rowoff = (size_t)((rg * 2 + (lw >> 1)) * 32 + r32) * D_ + cs * 64 + (lw & 1) * 32 + 8 * h;

  {
    const int head = hg * 4 + (wave >> 1), kh = wave & 1;
    const u32x4 q0 = *(LAS u32x4*)(smem + L_Q1 + (wave >> 1) * 2048 + lane * 16), q1 = *(LAS u32x4*)(smem + L_Q1 + (wave >> 1) * 2048 + 1024 + lane * 16);
    BARRIER();
    const int drop = (wave >> 2) ^ 1;
    if (wave < 4) __builtin_amdgcn_s_setprio(2); else __builtin_amdgcn_s_setprio(1);
    if (wave >= 6) {
      const char* tp = lane < 43 ? (const char*)((lw & 1) ? w2 : w1) + b * 5504 + lane * 128
                     : lane < 51 ? (const char*)wo + b * 2048 + (lw & 1) * 1024 + (lane - 43) * 128 : (const char*)b1 + min(((lw & 1) * 13 + lane - 51) * 128, F_ * 2 - 4);
      const unsigned t = *(const unsigned*)tp; asm volatile("" ::"v"(t));
    }
    wait_flags(ws, WS_F1 + (((hg * 2 + (wave >> 2)) * 32 + 16 * kh) * 2 + r32) * 8, tag);
    STAMP(6); STAMPC(26);
    f32x16 o0, o1;
#pragma unroll
    for (int i = 0; i < 16; i++) { o0[i] = 0.f; o1[i] = 0.f; }
    {
      const unsigned koff = lane * 16, voff = h * 256 + (lane & 15) * 16;
      const char* kb = ws + WS_K + (size_t)head * 65536 + kh * 32768;
      const char* vb = ws + WS_V + (size_t)head * 65536 + kh * 32768;
      const unsigned vinit = (r32 == 16 || r32 == 20) ? 0x3f803f80u : 0u;
      const unsigned a0 = wave < 4 ? v30 : vw, a1 = wave < 4 ? v31 : vw + 8 * D_ * 2, a2 = wave < 4 ? v32 : vw + 16 * D_ * 2;
      const char* sa = wave < 4 ? (const char*)(w1 + (size_t)((lw * 4 + off4) & 31) * 16 * F_ + unit0 * 32) : w2at(15 + (lw >> 1) * 14);
      const char* sb = w2at(wave < 4 ? (lw >> 1) * 7 : 21 + (lw >> 1) * 14);
      const int ga = wave < 4 ? 16 * F_ * 2 : 24 * D_ * 2;
      const char* so = (const char*)(wo + (size_t)((wave >> 2) * 256 + ((lw * 4 + off3) & 15) * 16) * D_ + cs * 64) - 8192;
      const char* sh = (const char*)(w1 + (size_t)((16 + wave * 2 + off4) & 31) * 16 * F_ + unit0 * 32);
      const int ol = wave * 8192, hl = L_B1 + (16 + wave * 2) * 3072;
      asm volatile(
        "s_mov_b64 s[40:41], %[kin]\n\ts_mov_b64 s[42:43], %[vin]\n\tglobal_load_dwordx4 v[80:83], %[koff], s[40:41] offset:0 sc1\n\ts_mov_b64 s[46:47], %[sa]\n\t"
        "s_mov_b64 s[48:49], %[sb]\n\ts_mov_b64 s[50:51], %[so]\n\ts_mov_b64 s[52:53], %[sh]\n\tv_mov_b32 v96, %[vinit]\n\tv_mov_b32 v112, %[vinit]\n\tv_mov_b32 v97, %[vinit]\n\t"
        "v_mov_b32 v113, %[vinit]\n\tv_mov_b32 v98, %[vinit]\n\tv_mov_b32 v114, %[vinit]\n\tv_mov_b32 v99, %[vinit]\n\tv_mov_b32 v115, %[vinit]\n\t"
        "global_load_dwordx4 v[84:87], %[koff], s[40:41] offset:1024 sc1\n\ts_mov_b64 exec, %[vm]\n\tglobal_load_dwordx4 v[96:99], %[voff], s[42:43] offset:0 sc1\n\t"
        "global_load_dwordx4 v[112:115], %[voff], s[42:43] offset:512 sc1\n\ts_mov_b64 exec, -1\n\tv_mov_b32 v100, %[vinit]\n\tv_mov_b32 v116, %[vinit]\n\tv_mov_b32 v101, %[vinit]\n\t"
        "v_mov_b32 v117, %[vinit]\n\tv_mov_b32 v102, %[vinit]\n\tv_mov_b32 v118, %[vinit]\n\tv_mov_b32 v103, %[vinit]\n\tv_mov_b32 v119, %[vinit]\n\t"
        "global_load_dwordx4 v[88:91], %[koff], s[40:41] offset:2048 sc1\n\ts_mov_b64 exec, %[vm]\n\tglobal_load_dwordx4 v[100:103], %[voff], s[42:43] offset:1024 sc1\n\t"
        "global_load_dwordx4 v[116:119], %[voff], s[42:43] offset:1536 sc1\n\ts_mov_b64 exec, -1\n\tv_mov_b32 v104, %[vinit]\n\tv_mov_b32 v120, %[vinit]\n\tv_mov_b32 v105, %[vinit]\n\t"
        "v_mov_b32 v121, %[vinit]\n\tv_mov_b32 v106, %[vinit]\n\tv_mov_b32 v122, %[vinit]\n\tv_mov_b32 v107, %[vinit]\n\tv_mov_b32 v123, %[vinit]\n\t"
        "global_load_dwordx4 v[92:95], %[koff], s[40:41] offset:3072 sc1\n\ts_mov_b64 exec, %[vm]\n\tglobal_load_dwordx4 v[104:107], %[voff], s[42:43] offset:2048 sc1\n\t"
        "global_load_dwordx4 v[120:123], %[voff], s[42:43] offset:2560 sc1\n\ts_mov_b64 exec, -1\n\tv_mov_b32 v108, %[vinit]\n\tv_mov_b32 v124, %[vinit]\n\tv_mov_b32 v109, %[vinit]\n\t"
        "v_mov_b32 v125, %[vinit]\n\tv_mov_b32 v110, %[vinit]\n\tv_mov_b32 v126, %[vinit]\n\tv_mov_b32 v111, %[vinit]\n\tv_mov_b32 v127, %[vinit]\n\tv_mov_b32 v128, 0xdddd\n\t"
        "s_waitcnt vmcnt(9)\n\tv_mfma_f32_32x32x16_bf16 v[48:63], v[80:83], %[q0], 0\n\ts_nop 15\n\tv_exp_f32 v48, v48\n\tv_exp_f32 v49, v49\n\tv_exp_f32 v50, v50\n\tv_exp_f32 v51, v51\n\t"
        "v_exp_f32 v52, v52\n\tv_mfma_f32_32x32x16_bf16 v[64:79], v[80:83], %[q1], 0\n\ts_add_u32 s40, s40, 4096\n\ts_addc_u32 s41, s41, 0\n\t"
        "global_load_dwordx4 v[80:83], %[koff], s[40:41] offset:0 sc1\n\ts_mov_b64 exec, %[vm]\n\tglobal_load_dwordx4 v[108:111], %[voff], s[42:43] offset:3072 sc1\n\t"
        "global_load_dwordx4 v[124:127], %[voff], s[42:43] offset:3584 sc1\n\ts_mov_b64 exec, -1\n\tv_exp_f32 v53, v53\n\tv_exp_f32 v54, v54\n\tv_exp_f32 v55, v55\n\tv_exp_f32 v56, v56\n\t"
        "v_exp_f32 v57, v57\n\ts_waitcnt vmcnt(9)\n\tv_smfmac_f32_32x32x32_bf16 %[o0], v[96:99], v[48:55], v128\n\tv_exp_f32 v58, v58\n\tv_exp_f32 v59, v59\n\tv_exp_f32 v60, v60\n\t"
        "v_exp_f32 v61, v61\n\tv_exp_f32 v62, v62\n\tv_exp_f32 v63, v63\n\tv_exp_f32 v64, v64\n\tv_exp_f32 v65, v65\n\tv_smfmac_f32_32x32x32_bf16 %[o0], v[112:115], v[56:63], v128\n\t"
        "v_exp_f32 v66, v66\n\tv_exp_f32 v67, v67\n\tv_exp_f32 v68, v68\n\tv_mfma_f32_32x32x16_bf16 v[48:63], v[84:87], %[q0], 0\n\tv_exp_f32 v69, v69\n\tv_exp_f32 v70, v70\n\t"
        "v_exp_f32 v71, v71\n\tv_exp_f32 v72, v72\n\tv_exp_f32 v73, v73\n\tv_smfmac_f32_32x32x32_bf16 %[o1], v[96:99], v[64:71], v128\n\tv_exp_f32 v74, v74\n\tv_exp_f32 v75, v75\n\t"
        "v_exp_f32 v76, v76\n\tv_exp_f32 v77, v77\n\tv_exp_f32 v78, v78\n\tv_exp_f32 v79, v79\n\tv_exp_f32 v48, v48\n\tv_exp_f32 v49, v49\n\t"
        "v_smfmac_f32_32x32x32_bf16 %[o1], v[112:115], v[72:79], v128\n\tv_exp_f32 v50, v50\n\tv_exp_f32 v51, v51\n\tv_exp_f32 v52, v52\n\t"
        "v_mfma_f32_32x32x16_bf16 v[64:79], v[84:87], %[q1], 0\n\tglobal_load_dwordx4 v[84:87], %[koff], s[40:41] offset:1024 sc1\n\ts_add_u32 s42, s42, 4096\n\ts_addc_u32 s43, s43, 0\n\t"
        "s_mov_b64 exec, %[vm]\n\tglobal_load_dwordx4 v[96:99], %[voff], s[42:43] offset:0 sc1\n\tglobal_load_dwordx4 v[112:115], %[voff], s[42:43] offset:512 sc1\n\ts_mov_b64 exec, -1\n\t"
        "v_exp_f32 v53, v53\n\tv_exp_f32 v54, v54\n\tv_exp_f32 v55, v55\n\tv_exp_f32 v56, v56\n\tv_exp_f32 v57, v57\n\ts_waitcnt vmcnt(9)\n\t"
        "v_smfmac_f32_32x32x32_bf16 %[o0], v[100:103], v[48:55], v128\n\tv_exp_f32 v58, v58\n\tv_exp_f32 v59, v59\n\tv_exp_f32 v60, v60\n\tv_exp_f32 v61, v61\n\tv_exp_f32 v62, v62\n\t"
        "v_exp_f32 v63, v63\n\tv_exp_f32 v64, v64\n\tv_exp_f32 v65, v65\n\tv_smfmac_f32_32x32x32_bf16 %[o0], v[116:119], v[56:63], v128\n\tv_exp_f32 v66, v66\n\tv_exp_f32 v67, v67\n\t"
        "v_exp_f32 v68, v68\n\tv_mfma_f32_32x32x16_bf16 v[48:63], v[88:91], %[q0], 0\n\tglobal_load_dwordx4 v[144:147], %[a0], s[46:47]\n\ts_add_u32 m0, %[ol], 0\n\t"
        "s_add_u32 s50, s50, 8192\n\ts_addc_u32 s51, s51, 0\n\tglobal_load_lds_dwordx4 %[vw], s[50:51]\n\tv_exp_f32 v69, v69\n\tv_exp_f32 v70, v70\n\tv_exp_f32 v71, v71\n\t"
        "v_exp_f32 v72, v72\n\tv_exp_f32 v73, v73\n\tv_smfmac_f32_32x32x32_bf16 %[o1], v[100:103], v[64:71], v128\n\tv_exp_f32 v74, v74\n\tv_exp_f32 v75, v75\n\tv_exp_f32 v76, v76\n\t"
        "v_exp_f32 v77, v77\n\tv_exp_f32 v78, v78\n\tv_exp_f32 v79, v79\n\tv_exp_f32 v48, v48\n\tv_exp_f32 v49, v49\n\tv_smfmac_f32_32x32x32_bf16 %[o1], v[116:119], v[72:79], v128\n\t"
        "v_exp_f32 v50, v50\n\tv_exp_f32 v51, v51\n\tv_exp_f32 v52, v52\n\tv_mfma_f32_32x32x16_bf16 v[64:79], v[88:91], %[q1], 0\n\t"
        "global_load_dwordx4 v[88:91], %[koff], s[40:41] offset:2048 sc1\n\ts_mov_b64 exec, %[vm]\n\tglobal_load_dwordx4 v[100:103], %[voff], s[42:43] offset:1024 sc1\n\t"
        "global_load_dwordx4 v[116:119], %[voff], s[42:43] offset:1536 sc1\n\ts_mov_b64 exec, -1\n\tv_exp_f32 v53, v53\n\tv_exp_f32 v54, v54\n\tv_exp_f32 v55, v55\n\tv_exp_f32 v56, v56\n\t"
        "v_exp_f32 v57, v57\n\ts_waitcnt vmcnt(11)\n\tv_smfmac_f32_32x32x32_bf16 %[o0], v[104:107], v[48:55], v128\n\tv_exp_f32 v58, v58\n\tv_exp_f32 v59, v59\n\tv_exp_f32 v60, v60\n\t"
        "v_exp_f32 v61, v61\n\tv_exp_f32 v62, v62\n\tv_exp_f32 v63, v63\n\tv_exp_f32 v64, v64\n\tv_exp_f32 v65, v65\n\tv_smfmac_f32_32x32x32_bf16 %[o0], v[120:123], v[56:63], v128\n\t"
        "v_exp_f32 v66, v66\n\tv_exp_f32 v67, v67\n\tv_exp_f32 v68, v68\n\tv_mfma_f32_32x32x16_bf16 v[48:63], v[92:95], %[q0], 0\n\tglobal_load_dwordx4 v[148:151], %[a1], s[46:47]\n\t"
        "v_exp_f32 v69, v69\n\tv_exp_f32 v70, v70\n\tv_exp_f32 v71, v71\n\tv_exp_f32 v72, v72\n\tv_exp_f32 v73, v73\n\tv_smfmac_f32_32x32x32_bf16 %[o1], v[104:107], v[64:71], v128\n\t"
        "v_exp_f32 v74, v74\n\tv_exp_f32 v75, v75\n\tv_exp_f32 v76, v76\n\tv_exp_f32 v77, v77\n\tv_exp_f32 v78, v78\n\tv_exp_f32 v79, v79\n\tv_exp_f32 v48, v48\n\tv_exp_f32 v49, v49\n\t"
        "v_smfmac_f32_32x32x32_bf16 %[o1], v[120:123], v[72:79], v128\n\tv_exp_f32 v50, v50\n\tv_exp_f32 v51, v51\n\tv_exp_f32 v52, v52\n\t"
        "v_mfma_f32_32x32x16_bf16 v[64:79], v[92:95], %[q1], 0\n\tglobal_load_dwordx4 v[92:95], %[koff], s[40:41] offset:3072 sc1\n\ts_mov_b64 exec, %[vm]\n\t"
        "global_load_dwordx4 v[104:107], %[voff], s[42:43] offset:2048 sc1\n\tglobal_load_dwordx4 v[120:123], %[voff], s[42:43] offset:2560 sc1\n\ts_mov_b64 exec, -1\n\t"
        "v_exp_f32 v53, v53\n\tv_exp_f32 v54, v54\n\tv_exp_f32 v55, v55\n\tv_exp_f32 v56, v56\n\tv_exp_f32 v57, v57\n\ts_waitcnt vmcnt(12)\n\t"
        "v_smfmac_f32_32x32x32_bf16 %[o0], v[108:111], v[48:55], v128\n\tv_exp_f32 v58, v58\n\tv_exp_f32 v59, v59\n\tv_exp_f32 v60, v60\n\tv_exp_f32 v61, v61\n\tv_exp_f32 v62, v62\n\t"
        "v_exp_f32 v63, v63\n\tv_exp_f32 v64, v64\n\tv_exp_f32 v65, v65\n\tv_smfmac_f32_32x32x32_bf16 %[o0], v[124:127], v[56:63], v128\n\tv_exp_f32 v66, v66\n\tv_exp_f32 v67, v67\n\t"
        "v_exp_f32 v68, v68\n\tv_mfma_f32_32x32x16_bf16 v[48:63], v[80:83], %[q0], 0\n\tglobal_load_dwordx4 v[152:155], %[a2], s[46:47]\n\ts_add_u32 s46, s46, %[ga]\n\t"
        "s_addc_u32 s47, s47, 0\n\ts_add_u32 m0, %[ol], 1024\n\ts_add_u32 s50, s50, 8192\n\ts_addc_u32 s51, s51, 0\n\tglobal_load_lds_dwordx4 %[vw], s[50:51]\n\tv_exp_f32 v69, v69\n\t"
        "v_exp_f32 v70, v70\n\tv_exp_f32 v71, v71\n\tv_exp_f32 v72, v72\n\tv_exp_f32 v73, v73\n\tv_smfmac_f32_32x32x32_bf16 %[o1], v[108:111], v[64:71], v128\n\tv_exp_f32 v74, v74\n\t"
        "v_exp_f32 v75, v75\n\tv_exp_f32 v76, v76\n\tv_exp_f32 v77, v77\n\tv_exp_f32 v78, v78\n\tv_exp_f32 v79, v79\n\tv_exp_f32 v48, v48\n\tv_exp_f32 v49, v49\n\t"
        "v_smfmac_f32_32x32x32_bf16 %[o1], v[124:127], v[72:79], v128\n\tv_exp_f32 v50, v50\n\tv_exp_f32 v51, v51\n\tv_exp_f32 v52, v52\n\t"
        "v_mfma_f32_32x32x16_bf16 v[64:79], v[80:83], %[q1], 0\n\ts_add_u32 s40, s40, 4096\n\ts_addc_u32 s41, s41, 0\n\tglobal_load_dwordx4 v[80:83], %[koff], s[40:41] offset:0 sc1\n\t"
        "s_mov_b64 exec, %[vm]\n\tglobal_load_dwordx4 v[108:111], %[voff], s[42:43] offset:3072 sc1\n\tglobal_load_dwordx4 v[124:127], %[voff], s[42:43] offset:3584 sc1\n\t"
        "s_mov_b64 exec, -1\n\tv_exp_f32 v53, v53\n\tv_exp_f32 v54, v54\n\tv_exp_f32 v55, v55\n\tv_exp_f32 v56, v56\n\tv_exp_f32 v57, v57\n\ts_waitcnt vmcnt(14)\n\t"
        "v_smfmac_f32_32x32x32_bf16 %[o0], v[96:99], v[48:55], v128\n\tv_exp_f32 v58, v58\n\tv_exp_f32 v59, v59\n\tv_exp_f32 v60, v60\n\tv_exp_f32 v61, v61\n\tv_exp_f32 v62, v62\n\t"
        "v_exp_f32 v63, v63\n\tv_exp_f32 v64, v64\n\tv_exp_f32 v65, v65\n\tv_smfmac_f32_32x32x32_bf16 %[o0], v[112:115], v[56:63], v128\n\tv_exp_f32 v66, v66\n\tv_exp_f32 v67, v67\n\t"
        "v_exp_f32 v68, v68\n\tv_mfma_f32_32x32x16_bf16 v[48:63], v[84:87], %[q0], 0\n\tglobal_load_dwordx4 v[156:159], %[a0], s[46:47]\n\tv_exp_f32 v69, v69\n\tv_exp_f32 v70, v70\n\t"
        "v_exp_f32 v71, v71\n\tv_exp_f32 v72, v72\n\tv_exp_f32 v73, v73\n\tv_smfmac_f32_32x32x32_bf16 %[o1], v[96:99], v[64:71], v128\n\tv_exp_f32 v74, v74\n\tv_exp_f32 v75, v75\n\t"
        "v_exp_f32 v76, v76\n\tv_exp_f32 v77, v77\n\tv_exp_f32 v78, v78\n\tv_exp_f32 v79, v79\n\tv_exp_f32 v48, v48\n\tv_exp_f32 v49, v49\n\t"
        "v_smfmac_f32_32x32x32_bf16 %[o1], v[112:115], v[72:79], v128\n\tv_exp_f32 v50, v50\n\tv_exp_f32 v51, v51\n\tv_exp_f32 v52, v52\n\t"
        "v_mfma_f32_32x32x16_bf16 v[64:79], v[84:87], %[q1], 0\n\tglobal_load_dwordx4 v[84:87], %[koff], s[40:41] offset:1024 sc1\n\ts_add_u32 s42, s42, 4096\n\ts_addc_u32 s43, s43, 0\n\t"
        "s_mov_b64 exec, %[vm]\n\tglobal_load_dwordx4 v[96:99], %[voff], s[42:43] offset:0 sc1\n\tglobal_load_dwordx4 v[112:115], %[voff], s[42:43] offset:512 sc1\n\ts_mov_b64 exec, -1\n\t"
        "v_exp_f32 v53, v53\n\tv_exp_f32 v54, v54\n\tv_exp_f32 v55, v55\n\tv_exp_f32 v56, v56\n\tv_exp_f32 v57, v57\n\ts_waitcnt vmcnt(13)\n\t"
        "v_smfmac_f32_32x32x32_bf16 %[o0], v[100:103], v[48:55], v128\n\tv_exp_f32 v58, v58\n\tv_exp_f32 v59, v59\n\tv_exp_f32 v60, v60\n\tv_exp_f32 v61, v61\n\tv_exp_f32 v62, v62\n\t"
        "v_exp_f32 v63, v63\n\tv_exp_f32 v64, v64\n\tv_exp_f32 v65, v65\n\tv_smfmac_f32_32x32x32_bf16 %[o0], v[116:119], v[56:63], v128\n\tv_exp_f32 v66, v66\n\tv_exp_f32 v67, v67\n\t"
        "v_exp_f32 v68, v68\n\tv_mfma_f32_32x32x16_bf16 v[48:63], v[88:91], %[q0], 0\n\tglobal_load_dwordx4 v[160:163], %[a1], s[46:47]\n\ts_add_u32 m0, %[ol], 2048\n\t"
        "s_add_u32 s50, s50, 8192\n\ts_addc_u32 s51, s51, 0\n\tglobal_load_lds_dwordx4 %[vw], s[50:51]\n\tv_exp_f32 v69, v69\n\tv_exp_f32 v70, v70\n\tv_exp_f32 v71, v71\n\t"
        "v_exp_f32 v72, v72\n\tv_exp_f32 v73, v73\n\tv_smfmac_f32_32x32x32_bf16 %[o1], v[100:103], v[64:71], v128\n\tv_exp_f32 v74, v74\n\tv_exp_f32 v75, v75\n\tv_exp_f32 v76, v76\n\t"
        "v_exp_f32 v77, v77\n\tv_exp_f32 v78, v78\n\tv_exp_f32 v79, v79\n\tv_exp_f32 v48, v48\n\tv_exp_f32 v49, v49\n\tv_smfmac_f32_32x32x32_bf16 %[o1], v[116:119], v[72:79], v128\n\t"
        "v_exp_f32 v50, v50\n\tv_exp_f32 v51, v51\n\tv_exp_f32 v52, v52\n\tv_mfma_f32_32x32x16_bf16 v[64:79], v[88:91], %[q1], 0\n\t"
        "global_load_dwordx4 v[88:91], %[koff], s[40:41] offset:2048 sc1\n\ts_mov_b64 exec, %[vm]\n\tglobal_load_dwordx4 v[100:103], %[voff], s[42:43] offset:1024 sc1\n\t"
        "global_load_dwordx4 v[116:119], %[voff], s[42:43] offset:1536 sc1\n\ts_mov_b64 exec, -1\n\tv_exp_f32 v53, v53\n\tv_exp_f32 v54, v54\n\tv_exp_f32 v55, v55\n\tv_exp_f32 v56, v56\n\t"
        "v_exp_f32 v57, v57\n\ts_waitcnt vmcnt(14)\n\tv_smfmac_f32_32x32x32_bf16 %[o0], v[104:107], v[48:55], v128\n\tv_exp_f32 v58, v58\n\tv_exp_f32 v59, v59\n\tv_exp_f32 v60, v60\n\t"
        "v_exp_f32 v61, v61\n\tv_exp_f32 v62, v62\n\tv_exp_f32 v63, v63\n\tv_exp_f32 v64, v64\n\tv_exp_f32 v65, v65\n\tv_smfmac_f32_32x32x32_bf16 %[o0], v[120:123], v[56:63], v128\n\t"
        "v_exp_f32 v66, v66\n\tv_exp_f32 v67, v67\n\tv_exp_f32 v68, v68\n\tv_mfma_f32_32x32x16_bf16 v[48:63], v[92:95], %[q0], 0\n\tglobal_load_dwordx4 v[164:167], %[a2], s[46:47]\n\t"
        "s_add_u32 s46, s46, %[ga]\n\ts_addc_u32 s47, s47, 0\n\tv_exp_f32 v69, v69\n\tv_exp_f32 v70, v70\n\tv_exp_f32 v71, v71\n\tv_exp_f32 v72, v72\n\tv_exp_f32 v73, v73\n\t"
        "v_smfmac_f32_32x32x32_bf16 %[o1], v[104:107], v[64:71], v128\n\tv_exp_f32 v74, v74\n\tv_exp_f32 v75, v75\n\tv_exp_f32 v76, v76\n\tv_exp_f32 v77, v77\n\tv_exp_f32 v78, v78\n\t"
        "v_exp_f32 v79, v79\n\tv_exp_f32 v48, v48\n\tv_exp_f32 v49, v49\n\tv_smfmac_f32_32x32x32_bf16 %[o1], v[120:123], v[72:79], v128\n\tv_exp_f32 v50, v50\n\tv_exp_f32 v51, v51\n\t"
        "v_exp_f32 v52, v52\n\tv_mfma_f32_32x32x16_bf16 v[64:79], v[92:95], %[q1], 0\n\tglobal_load_dwordx4 v[92:95], %[koff], s[40:41] offset:3072 sc1\n\ts_mov_b64 exec, %[vm]\n\t"
        "global_load_dwordx4 v[104:107], %[voff], s[42:43] offset:2048 sc1\n\tglobal_load_dwordx4 v[120:123], %[voff], s[42:43] offset:2560 sc1\n\ts_mov_b64 exec, -1\n\t"
        "v_exp_f32 v53, v53\n\tv_exp_f32 v54, v54\n\tv_exp_f32 v55, v55\n\tv_exp_f32 v56, v56\n\tv_exp_f32 v57, v57\n\ts_waitcnt vmcnt(13)\n\t"
        "v_smfmac_f32_32x32x32_bf16 %[o0], v[108:111], v[48:55], v128\n\tv_exp_f32 v58, v58\n\tv_exp_f32 v59, v59\n\tv_exp_f32 v60, v60\n\tv_exp_f32 v61, v61\n\tv_exp_f32 v62, v62\n\t"
        "v_exp_f32 v63, v63\n\tv_exp_f32 v64, v64\n\tv_exp_f32 v65, v65\n\tv_smfmac_f32_32x32x32_bf16 %[o0], v[124:127], v[56:63], v128\n\tv_exp_f32 v66, v66\n\tv_exp_f32 v67, v67\n\t"
        "v_exp_f32 v68, v68\n\tv_mfma_f32_32x32x16_bf16 v[48:63], v[80:83], %[q0], 0\n\tglobal_load_dwordx4 v[168:171], %[a0], s[46:47]\n\ts_add_u32 m0, %[ol], 3072\n\t"
        "s_add_u32 s50, s50, 8192\n\ts_addc_u32 s51, s51, 0\n\tglobal_load_lds_dwordx4 %[vw], s[50:51]\n\tv_exp_f32 v69, v69\n\tv_exp_f32 v70, v70\n\tv_exp_f32 v71, v71\n\t"
        "v_exp_f32 v72, v72\n\tv_exp_f32 v73, v73\n\tv_smfmac_f32_32x32x32_bf16 %[o1], v[108:111], v[64:71], v128\n\tv_exp_f32 v74, v74\n\tv_exp_f32 v75, v75\n\tv_exp_f32 v76, v76\n\t"
        "v_exp_f32 v77, v77\n\tv_exp_f32 v78, v78\n\tv_exp_f32 v79, v79\n\tv_exp_f32 v48, v48\n\tv_exp_f32 v49, v49\n\tv_smfmac_f32_32x32x32_bf16 %[o1], v[124:127], v[72:79], v128\n\t"
        "v_exp_f32 v50, v50\n\tv_exp_f32 v51, v51\n\tv_exp_f32 v52, v52\n\tv_mfma_f32_32x32x16_bf16 v[64:79], v[80:83], %[q1], 0\n\ts_add_u32 s40, s40, 4096\n\ts_addc_u32 s41, s41, 0\n\t"
        "global_load_dwordx4 v[80:83], %[koff], s[40:41] offset:0 sc1\n\ts_mov_b64 exec, %[vm]\n\tglobal_load_dwordx4 v[108:111], %[voff], s[42:43] offset:3072 sc1\n\t"
        "global_load_dwordx4 v[124:127], %[voff], s[42:43] offset:3584 sc1\n\ts_mov_b64 exec, -1\n\tv_exp_f32 v53, v53\n\tv_exp_f32 v54, v54\n\tv_exp_f32 v55, v55\n\tv_exp_f32 v56, v56\n\t"
        "v_exp_f32 v57, v57\n\ts_waitcnt vmcnt(14)\n\tv_smfmac_f32_32x32x32_bf16 %[o0], v[96:99], v[48:55], v128\n\tv_exp_f32 v58, v58\n\tv_exp_f32 v59, v59\n\tv_exp_f32 v60, v60\n\t"
        "v_exp_f32 v61, v61\n\tv_exp_f32 v62, v62\n\tv_exp_f32 v63, v63\n\tv_exp_f32 v64, v64\n\tv_exp_f32 v65, v65\n\tv_smfmac_f32_32x32x32_bf16 %[o0], v[112:115], v[56:63], v128\n\t"
        "v_exp_f32 v66, v66\n\tv_exp_f32 v67, v67\n\tv_exp_f32 v68, v68\n\tv_mfma_f32_32x32x16_bf16 v[48:63], v[84:87], %[q0], 0\n\tglobal_load_dwordx4 v[172:175], %[a1], s[46:47]\n\t"
        "v_exp_f32 v69, v69\n\tv_exp_f32 v70, v70\n\tv_exp_f32 v71, v71\n\tv_exp_f32 v72, v72\n\tv_exp_f32 v73, v73\n\tv_smfmac_f32_32x32x32_bf16 %[o1], v[96:99], v[64:71], v128\n\t"
        "v_exp_f32 v74, v74\n\tv_exp_f32 v75, v75\n\tv_exp_f32 v76, v76\n\tv_exp_f32 v77, v77\n\tv_exp_f32 v78, v78\n\tv_exp_f32 v79, v79\n\tv_exp_f32 v48, v48\n\tv_exp_f32 v49, v49\n\t"
        "v_smfmac_f32_32x32x32_bf16 %[o1], v[112:115], v[72:79], v128\n\tv_exp_f32 v50, v50\n\tv_exp_f32 v51, v51\n\tv_exp_f32 v52, v52\n\t"
        "v_mfma_f32_32x32x16_bf16 v[64:79], v[84:87], %[q1], 0\n\tglobal_load_dwordx4 v[84:87], %[koff], s[40:41] offset:1024 sc1\n\ts_add_u32 s42, s42, 4096\n\ts_addc_u32 s43, s43, 0\n\t"
        "s_mov_b64 exec, %[vm]\n\tglobal_load_dwordx4 v[96:99], %[voff], s[42:43] offset:0 sc1\n\tglobal_load_dwordx4 v[112:115], %[voff], s[42:43] offset:512 sc1\n\ts_mov_b64 exec, -1\n\t"
        "v_exp_f32 v53, v53\n\tv_exp_f32 v54, v54\n\tv_exp_f32 v55, v55\n\tv_exp_f32 v56, v56\n\tv_exp_f32 v57, v57\n\ts_waitcnt vmcnt(13)\n\t"
        "v_smfmac_f32_32x32x32_bf16 %[o0], v[100:103], v[48:55], v128\n\tv_exp_f32 v58, v58\n\tv_exp_f32 v59, v59\n\tv_exp_f32 v60, v60\n\tv_exp_f32 v61, v61\n\tv_exp_f32 v62, v62\n\t"
        "v_exp_f32 v63, v63\n\tv_exp_f32 v64, v64\n\tv_exp_f32 v65, v65\n\tv_smfmac_f32_32x32x32_bf16 %[o0], v[116:119], v[56:63], v128\n\tv_exp_f32 v66, v66\n\tv_exp_f32 v67, v67\n\t"
        "v_exp_f32 v68, v68\n\tv_mfma_f32_32x32x16_bf16 v[48:63], v[88:91], %[q0], 0\n\tglobal_load_dwordx4 v[176:179], %[a2], s[46:47]\n\ts_add_u32 s46, s46, %[ga]\n\t"
        "s_addc_u32 s47, s47, 0\n\ts_add_u32 m0, %[ol], 4096\n\ts_add_u32 s50, s50, 8192\n\ts_addc_u32 s51, s51, 0\n\tglobal_load_lds_dwordx4 %[vw], s[50:51]\n\tv_exp_f32 v69, v69\n\t"
        "v_exp_f32 v70, v70\n\tv_exp_f32 v71, v71\n\tv_exp_f32 v72, v72\n\tv_exp_f32 v73, v73\n\tv_smfmac_f32_32x32x32_bf16 %[o1], v[100:103], v[64:71], v128\n\tv_exp_f32 v74, v74\n\t"
        "v_exp_f32 v75, v75\n\tv_exp_f32 v76, v76\n\tv_exp_f32 v77, v77\n\tv_exp_f32 v78, v78\n\tv_exp_f32 v79, v79\n\tv_exp_f32 v48, v48\n\tv_exp_f32 v49, v49\n\t"
        "v_smfmac_f32_32x32x32_bf16 %[o1], v[116:119], v[72:79], v128\n\tv_exp_f32 v50, v50\n\tv_exp_f32 v51, v51\n\tv_exp_f32 v52, v52\n\t"
        "v_mfma_f32_32x32x16_bf16 v[64:79], v[88:91], %[q1], 0\n\tglobal_load_dwordx4 v[88:91], %[koff], s[40:41] offset:2048 sc1\n\ts_mov_b64 exec, %[vm]\n\t"
        "global_load_dwordx4 v[100:103], %[voff], s[42:43] offset:1024 sc1\n\tglobal_load_dwordx4 v[116:119], %[voff], s[42:43] offset:1536 sc1\n\ts_mov_b64 exec, -1\n\t"
        "v_exp_f32 v53, v53\n\tv_exp_f32 v54, v54\n\tv_exp_f32 v55, v55\n\tv_exp_f32 v56, v56\n\tv_exp_f32 v57, v57\n\ts_waitcnt vmcnt(14)\n\t"
        "v_smfmac_f32_32x32x32_bf16 %[o0], v[104:107], v[48:55], v128\n\tv_exp_f32 v58, v58\n\tv_exp_f32 v59, v59\n\tv_exp_f32 v60, v60\n\tv_exp_f32 v61, v61\n\tv_exp_f32 v62, v62\n\t"
        "v_exp_f32 v63, v63\n\tv_exp_f32 v64, v64\n\tv_exp_f32 v65, v65\n\tv_smfmac_f32_32x32x32_bf16 %[o0], v[120:123], v[56:63], v128\n\tv_exp_f32 v66, v66\n\tv_exp_f32 v67, v67\n\t"
        "v_exp_f32 v68, v68\n\tv_mfma_f32_32x32x16_bf16 v[48:63], v[92:95], %[q0], 0\n\tglobal_load_dwordx4 v[180:183], %[a0], s[46:47]\n\tv_exp_f32 v69, v69\n\tv_exp_f32 v70, v70\n\t"
        "v_exp_f32 v71, v71\n\tv_exp_f32 v72, v72\n\tv_exp_f32 v73, v73\n\tv_smfmac_f32_32x32x32_bf16 %[o1], v[104:107], v[64:71], v128\n\tv_exp_f32 v74, v74\n\tv_exp_f32 v75, v75\n\t"
        "v_exp_f32 v76, v76\n\tv_exp_f32 v77, v77\n\tv_exp_f32 v78, v78\n\tv_exp_f32 v79, v79\n\tv_exp_f32 v48, v48\n\tv_exp_f32 v49, v49\n\t"
        "v_smfmac_f32_32x32x32_bf16 %[o1], v[120:123], v[72:79], v128\n\tv_exp_f32 v50, v50\n\tv_exp_f32 v51, v51\n\tv_exp_f32 v52, v52\n\t"
        "v_mfma_f32_32x32x16_bf16 v[64:79], v[92:95], %[q1], 0\n\tglobal_load_dwordx4 v[92:95], %[koff], s[40:41] offset:3072 sc1\n\ts_mov_b64 exec, %[vm]\n\t"
        "global_load_dwordx4 v[104:107], %[voff], s[42:43] offset:2048 sc1\n\tglobal_load_dwordx4 v[120:123], %[voff], s[42:43] offset:2560 sc1\n\ts_mov_b64 exec, -1\n\t"
        "v_exp_f32 v53, v53\n\tv_exp_f32 v54, v54\n\tv_exp_f32 v55, v55\n\tv_exp_f32 v56, v56\n\tv_exp_f32 v57, v57\n\ts_waitcnt vmcnt(13)\n\t"
        "v_smfmac_f32_32x32x32_bf16 %[o0], v[108:111], v[48:55], v128\n\tv_exp_f32 v58, v58\n\tv_exp_f32 v59, v59\n\tv_exp_f32 v60, v60\n\tv_exp_f32 v61, v61\n\tv_exp_f32 v62, v62\n\t"
        "v_exp_f32 v63, v63\n\tv_exp_f32 v64, v64\n\tv_exp_f32 v65, v65\n\tv_smfmac_f32_32x32x32_bf16 %[o0], v[124:127], v[56:63], v128\n\tv_exp_f32 v66, v66\n\tv_exp_f32 v67, v67\n\t"
        "v_exp_f32 v68, v68\n\tv_mfma_f32_32x32x16_bf16 v[48:63], v[80:83], %[q0], 0\n\tglobal_load_dwordx4 v[184:187], %[a1], s[46:47]\n\ts_add_u32 m0, %[ol], 5120\n\t"
        "s_add_u32 s50, s50, 8192\n\ts_addc_u32 s51, s51, 0\n\tglobal_load_lds_dwordx4 %[vw], s[50:51]\n\tv_exp_f32 v69, v69\n\tv_exp_f32 v70, v70\n\tv_exp_f32 v71, v71\n\t"
        "v_exp_f32 v72, v72\n\tv_exp_f32 v73, v73\n\tv_smfmac_f32_32x32x32_bf16 %[o1], v[108:111], v[64:71], v128\n\tv_exp_f32 v74, v74\n\tv_exp_f32 v75, v75\n\tv_exp_f32 v76, v76\n\t"
        "v_exp_f32 v77, v77\n\tv_exp_f32 v78, v78\n\tv_exp_f32 v79, v79\n\tv_exp_f32 v48, v48\n\tv_exp_f32 v49, v49\n\tv_smfmac_f32_32x32x32_bf16 %[o1], v[124:127], v[72:79], v128\n\t"
        "v_exp_f32 v50, v50\n\tv_exp_f32 v51, v51\n\tv_exp_f32 v52, v52\n\tv_mfma_f32_32x32x16_bf16 v[64:79], v[80:83], %[q1], 0\n\ts_add_u32 s40, s40, 4096\n\ts_addc_u32 s41, s41, 0\n\t"
        "global_load_dwordx4 v[80:83], %[koff], s[40:41] offset:0 sc1\n\ts_mov_b64 exec, %[vm]\n\tglobal_load_dwordx4 v[108:111], %[voff], s[42:43] offset:3072 sc1\n\t"
        "global_load_dwordx4 v[124:127], %[voff], s[42:43] offset:3584 sc1\n\ts_mov_b64 exec, -1\n\tv_exp_f32 v53, v53\n\tv_exp_f32 v54, v54\n\tv_exp_f32 v55, v55\n\tv_exp_f32 v56, v56\n\t"
        "v_exp_f32 v57, v57\n\ts_waitcnt vmcnt(14)\n\tv_smfmac_f32_32x32x32_bf16 %[o0], v[96:99], v[48:55], v128\n\tv_exp_f32 v58, v58\n\tv_exp_f32 v59, v59\n\tv_exp_f32 v60, v60\n\t"
        "v_exp_f32 v61, v61\n\tv_exp_f32 v62, v62\n\tv_exp_f32 v63, v63\n\tv_exp_f32 v64, v64\n\tv_exp_f32 v65, v65\n\tv_smfmac_f32_32x32x32_bf16 %[o0], v[112:115], v[56:63], v128\n\t"
        "v_exp_f32 v66, v66\n\tv_exp_f32 v67, v67\n\tv_exp_f32 v68, v68\n\tv_mfma_f32_32x32x16_bf16 v[48:63], v[84:87], %[q0], 0\n\tglobal_load_dwordx4 v[188:191], %[a2], s[46:47]\n\t"
        "s_add_u32 s46, s46, %[ga]\n\ts_addc_u32 s47, s47, 0\n\tv_exp_f32 v69, v69\n\tv_exp_f32 v70, v70\n\tv_exp_f32 v71, v71\n\tv_exp_f32 v72, v72\n\tv_exp_f32 v73, v73\n\t"
        "v_smfmac_f32_32x32x32_bf16 %[o1], v[96:99], v[64:71], v128\n\tv_exp_f32 v74, v74\n\tv_exp_f32 v75, v75\n\tv_exp_f32 v76, v76\n\tv_exp_f32 v77, v77\n\tv_exp_f32 v78, v78\n\t"
        "v_exp_f32 v79, v79\n\tv_exp_f32 v48, v48\n\tv_exp_f32 v49, v49\n\tv_smfmac_f32_32x32x32_bf16 %[o1], v[112:115], v[72:79], v128\n\tv_exp_f32 v50, v50\n\tv_exp_f32 v51, v51\n\t"
        "v_exp_f32 v52, v52\n\tv_mfma_f32_32x32x16_bf16 v[64:79], v[84:87], %[q1], 0\n\tglobal_load_dwordx4 v[84:87], %[koff], s[40:41] offset:1024 sc1\n\ts_add_u32 s42, s42, 4096\n\t"
        "s_addc_u32 s43, s43, 0\n\ts_mov_b64 exec, %[vm]\n\tglobal_load_dwordx4 v[96:99], %[voff], s[42:43] offset:0 sc1\n\t"
        "global_load_dwordx4 v[112:115], %[voff], s[42:43] offset:512 sc1\n\ts_mov_b64 exec, -1\n\tv_exp_f32 v53, v53\n\tv_exp_f32 v54, v54\n\tv_exp_f32 v55, v55\n\tv_exp_f32 v56, v56\n\t"
        "v_exp_f32 v57, v57\n\ts_waitcnt vmcnt(13)\n\tv_smfmac_f32_32x32x32_bf16 %[o0], v[100:103], v[48:55], v128\n\tv_exp_f32 v58, v58\n\tv_exp_f32 v59, v59\n\tv_exp_f32 v60, v60\n\t"
        "v_exp_f32 v61, v61\n\tv_exp_f32 v62, v62\n\tv_exp_f32 v63, v63\n\tv_exp_f32 v64, v64\n\tv_exp_f32 v65, v65\n\tv_smfmac_f32_32x32x32_bf16 %[o0], v[116:119], v[56:63], v128\n\t"
        "v_exp_f32 v66, v66\n\tv_exp_f32 v67, v67\n\tv_exp_f32 v68, v68\n\tv_mfma_f32_32x32x16_bf16 v[48:63], v[88:91], %[q0], 0\n\tglobal_load_dwordx4 v[192:195], %[vw], s[48:49]\n\t"
        "s_add_u32 s48, s48, 8192\n\ts_addc_u32 s49, s49, 0\n\ts_add_u32 m0, %[ol], 6144\n\ts_add_u32 s50, s50, 8192\n\ts_addc_u32 s51, s51, 0\n\tglobal_load_lds_dwordx4 %[vw], s[50:51]\n\t"
        "v_exp_f32 v69, v69\n\tv_exp_f32 v70, v70\n\tv_exp_f32 v71, v71\n\tv_exp_f32 v72, v72\n\tv_exp_f32 v73, v73\n\tv_smfmac_f32_32x32x32_bf16 %[o1], v[100:103], v[64:71], v128\n\t"
        "v_exp_f32 v74, v74\n\tv_exp_f32 v75, v75\n\tv_exp_f32 v76, v76\n\tv_exp_f32 v77, v77\n\tv_exp_f32 v78, v78\n\tv_exp_f32 v79, v79\n\tv_exp_f32 v48, v48\n\tv_exp_f32 v49, v49\n\t"
        "v_smfmac_f32_32x32x32_bf16 %[o1], v[116:119], v[72:79], v128\n\tv_exp_f32 v50, v50\n\tv_exp_f32 v51, v51\n\tv_exp_f32 v52, v52\n\t"
        "v_mfma_f32_32x32x16_bf16 v[64:79], v[88:91], %[q1], 0\n\tglobal_load_dwordx4 v[88:91], %[koff], s[40:41] offset:2048 sc1\n\ts_mov_b64 exec, %[vm]\n\t"
        "global_load_dwordx4 v[100:103], %[voff], s[42:43] offset:1024 sc1\n\tglobal_load_dwordx4 v[116:119], %[voff], s[42:43] offset:1536 sc1\n\ts_mov_b64 exec, -1\n\t"
        "v_exp_f32 v53, v53\n\tv_exp_f32 v54, v54\n\tv_exp_f32 v55, v55\n\tv_exp_f32 v56, v56\n\tv_exp_f32 v57, v57\n\ts_waitcnt vmcnt(14)\n\t"
        "v_smfmac_f32_32x32x32_bf16 %[o0], v[104:107], v[48:55], v128\n\tv_exp_f32 v58, v58\n\tv_exp_f32 v59, v59\n\tv_exp_f32 v60, v60\n\tv_exp_f32 v61, v61\n\tv_exp_f32 v62, v62\n\t"
        "v_exp_f32 v63, v63\n\tv_exp_f32 v64, v64\n\tv_exp_f32 v65, v65\n\tv_smfmac_f32_32x32x32_bf16 %[o0], v[120:123], v[56:63], v128\n\tv_exp_f32 v66, v66\n\tv_exp_f32 v67, v67\n\t"
        "v_exp_f32 v68, v68\n\tv_mfma_f32_32x32x16_bf16 v[48:63], v[92:95], %[q0], 0\n\tglobal_load_dwordx4 v[196:199], %[vw], s[48:49]\n\ts_add_u32 s48, s48, 8192\n\t"
        "s_addc_u32 s49, s49, 0\n\tv_exp_f32 v69, v69\n\tv_exp_f32 v70, v70\n\tv_exp_f32 v71, v71\n\tv_exp_f32 v72, v72\n\tv_exp_f32 v73, v73\n\t"
        "v_smfmac_f32_32x32x32_bf16 %[o1], v[104:107], v[64:71], v128\n\tv_exp_f32 v74, v74\n\tv_exp_f32 v75, v75\n\tv_exp_f32 v76, v76\n\tv_exp_f32 v77, v77\n\tv_exp_f32 v78, v78\n\t"
        "v_exp_f32 v79, v79\n\tv_exp_f32 v48, v48\n\tv_exp_f32 v49, v49\n\tv_smfmac_f32_32x32x32_bf16 %[o1], v[120:123], v[72:79], v128\n\tv_exp_f32 v50, v50\n\tv_exp_f32 v51, v51\n\t"
        "v_exp_f32 v52, v52\n\tv_mfma_f32_32x32x16_bf16 v[64:79], v[92:95], %[q1], 0\n\tglobal_load_dwordx4 v[92:95], %[koff], s[40:41] offset:3072 sc1\n\ts_mov_b64 exec, %[vm]\n\t"
        "global_load_dwordx4 v[104:107], %[voff], s[42:43] offset:2048 sc1\n\tglobal_load_dwordx4 v[120:123], %[voff], s[42:43] offset:2560 sc1\n\ts_mov_b64 exec, -1\n\t"
        "v_exp_f32 v53, v53\n\tv_exp_f32 v54, v54\n\tv_exp_f32 v55, v55\n\tv_exp_f32 v56, v56\n\tv_exp_f32 v57, v57\n\ts_waitcnt vmcnt(13)\n\t"
        "v_smfmac_f32_32x32x32_bf16 %[o0], v[108:111], v[48:55], v128\n\tv_exp_f32 v58, v58\n\tv_exp_f32 v59, v59\n\tv_exp_f32 v60, v60\n\tv_exp_f32 v61, v61\n\tv_exp_f32 v62, v62\n\t"
        "v_exp_f32 v63, v63\n\tv_exp_f32 v64, v64\n\tv_exp_f32 v65, v65\n\tv_smfmac_f32_32x32x32_bf16 %[o0], v[124:127], v[56:63], v128\n\tv_exp_f32 v66, v66\n\tv_exp_f32 v67, v67\n\t"
        "v_exp_f32 v68, v68\n\tv_mfma_f32_32x32x16_bf16 v[48:63], v[80:83], %[q0], 0\n\tglobal_load_dwordx4 v[200:203], %[vw], s[48:49]\n\ts_add_u32 s48, s48, 8192\n\t"
        "s_addc_u32 s49, s49, 0\n\ts_add_u32 m0, %[ol], 7168\n\ts_add_u32 s50, s50, 8192\n\ts_addc_u32 s51, s51, 0\n\tglobal_load_lds_dwordx4 %[vw], s[50:51]\n\tv_exp_f32 v69, v69\n\t"
        "v_exp_f32 v70, v70\n\tv_exp_f32 v71, v71\n\tv_exp_f32 v72, v72\n\tv_exp_f32 v73, v73\n\tv_smfmac_f32_32x32x32_bf16 %[o1], v[108:111], v[64:71], v128\n\tv_exp_f32 v74, v74\n\t"
        "v_exp_f32 v75, v75\n\tv_exp_f32 v76, v76\n\tv_exp_f32 v77, v77\n\tv_exp_f32 v78, v78\n\tv_exp_f32 v79, v79\n\tv_exp_f32 v48, v48\n\tv_exp_f32 v49, v49\n\t"
        "v_smfmac_f32_32x32x32_bf16 %[o1], v[124:127], v[72:79], v128\n\tv_exp_f32 v50, v50\n\tv_exp_f32 v51, v51\n\tv_exp_f32 v52, v52\n\t"
        "v_mfma_f32_32x32x16_bf16 v[64:79], v[80:83], %[q1], 0\n\ts_add_u32 s40, s40, 4096\n\ts_addc_u32 s41, s41, 0\n\tglobal_load_dwordx4 v[80:83], %[koff], s[40:41] offset:0 sc1\n\t"
        "s_mov_b64 exec, %[vm]\n\tglobal_load_dwordx4 v[108:111], %[voff], s[42:43] offset:3072 sc1\n\tglobal_load_dwordx4 v[124:127], %[voff], s[42:43] offset:3584 sc1\n\t"
        "s_mov_b64 exec, -1\n\tv_exp_f32 v53, v53\n\tv_exp_f32 v54, v54\n\tv_exp_f32 v55, v55\n\tv_exp_f32 v56, v56\n\tv_exp_f32 v57, v57\n\ts_waitcnt vmcnt(14)\n\t"
        "v_smfmac_f32_32x32x32_bf16 %[o0], v[96:99], v[48:55], v128\n\tv_exp_f32 v58, v58\n\tv_exp_f32 v59, v59\n\tv_exp_f32 v60, v60\n\tv_exp_f32 v61, v61\n\tv_exp_f32 v62, v62\n\t"
        "v_exp_f32 v63, v63\n\tv_exp_f32 v64, v64\n\tv_exp_f32 v65, v65\n\tv_smfmac_f32_32x32x32_bf16 %[o0], v[112:115], v[56:63], v128\n\tv_exp_f32 v66, v66\n\tv_exp_f32 v67, v67\n\t"
        "v_exp_f32 v68, v68\n\tv_mfma_f32_32x32x16_bf16 v[48:63], v[84:87], %[q0], 0\n\tglobal_load_dwordx4 v[204:207], %[vw], s[48:49]\n\ts_add_u32 s48, s48, 8192\n\t"
        "s_addc_u32 s49, s49, 0\n\tv_exp_f32 v69, v69\n\tv_exp_f32 v70, v70\n\tv_exp_f32 v71, v71\n\tv_exp_f32 v72, v72\n\tv_exp_f32 v73, v73\n\t"
        "v_smfmac_f32_32x32x32_bf16 %[o1], v[96:99], v[64:71], v128\n\tv_exp_f32 v74, v74\n\tv_exp_f32 v75, v75\n\tv_exp_f32 v76, v76\n\tv_exp_f32 v77, v77\n\tv_exp_f32 v78, v78\n\t"
        "v_exp_f32 v79, v79\n\tv_exp_f32 v48, v48\n\tv_exp_f32 v49, v49\n\tv_smfmac_f32_32x32x32_bf16 %[o1], v[112:115], v[72:79], v128\n\tv_exp_f32 v50, v50\n\tv_exp_f32 v51, v51\n\t"
        "v_exp_f32 v52, v52\n\tv_mfma_f32_32x32x16_bf16 v[64:79], v[84:87], %[q1], 0\n\tglobal_load_dwordx4 v[84:87], %[koff], s[40:41] offset:1024 sc1\n\ts_add_u32 s42, s42, 4096\n\t"
        "s_addc_u32 s43, s43, 0\n\ts_mov_b64 exec, %[vm]\n\tglobal_load_dwordx4 v[96:99], %[voff], s[42:43] offset:0 sc1\n\t"
        "global_load_dwordx4 v[112:115], %[voff], s[42:43] offset:512 sc1\n\ts_mov_b64 exec, -1\n\tv_exp_f32 v53, v53\n\tv_exp_f32 v54, v54\n\tv_exp_f32 v55, v55\n\tv_exp_f32 v56, v56\n\t"
        "v_exp_f32 v57, v57\n\ts_waitcnt vmcnt(13)\n\tv_smfmac_f32_32x32x32_bf16 %[o0], v[100:103], v[48:55], v128\n\tv_exp_f32 v58, v58\n\tv_exp_f32 v59, v59\n\tv_exp_f32 v60, v60\n\t"
        "v_exp_f32 v61, v61\n\tv_exp_f32 v62, v62\n\tv_exp_f32 v63, v63\n\tv_exp_f32 v64, v64\n\tv_exp_f32 v65, v65\n\tv_smfmac_f32_32x32x32_bf16 %[o0], v[116:119], v[56:63], v128\n\t"
        "v_exp_f32 v66, v66\n\tv_exp_f32 v67, v67\n\tv_exp_f32 v68, v68\n\tv_mfma_f32_32x32x16_bf16 v[48:63], v[88:91], %[q0], 0\n\tglobal_load_dwordx4 v[208:211], %[vw], s[48:49]\n\t"
        "s_add_u32 s48, s48, 8192\n\ts_addc_u32 s49, s49, 0\n\ts_add_u32 m0, %[hl], 0\n\ts_nop 0\n\tglobal_load_lds_dwordx4 %[b0], s[52:53]\n\tv_exp_f32 v69, v69\n\tv_exp_f32 v70, v70\n\t"
        "v_exp_f32 v71, v71\n\tv_exp_f32 v72, v72\n\tv_exp_f32 v73, v73\n\tv_smfmac_f32_32x32x32_bf16 %[o1], v[100:103], v[64:71], v128\n\tv_exp_f32 v74, v74\n\tv_exp_f32 v75, v75\n\t"
        "v_exp_f32 v76, v76\n\tv_exp_f32 v77, v77\n\tv_exp_f32 v78, v78\n\tv_exp_f32 v79, v79\n\tv_exp_f32 v48, v48\n\tv_exp_f32 v49, v49\n\t"
        "v_smfmac_f32_32x32x32_bf16 %[o1], v[116:119], v[72:79], v128\n\tv_exp_f32 v50, v50\n\tv_exp_f32 v51, v51\n\tv_exp_f32 v52, v52\n\t"
        "v_mfma_f32_32x32x16_bf16 v[64:79], v[88:91], %[q1], 0\n\tglobal_load_dwordx4 v[88:91], %[koff], s[40:41] offset:2048 sc1\n\ts_mov_b64 exec, %[vm]\n\t"
        "global_load_dwordx4 v[100:103], %[voff], s[42:43] offset:1024 sc1\n\tglobal_load_dwordx4 v[116:119], %[voff], s[42:43] offset:1536 sc1\n\ts_mov_b64 exec, -1\n\t"
        "v_exp_f32 v53, v53\n\tv_exp_f32 v54, v54\n\tv_exp_f32 v55, v55\n\tv_exp_f32 v56, v56\n\tv_exp_f32 v57, v57\n\ts_waitcnt vmcnt(14)\n\t"
        "v_smfmac_f32_32x32x32_bf16 %[o0], v[104:107], v[48:55], v128\n\tv_exp_f32 v58, v58\n\tv_exp_f32 v59, v59\n\tv_exp_f32 v60, v60\n\tv_exp_f32 v61, v61\n\tv_exp_f32 v62, v62\n\t"
        "v_exp_f32 v63, v63\n\tv_exp_f32 v64, v64\n\tv_exp_f32 v65, v65\n\tv_smfmac_f32_32x32x32_bf16 %[o0], v[120:123], v[56:63], v128\n\tv_exp_f32 v66, v66\n\tv_exp_f32 v67, v67\n\t"
        "v_exp_f32 v68, v68\n\tv_mfma_f32_32x32x16_bf16 v[48:63], v[92:95], %[q0], 0\n\tglobal_load_dwordx4 v[212:215], %[vw], s[48:49]\n\ts_add_u32 s48, s48, 8192\n\t"
        "s_addc_u32 s49, s49, 0\n\tv_exp_f32 v69, v69\n\tv_exp_f32 v70, v70\n\tv_exp_f32 v71, v71\n\tv_exp_f32 v72, v72\n\tv_exp_f32 v73, v73\n\t"
        "v_smfmac_f32_32x32x32_bf16 %[o1], v[104:107], v[64:71], v128\n\tv_exp_f32 v74, v74\n\tv_exp_f32 v75, v75\n\tv_exp_f32 v76, v76\n\tv_exp_f32 v77, v77\n\tv_exp_f32 v78, v78\n\t"
        "v_exp_f32 v79, v79\n\tv_exp_f32 v48, v48\n\tv_exp_f32 v49, v49\n\tv_smfmac_f32_32x32x32_bf16 %[o1], v[120:123], v[72:79], v128\n\tv_exp_f32 v50, v50\n\tv_exp_f32 v51, v51\n\t"
        "v_exp_f32 v52, v52\n\tv_mfma_f32_32x32x16_bf16 v[64:79], v[92:95], %[q1], 0\n\tglobal_load_dwordx4 v[92:95], %[koff], s[40:41] offset:3072 sc1\n\ts_mov_b64 exec, %[vm]\n\t"
        "global_load_dwordx4 v[104:107], %[voff], s[42:43] offset:2048 sc1\n\tglobal_load_dwordx4 v[120:123], %[voff], s[42:43] offset:2560 sc1\n\ts_mov_b64 exec, -1\n\t"
        "v_exp_f32 v53, v53\n\tv_exp_f32 v54, v54\n\tv_exp_f32 v55, v55\n\tv_exp_f32 v56, v56\n\tv_exp_f32 v57, v57\n\ts_waitcnt vmcnt(13)\n\t"
        "v_smfmac_f32_32x32x32_bf16 %[o0], v[108:111], v[48:55], v128\n\tv_exp_f32 v58, v58\n\tv_exp_f32 v59, v59\n\tv_exp_f32 v60, v60\n\tv_exp_f32 v61, v61\n\tv_exp_f32 v62, v62\n\t"
        "v_exp_f32 v63, v63\n\tv_exp_f32 v64, v64\n\tv_exp_f32 v65, v65\n\tv_smfmac_f32_32x32x32_bf16 %[o0], v[124:127], v[56:63], v128\n\tv_exp_f32 v66, v66\n\tv_exp_f32 v67, v67\n\t"
        "v_exp_f32 v68, v68\n\tv_mfma_f32_32x32x16_bf16 v[48:63], v[80:83], %[q0], 0\n\tglobal_load_dwordx4 v[216:219], %[vw], s[48:49]\n\ts_add_u32 s48, s48, 8192\n\t"
        "s_addc_u32 s49, s49, 0\n\ts_add_u32 m0, %[hl], 1024\n\ts_nop 0\n\tglobal_load_lds_dwordx4 %[b1], s[52:53]\n\tv_exp_f32 v69, v69\n\tv_exp_f32 v70, v70\n\tv_exp_f32 v71, v71\n\t"
        "v_exp_f32 v72, v72\n\tv_exp_f32 v73, v73\n\tv_smfmac_f32_32x32x32_bf16 %[o1], v[108:111], v[64:71], v128\n\tv_exp_f32 v74, v74\n\tv_exp_f32 v75, v75\n\tv_exp_f32 v76, v76\n\t"
        "v_exp_f32 v77, v77\n\tv_exp_f32 v78, v78\n\tv_exp_f32 v79, v79\n\tv_exp_f32 v48, v48\n\tv_exp_f32 v49, v49\n\tv_smfmac_f32_32x32x32_bf16 %[o1], v[124:127], v[72:79], v128\n\t"
        "v_exp_f32 v50, v50\n\tv_exp_f32 v51, v51\n\tv_exp_f32 v52, v52\n\tv_mfma_f32_32x32x16_bf16 v[64:79], v[80:83], %[q1], 0\n\ts_add_u32 s40, s40, 4096\n\ts_addc_u32 s41, s41, 0\n\t"
        "global_load_dwordx4 v[80:83], %[koff], s[40:41] offset:0 sc1\n\ts_mov_b64 exec, %[vm]\n\tglobal_load_dwordx4 v[108:111], %[voff], s[42:43] offset:3072 sc1\n\t"
        "global_load_dwordx4 v[124:127], %[voff], s[42:43] offset:3584 sc1\n\ts_mov_b64 exec, -1\n\tv_exp_f32 v53, v53\n\tv_exp_f32 v54, v54\n\tv_exp_f32 v55, v55\n\tv_exp_f32 v56, v56\n\t"
        "v_exp_f32 v57, v57\n\ts_waitcnt vmcnt(14)\n\tv_smfmac_f32_32x32x32_bf16 %[o0], v[96:99], v[48:55], v128\n\tv_exp_f32 v58, v58\n\tv_exp_f32 v59, v59\n\tv_exp_f32 v60, v60\n\t"
        "v_exp_f32 v61, v61\n\tv_exp_f32 v62, v62\n\tv_exp_f32 v63, v63\n\tv_exp_f32 v64, v64\n\tv_exp_f32 v65, v65\n\tv_smfmac_f32_32x32x32_bf16 %[o0], v[112:115], v[56:63], v128\n\t"
        "v_exp_f32 v66, v66\n\tv_exp_f32 v67, v67\n\tv_exp_f32 v68, v68\n\tv_mfma_f32_32x32x16_bf16 v[48:63], v[84:87], %[q0], 0\n\tglobal_load_dwordx4 v[220:223], %[vw], s[48:49]\n\t"
        "s_add_u32 s48, s48, 8192\n\ts_addc_u32 s49, s49, 0\n\tv_exp_f32 v69, v69\n\tv_exp_f32 v70, v70\n\tv_exp_f32 v71, v71\n\tv_exp_f32 v72, v72\n\tv_exp_f32 v73, v73\n\t"
        "v_smfmac_f32_32x32x32_bf16 %[o1], v[96:99], v[64:71], v128\n\tv_exp_f32 v74, v74\n\tv_exp_f32 v75, v75\n\tv_exp_f32 v76, v76\n\tv_exp_f32 v77, v77\n\tv_exp_f32 v78, v78\n\t"
        "v_exp_f32 v79, v79\n\tv_exp_f32 v48, v48\n\tv_exp_f32 v49, v49\n\tv_smfmac_f32_32x32x32_bf16 %[o1], v[112:115], v[72:79], v128\n\tv_exp_f32 v50, v50\n\tv_exp_f32 v51, v51\n\t"
        "v_exp_f32 v52, v52\n\tv_mfma_f32_32x32x16_bf16 v[64:79], v[84:87], %[q1], 0\n\tglobal_load_dwordx4 v[84:87], %[koff], s[40:41] offset:1024 sc1\n\ts_add_u32 s42, s42, 4096\n\t"
        "s_addc_u32 s43, s43, 0\n\ts_mov_b64 exec, %[vm]\n\tglobal_load_dwordx4 v[96:99], %[voff], s[42:43] offset:0 sc1\n\t"
        "global_load_dwordx4 v[112:115], %[voff], s[42:43] offset:512 sc1\n\ts_mov_b64 exec, -1\n\tv_exp_f32 v53, v53\n\tv_exp_f32 v54, v54\n\tv_exp_f32 v55, v55\n\tv_exp_f32 v56, v56\n\t"
        "v_exp_f32 v57, v57\n\ts_waitcnt vmcnt(13)\n\tv_smfmac_f32_32x32x32_bf16 %[o0], v[100:103], v[48:55], v128\n\tv_exp_f32 v58, v58\n\tv_exp_f32 v59, v59\n\tv_exp_f32 v60, v60\n\t"
        "v_exp_f32 v61, v61\n\tv_exp_f32 v62, v62\n\tv_exp_f32 v63, v63\n\tv_exp_f32 v64, v64\n\tv_exp_f32 v65, v65\n\tv_smfmac_f32_32x32x32_bf16 %[o0], v[116:119], v[56:63], v128\n\t"
        "v_exp_f32 v66, v66\n\tv_exp_f32 v67, v67\n\tv_exp_f32 v68, v68\n\tv_mfma_f32_32x32x16_bf16 v[48:63], v[88:91], %[q0], 0\n\tglobal_load_dwordx4 v[224:227], %[vw], s[48:49]\n\t"
        "s_add_u32 s48, s48, 8192\n\ts_addc_u32 s49, s49, 0\n\ts_add_u32 m0, %[hl], 2048\n\ts_nop 0\n\tglobal_load_lds_dwordx4 %[b2], s[52:53]\n\tv_exp_f32 v69, v69\n\t"
        "v_exp_f32 v70, v70\n\tv_exp_f32 v71, v71\n\tv_exp_f32 v72, v72\n\tv_exp_f32 v73, v73\n\tv_smfmac_f32_32x32x32_bf16 %[o1], v[100:103], v[64:71], v128\n\tv_exp_f32 v74, v74\n\t"
        "v_exp_f32 v75, v75\n\tv_exp_f32 v76, v76\n\tv_exp_f32 v77, v77\n\tv_exp_f32 v78, v78\n\tv_exp_f32 v79, v79\n\tv_exp_f32 v48, v48\n\tv_exp_f32 v49, v49\n\t"
        "v_smfmac_f32_32x32x32_bf16 %[o1], v[116:119], v[72:79], v128\n\tv_exp_f32 v50, v50\n\tv_exp_f32 v51, v51\n\tv_exp_f32 v52, v52\n\t"
        "v_mfma_f32_32x32x16_bf16 v[64:79], v[88:91], %[q1], 0\n\tglobal_load_dwordx4 v[88:91], %[koff], s[40:41] offset:2048 sc1\n\ts_mov_b64 exec, %[vm]\n\t"
        "global_load_dwordx4 v[100:103], %[voff], s[42:43] offset:1024 sc1\n\tglobal_load_dwordx4 v[116:119], %[voff], s[42:43] offset:1536 sc1\n\ts_mov_b64 exec, -1\n\t"
        "v_exp_f32 v53, v53\n\tv_exp_f32 v54, v54\n\tv_exp_f32 v55, v55\n\tv_exp_f32 v56, v56\n\tv_exp_f32 v57, v57\n\ts_waitcnt vmcnt(14)\n\t"
        "v_smfmac_f32_32x32x32_bf16 %[o0], v[104:107], v[48:55], v128\n\tv_exp_f32 v58, v58\n\tv_exp_f32 v59, v59\n\tv_exp_f32 v60, v60\n\tv_exp_f32 v61, v61\n\tv_exp_f32 v62, v62\n\t"
        "v_exp_f32 v63, v63\n\tv_exp_f32 v64, v64\n\tv_exp_f32 v65, v65\n\tv_smfmac_f32_32x32x32_bf16 %[o0], v[120:123], v[56:63], v128\n\tv_exp_f32 v66, v66\n\tv_exp_f32 v67, v67\n\t"
        "v_exp_f32 v68, v68\n\tv_mfma_f32_32x32x16_bf16 v[48:63], v[92:95], %[q0], 0\n\tglobal_load_dwordx4 v[228:231], %[vw], s[48:49]\n\ts_add_u32 s48, s48, 8192\n\t"
        "s_addc_u32 s49, s49, 0\n\tv_exp_f32 v69, v69\n\tv_exp_f32 v70, v70\n\tv_exp_f32 v71, v71\n\tv_exp_f32 v72, v72\n\tv_exp_f32 v73, v73\n\t"
        "v_smfmac_f32_32x32x32_bf16 %[o1], v[104:107], v[64:71], v128\n\tv_exp_f32 v74, v74\n\tv_exp_f32 v75, v75\n\tv_exp_f32 v76, v76\n\tv_exp_f32 v77, v77\n\tv_exp_f32 v78, v78\n\t"
        "v_exp_f32 v79, v79\n\tv_exp_f32 v48, v48\n\tv_exp_f32 v49, v49\n\tv_smfmac_f32_32x32x32_bf16 %[o1], v[120:123], v[72:79], v128\n\tv_exp_f32 v50, v50\n\tv_exp_f32 v51, v51\n\t"
        "v_exp_f32 v52, v52\n\tv_mfma_f32_32x32x16_bf16 v[64:79], v[92:95], %[q1], 0\n\tglobal_load_dwordx4 v[92:95], %[koff], s[40:41] offset:3072 sc1\n\ts_mov_b64 exec, %[vm]\n\t"
        "global_load_dwordx4 v[104:107], %[voff], s[42:43] offset:2048 sc1\n\tglobal_load_dwordx4 v[120:123], %[voff], s[42:43] offset:2560 sc1\n\ts_mov_b64 exec, -1\n\t"
        "v_exp_f32 v53, v53\n\tv_exp_f32 v54, v54\n\tv_exp_f32 v55, v55\n\tv_exp_f32 v56, v56\n\tv_exp_f32 v57, v57\n\ts_waitcnt vmcnt(13)\n\t"
        "v_smfmac_f32_32x32x32_bf16 %[o0], v[108:111], v[48:55], v128\n\tv_exp_f32 v58, v58\n\tv_exp_f32 v59, v59\n\tv_exp_f32 v60, v60\n\tv_exp_f32 v61, v61\n\tv_exp_f32 v62, v62\n\t"
        "v_exp_f32 v63, v63\n\tv_exp_f32 v64, v64\n\tv_exp_f32 v65, v65\n\tv_smfmac_f32_32x32x32_bf16 %[o0], v[124:127], v[56:63], v128\n\tv_exp_f32 v66, v66\n\tv_exp_f32 v67, v67\n\t"
        "v_exp_f32 v68, v68\n\tv_mfma_f32_32x32x16_bf16 v[48:63], v[80:83], %[q0], 0\n\tglobal_load_dwordx4 v[232:235], %[vw], s[48:49]\n\ts_add_u32 s48, s48, 8192\n\t"
        "s_addc_u32 s49, s49, 0\n\ts_add_u32 m0, %[hl], 3072\n\ts_add_u32 s52, s52, 44032\n\ts_addc_u32 s53, s53, 0\n\tglobal_load_lds_dwordx4 %[b0], s[52:53]\n\tv_exp_f32 v69, v69\n\t"
        "v_exp_f32 v70, v70\n\tv_exp_f32 v71, v71\n\tv_exp_f32 v72, v72\n\tv_exp_f32 v73, v73\n\tv_smfmac_f32_32x32x32_bf16 %[o1], v[108:111], v[64:71], v128\n\tv_exp_f32 v74, v74\n\t"
        "v_exp_f32 v75, v75\n\tv_exp_f32 v76, v76\n\tv_exp_f32 v77, v77\n\tv_exp_f32 v78, v78\n\tv_exp_f32 v79, v79\n\tv_exp_f32 v48, v48\n\tv_exp_f32 v49, v49\n\t"
        "v_smfmac_f32_32x32x32_bf16 %[o1], v[124:127], v[72:79], v128\n\tv_exp_f32 v50, v50\n\tv_exp_f32 v51, v51\n\tv_exp_f32 v52, v52\n\t"
        "v_mfma_f32_32x32x16_bf16 v[64:79], v[80:83], %[q1], 0\n\ts_add_u32 s40, s40, 4096\n\ts_addc_u32 s41, s41, 0\n\tglobal_load_dwordx4 v[80:83], %[koff], s[40:41] offset:0 sc1\n\t"
        "s_mov_b64 exec, %[vm]\n\tglobal_load_dwordx4 v[108:111], %[voff], s[42:43] offset:3072 sc1\n\tglobal_load_dwordx4 v[124:127], %[voff], s[42:43] offset:3584 sc1\n\t"
        "s_mov_b64 exec, -1\n\tv_exp_f32 v53, v53\n\tv_exp_f32 v54, v54\n\tv_exp_f32 v55, v55\n\tv_exp_f32 v56, v56\n\tv_exp_f32 v57, v57\n\ts_waitcnt vmcnt(14)\n\t"
        "v_smfmac_f32_32x32x32_bf16 %[o0], v[96:99], v[48:55], v128\n\tv_exp_f32 v58, v58\n\tv_exp_f32 v59, v59\n\tv_exp_f32 v60, v60\n\tv_exp_f32 v61, v61\n\tv_exp_f32 v62, v62\n\t"
        "v_exp_f32 v63, v63\n\tv_exp_f32 v64, v64\n\tv_exp_f32 v65, v65\n\tv_smfmac_f32_32x32x32_bf16 %[o0], v[112:115], v[56:63], v128\n\tv_exp_f32 v66, v66\n\tv_exp_f32 v67, v67\n\t"
        "v_exp_f32 v68, v68\n\tv_mfma_f32_32x32x16_bf16 v[48:63], v[84:87], %[q0], 0\n\tglobal_load_dwordx4 v[236:239], %[vw], s[48:49]\n\ts_add_u32 s48, s48, 8192\n\t"
        "s_addc_u32 s49, s49, 0\n\tv_exp_f32 v69, v69\n\tv_exp_f32 v70, v70\n\tv_exp_f32 v71, v71\n\tv_exp_f32 v72, v72\n\tv_exp_f32 v73, v73\n\t"
        "v_smfmac_f32_32x32x32_bf16 %[o1], v[96:99], v[64:71], v128\n\tv_exp_f32 v74, v74\n\tv_exp_f32 v75, v75\n\tv_exp_f32 v76, v76\n\tv_exp_f32 v77, v77\n\tv_exp_f32 v78, v78\n\t"
        "v_exp_f32 v79, v79\n\tv_exp_f32 v48, v48\n\tv_exp_f32 v49, v49\n\tv_smfmac_f32_32x32x32_bf16 %[o1], v[112:115], v[72:79], v128\n\tv_exp_f32 v50, v50\n\tv_exp_f32 v51, v51\n\t"
        "v_exp_f32 v52, v52\n\tv_mfma_f32_32x32x16_bf16 v[64:79], v[84:87], %[q1], 0\n\tglobal_load_dwordx4 v[84:87], %[koff], s[40:41] offset:1024 sc1\n\ts_add_u32 s42, s42, 4096\n\t"
        "s_addc_u32 s43, s43, 0\n\ts_mov_b64 exec, %[vm]\n\tglobal_load_dwordx4 v[96:99], %[voff], s[42:43] offset:0 sc1\n\t"
        "global_load_dwordx4 v[112:115], %[voff], s[42:43] offset:512 sc1\n\ts_mov_b64 exec, -1\n\tv_exp_f32 v53, v53\n\tv_exp_f32 v54, v54\n\tv_exp_f32 v55, v55\n\tv_exp_f32 v56, v56\n\t"
        "v_exp_f32 v57, v57\n\ts_waitcnt vmcnt(13)\n\tv_smfmac_f32_32x32x32_bf16 %[o0], v[100:103], v[48:55], v128\n\tv_exp_f32 v58, v58\n\tv_exp_f32 v59, v59\n\tv_exp_f32 v60, v60\n\t"
        "v_exp_f32 v61, v61\n\tv_exp_f32 v62, v62\n\tv_exp_f32 v63, v63\n\tv_exp_f32 v64, v64\n\tv_exp_f32 v65, v65\n\tv_smfmac_f32_32x32x32_bf16 %[o0], v[116:119], v[56:63], v128\n\t"
        "v_exp_f32 v66, v66\n\tv_exp_f32 v67, v67\n\tv_exp_f32 v68, v68\n\tv_mfma_f32_32x32x16_bf16 v[48:63], v[88:91], %[q0], 0\n\tglobal_load_dwordx4 v[240:243], %[vw], s[48:49]\n\t"
        "s_add_u32 s48, s48, 8192\n\ts_addc_u32 s49, s49, 0\n\ts_add_u32 m0, %[hl], 4096\n\ts_nop 0\n\tglobal_load_lds_dwordx4 %[b1], s[52:53]\n\tv_exp_f32 v69, v69\n\t"
        "v_exp_f32 v70, v70\n\tv_exp_f32 v71, v71\n\tv_exp_f32 v72, v72\n\tv_exp_f32 v73, v73\n\tv_smfmac_f32_32x32x32_bf16 %[o1], v[100:103], v[64:71], v128\n\tv_exp_f32 v74, v74\n\t"
        "v_exp_f32 v75, v75\n\tv_exp_f32 v76, v76\n\tv_exp_f32 v77, v77\n\tv_exp_f32 v78, v78\n\tv_exp_f32 v79, v79\n\tv_exp_f32 v48, v48\n\tv_exp_f32 v49, v49\n\t"
        "v_smfmac_f32_32x32x32_bf16 %[o1], v[116:119], v[72:79], v128\n\tv_exp_f32 v50, v50\n\tv_exp_f32 v51, v51\n\tv_exp_f32 v52, v52\n\t"
        "v_mfma_f32_32x32x16_bf16 v[64:79], v[88:91], %[q1], 0\n\tglobal_load_dwordx4 v[88:91], %[koff], s[40:41] offset:2048 sc1\n\ts_mov_b64 exec, %[vm]\n\t"
        "global_load_dwordx4 v[100:103], %[voff], s[42:43] offset:1024 sc1\n\tglobal_load_dwordx4 v[116:119], %[voff], s[42:43] offset:1536 sc1\n\ts_mov_b64 exec, -1\n\t"
        "v_exp_f32 v53, v53\n\tv_exp_f32 v54, v54\n\tv_exp_f32 v55, v55\n\tv_exp_f32 v56, v56\n\tv_exp_f32 v57, v57\n\ts_waitcnt vmcnt(14)\n\t"
        "v_smfmac_f32_32x32x32_bf16 %[o0], v[104:107], v[48:55], v128\n\tv_exp_f32 v58, v58\n\tv_exp_f32 v59, v59\n\tv_exp_f32 v60, v60\n\tv_exp_f32 v61, v61\n\tv_exp_f32 v62, v62\n\t"
        "v_exp_f32 v63, v63\n\tv_exp_f32 v64, v64\n\tv_exp_f32 v65, v65\n\tv_smfmac_f32_32x32x32_bf16 %[o0], v[120:123], v[56:63], v128\n\tv_exp_f32 v66, v66\n\tv_exp_f32 v67, v67\n\t"
        "v_exp_f32 v68, v68\n\tv_mfma_f32_32x32x16_bf16 v[48:63], v[92:95], %[q0], 0\n\tglobal_load_dwordx4 v[244:247], %[vw], s[48:49]\n\ts_add_u32 s48, s48, 8192\n\t"
        "s_addc_u32 s49, s49, 0\n\tv_exp_f32 v69, v69\n\tv_exp_f32 v70, v70\n\tv_exp_f32 v71, v71\n\tv_exp_f32 v72, v72\n\tv_exp_f32 v73, v73\n\t"
        "v_smfmac_f32_32x32x32_bf16 %[o1], v[104:107], v[64:71], v128\n\tv_exp_f32 v74, v74\n\tv_exp_f32 v75, v75\n\tv_exp_f32 v76, v76\n\tv_exp_f32 v77, v77\n\tv_exp_f32 v78, v78\n\t"
        "v_exp_f32 v79, v79\n\tv_exp_f32 v48, v48\n\tv_exp_f32 v49, v49\n\tv_smfmac_f32_32x32x32_bf16 %[o1], v[120:123], v[72:79], v128\n\tv_exp_f32 v50, v50\n\tv_exp_f32 v51, v51\n\t"
        "v_exp_f32 v52, v52\n\tv_mfma_f32_32x32x16_bf16 v[64:79], v[92:95], %[q1], 0\n\tglobal_load_dwordx4 v[92:95], %[koff], s[40:41] offset:3072 sc1\n\ts_mov_b64 exec, %[vm]\n\t"
        "global_load_dwordx4 v[104:107], %[voff], s[42:43] offset:2048 sc1\n\tglobal_load_dwordx4 v[120:123], %[voff], s[42:43] offset:2560 sc1\n\ts_mov_b64 exec, -1\n\t"
        "v_exp_f32 v53, v53\n\tv_exp_f32 v54, v54\n\tv_exp_f32 v55, v55\n\tv_exp_f32 v56, v56\n\tv_exp_f32 v57, v57\n\ts_waitcnt vmcnt(13)\n\t"
        "v_smfmac_f32_32x32x32_bf16 %[o0], v[108:111], v[48:55], v128\n\tv_exp_f32 v58, v58\n\tv_exp_f32 v59, v59\n\tv_exp_f32 v60, v60\n\tv_exp_f32 v61, v61\n\tv_exp_f32 v62, v62\n\t"
        "v_exp_f32 v63, v63\n\tv_exp_f32 v64, v64\n\tv_exp_f32 v65, v65\n\tv_smfmac_f32_32x32x32_bf16 %[o0], v[124:127], v[56:63], v128\n\tv_exp_f32 v66, v66\n\tv_exp_f32 v67, v67\n\t"
        "v_exp_f32 v68, v68\n\tv_mfma_f32_32x32x16_bf16 v[48:63], v[80:83], %[q0], 0\n\tglobal_load_dwordx4 v[248:251], %[vw], s[48:49]\n\ts_add_u32 s48, s48, 8192\n\t"
        "s_addc_u32 s49, s49, 0\n\ts_add_u32 m0, %[hl], 5120\n\ts_nop 0\n\tglobal_load_lds_dwordx4 %[b2], s[52:53]\n\ts_cmp_eq_u32 %[drop], 0\n\ts_cbranch_scc1 2f\n\ts_setprio 0\n\t2:\n\t"
        "v_exp_f32 v69, v69\n\tv_exp_f32 v70, v70\n\tv_exp_f32 v71, v71\n\tv_exp_f32 v72, v72\n\tv_exp_f32 v73, v73\n\tv_smfmac_f32_32x32x32_bf16 %[o1], v[108:111], v[64:71], v128\n\t"
        "v_exp_f32 v74, v74\n\tv_exp_f32 v75, v75\n\tv_exp_f32 v76, v76\n\tv_exp_f32 v77, v77\n\tv_exp_f32 v78, v78\n\tv_exp_f32 v79, v79\n\tv_exp_f32 v48, v48\n\tv_exp_f32 v49, v49\n\t"
        "v_smfmac_f32_32x32x32_bf16 %[o1], v[124:127], v[72:79], v128\n\tv_exp_f32 v50, v50\n\tv_exp_f32 v51, v51\n\tv_exp_f32 v52, v52\n\t"
        "v_mfma_f32_32x32x16_bf16 v[64:79], v[80:83], %[q1], 0\n\ts_mov_b64 exec, %[vm]\n\tglobal_load_dwordx4 v[108:111], %[voff], s[42:43] offset:3072 sc1\n\t"
        "global_load_dwordx4 v[124:127], %[voff], s[42:43] offset:3584 sc1\n\ts_mov_b64 exec, -1\n\tv_exp_f32 v53, v53\n\tv_exp_f32 v54, v54\n\tv_exp_f32 v55, v55\n\tv_exp_f32 v56, v56\n\t"
        "v_exp_f32 v57, v57\n\ts_waitcnt vmcnt(13)\n\tv_smfmac_f32_32x32x32_bf16 %[o0], v[96:99], v[48:55], v128\n\tv_exp_f32 v58, v58\n\tv_exp_f32 v59, v59\n\tv_exp_f32 v60, v60\n\t"
        "v_exp_f32 v61, v61\n\tv_exp_f32 v62, v62\n\tv_exp_f32 v63, v63\n\tv_exp_f32 v64, v64\n\tv_exp_f32 v65, v65\n\tv_smfmac_f32_32x32x32_bf16 %[o0], v[112:115], v[56:63], v128\n\t"
        "v_exp_f32 v66, v66\n\tv_exp_f32 v67, v67\n\tv_exp_f32 v68, v68\n\tv_mfma_f32_32x32x16_bf16 v[48:63], v[84:87], %[q0], 0\n\tglobal_load_dwordx4 v[252:255], %[vw], s[48:49]\n\t"
        "s_add_u32 s48, s48, 8192\n\ts_addc_u32 s49, s49, 0\n\tv_exp_f32 v69, v69\n\tv_exp_f32 v70, v70\n\tv_exp_f32 v71, v71\n\tv_exp_f32 v72, v72\n\tv_exp_f32 v73, v73\n\t"
        "v_smfmac_f32_32x32x32_bf16 %[o1], v[96:99], v[64:71], v128\n\tv_exp_f32 v74, v74\n\tv_exp_f32 v75, v75\n\tv_exp_f32 v76, v76\n\tv_exp_f32 v77, v77\n\tv_exp_f32 v78, v78\n\t"
        "v_exp_f32 v79, v79\n\tv_exp_f32 v48, v48\n\tv_exp_f32 v49, v49\n\tv_smfmac_f32_32x32x32_bf16 %[o1], v[112:115], v[72:79], v128\n\tv_exp_f32 v50, v50\n\tv_exp_f32 v51, v51\n\t"
        "v_exp_f32 v52, v52\n\tv_mfma_f32_32x32x16_bf16 v[64:79], v[84:87], %[q1], 0\n\tv_exp_f32 v53, v53\n\tv_exp_f32 v54, v54\n\tv_exp_f32 v55, v55\n\tv_exp_f32 v56, v56\n\t"
        "v_exp_f32 v57, v57\n\ts_waitcnt vmcnt(9)\n\tv_smfmac_f32_32x32x32_bf16 %[o0], v[100:103], v[48:55], v128\n\tv_exp_f32 v58, v58\n\tv_exp_f32 v59, v59\n\tv_exp_f32 v60, v60\n\t"
        "v_exp_f32 v61, v61\n\tv_exp_f32 v62, v62\n\tv_exp_f32 v63, v63\n\tv_exp_f32 v64, v64\n\tv_exp_f32 v65, v65\n\tv_smfmac_f32_32x32x32_bf16 %[o0], v[116:119], v[56:63], v128\n\t"
        "v_exp_f32 v66, v66\n\tv_exp_f32 v67, v67\n\tv_exp_f32 v68, v68\n\tv_mfma_f32_32x32x16_bf16 v[48:63], v[88:91], %[q0], 0\n\tv_exp_f32 v69, v69\n\tv_exp_f32 v70, v70\n\t"
        "v_exp_f32 v71, v71\n\tv_exp_f32 v72, v72\n\tv_exp_f32 v73, v73\n\tv_smfmac_f32_32x32x32_bf16 %[o1], v[100:103], v[64:71], v128\n\tv_exp_f32 v74, v74\n\tv_exp_f32 v75, v75\n\t"
        "v_exp_f32 v76, v76\n\tv_exp_f32 v77, v77\n\tv_exp_f32 v78, v78\n\tv_exp_f32 v79, v79\n\tv_exp_f32 v48, v48\n\tv_exp_f32 v49, v49\n\t"
        "v_smfmac_f32_32x32x32_bf16 %[o1], v[116:119], v[72:79], v128\n\tv_exp_f32 v50, v50\n\tv_exp_f32 v51, v51\n\tv_exp_f32 v52, v52\n\t"
        "v_mfma_f32_32x32x16_bf16 v[64:79], v[88:91], %[q1], 0\n\tv_exp_f32 v53, v53\n\tv_exp_f32 v54, v54\n\tv_exp_f32 v55, v55\n\tv_exp_f32 v56, v56\n\tv_exp_f32 v57, v57\n\t"
        "s_waitcnt vmcnt(5)\n\tv_smfmac_f32_32x32x32_bf16 %[o0], v[104:107], v[48:55], v128\n\tv_exp_f32 v58, v58\n\tv_exp_f32 v59, v59\n\tv_exp_f32 v60, v60\n\tv_exp_f32 v61, v61\n\t"
        "v_exp_f32 v62, v62\n\tv_exp_f32 v63, v63\n\tv_exp_f32 v64, v64\n\tv_exp_f32 v65, v65\n\tv_smfmac_f32_32x32x32_bf16 %[o0], v[120:123], v[56:63], v128\n\tv_exp_f32 v66, v66\n\t"
        "v_exp_f32 v67, v67\n\tv_exp_f32 v68, v68\n\tv_mfma_f32_32x32x16_bf16 v[48:63], v[92:95], %[q0], 0\n\tv_exp_f32 v69, v69\n\tv_exp_f32 v70, v70\n\tv_exp_f32 v71, v71\n\t"
        "v_exp_f32 v72, v72\n\tv_exp_f32 v73, v73\n\tv_smfmac_f32_32x32x32_bf16 %[o1], v[104:107], v[64:71], v128\n\tv_exp_f32 v74, v74\n\tv_exp_f32 v75, v75\n\tv_exp_f32 v76, v76\n\t"
        "v_exp_f32 v77, v77\n\tv_exp_f32 v78, v78\n\tv_exp_f32 v79, v79\n\tv_exp_f32 v48, v48\n\tv_exp_f32 v49, v49\n\tv_smfmac_f32_32x32x32_bf16 %[o1], v[120:123], v[72:79], v128\n\t"
        "v_exp_f32 v50, v50\n\tv_exp_f32 v51, v51\n\tv_exp_f32 v52, v52\n\tv_mfma_f32_32x32x16_bf16 v[64:79], v[92:95], %[q1], 0\n\tv_exp_f32 v53, v53\n\tv_exp_f32 v54, v54\n\t"
        "v_exp_f32 v55, v55\n\tv_exp_f32 v56, v56\n\tv_exp_f32 v57, v57\n\ts_waitcnt vmcnt(1)\n\tv_smfmac_f32_32x32x32_bf16 %[o0], v[108:111], v[48:55], v128\n\tv_exp_f32 v58, v58\n\t"
        "v_exp_f32 v59, v59\n\tv_exp_f32 v60, v60\n\tv_exp_f32 v61, v61\n\tv_exp_f32 v62, v62\n\tv_exp_f32 v63, v63\n\tv_exp_f32 v64, v64\n\tv_exp_f32 v65, v65\n\t"
        "v_smfmac_f32_32x32x32_bf16 %[o0], v[124:127], v[56:63], v128\n\tv_exp_f32 v66, v66\n\tv_exp_f32 v67, v67\n\tv_exp_f32 v68, v68\n\tv_exp_f32 v69, v69\n\tv_exp_f32 v70, v70\n\t"
        "v_exp_f32 v71, v71\n\tv_exp_f32 v72, v72\n\tv_exp_f32 v73, v73\n\tv_smfmac_f32_32x32x32_bf16 %[o1], v[108:111], v[64:71], v128\n\tv_exp_f32 v74, v74\n\tv_exp_f32 v75, v75\n\t"
        "v_exp_f32 v76, v76\n\tv_exp_f32 v77, v77\n\tv_exp_f32 v78, v78\n\tv_exp_f32 v79, v79\n\ts_setprio 0\n\ts_nop 0\n\tv_smfmac_f32_32x32x32_bf16 %[o1], v[124:127], v[72:79], v128\n\t"
        "s_waitcnt vmcnt(0)\n\ts_nop 15\n\t"
        : [o0] "+v"(o0), [o1] "+v"(o1), "=&{v[144:159]}"(sg[0]), "=&{v[160:175]}"(sg[1]), "=&{v[176:191]}"(sg[2]), "=&{v[192:207]}"(sg[3]), "=&{v[208:223]}"(sg[4]), "=&{v[224:239]}"(sg[5]), "=&{v[240:255]}"(sg[6])
        : [q0] "v"(q0), [q1] "v"(q1), [koff] "v"(koff), [voff] "v"(voff), [vinit] "v"(vinit), [vm] "s"(0x0000ffff0000ffffull), [kin] "s"(kb), [vin] "s"(vb), [drop] "s"(drop),
          [a0] "v"(a0), [a1] "v"(a1), [a2] "v"(a2), [b0] "v"(v30), [b1] "v"(v31), [b2] "v"(v32), [vw] "v"(vw), [sa] "s"(sa), [sb] "s"(sb), [so] "s"(so), [sh] "s"(sh), [ga] "s"(ga), [ol] "s"(ol), [hl] "s"(hl)
        : "memory", "scc", "s40", "s41", "s42", "s43", "s46", "s47", "s48", "s49", "s50", "s51", "s52", "s53",
          "v48","v49","v50","v51","v52","v53","v54","v55","v56","v57","v58","v59","v60","v61","v62","v63","v64","v65","v66","v67","v68","v69","v70","v71","v72","v73","v74","v75","v76","v77","v78","v79",
          "v80","v81","v82","v83","v84","v85","v86","v87","v88","v89","v90","v91","v92","v93","v94","v95","v96","v97","v98","v99","v100","v101","v102","v103","v104","v105","v106","v107","v108","v109","v110","v111",
          "v112","v113","v114","v115","v116","v117","v118","v119","v120","v121","v122","v123","v124","v125","v126","v127","v128");
    }
    STAMP(7); STAMPC(27);
    if (wave < 4) { res[0] = *(const u32x4*)(x + rowoff); res[1] = *(const u32x4*)(x + rowoff + 16); }
    LAS float* red = (LAS float*)(smem + L_RED);
#pragma unroll
    for (int i = 0; i < 9; i++) red[(wave * 9 + i) * 64 + lane] = kh ? o0[i] : o1[i];
    BARRIER();
    float o[9];
#pragma unroll
    for (int i = 0; i < 9; i++) o[i] = (kh ? o1[i] : o0[i]) + red[((wave ^ 1) * 9 + i) * 64 + lane];
    const bool ok = o[8] >= 0x1p-40f && o[8] <= 0x1p40f;
    if (!__all(ok)) {
      const u32x4 qq = kh ? q1 : q0;
      f32x16 z, oc;
#pragma unroll
      for (int i = 0; i < 16; i++) { z[i] = 0.f; oc[i] = 0.f; }
      float m = -__builtin_inff();
      const bool vl = (lane & 16) == 0;
      const u32x4 ones = {0x3f803f80u, 0x3f803f80u, 0x3f803f80u, 0x3f803f80u};
#pragma unroll 1
      for (int t = 0; t < 64; t++) {
        const u32x4 kk = ld16(rs, lane * 16, WS_K + head * 65536 + t * 1024);
        u32x4 wa = ld16(rs, h * 256 + (lane & 15) * 16, WS_V + head * 65536 + t * 1024), wb = ld16(rs, h * 256 + (lane & 15) * 16, WS_V + head * 65536 + t * 1024 + 512);
        wa = vl ? wa : ones; wb = vl ? wb : ones;
        f32x16 s = mfma(kk, qq, z);
        float tm = s[0];
#pragma unroll
        for (int i = 1; i < 16; i++) tm = fmaxf(tm, s[i]);
        tm = fmaxf(tm, __shfl_xor(tm, 32));
        const float mn = fmaxf(m, tm), al = __builtin_amdgcn_exp2f(m - mn);
        m = mn;
#pragma unroll
        for (int i = 0; i < 16; i++) { oc[i] *= al; s[i] -= mn; }
        oc = pv(wa, exp8(s, 0), oc); oc = pv(wb, exp8(s, 8), oc);
      }
#pragma unroll
      for (int i = 0; i < 9; i++) o[i] = oc[i];
    }
    const float rl = __builtin_amdgcn_rcpf(o[8]);
    u32x4 r;
#pragma unroll
    for (int j = 0; j < 4; j++) r[j] = pack2(o[2 * j] * rl, o[2 * j + 1] * rl);
    st16(rs, lane * 16, WS_O + ((rg1 * 2 + kh) * 32 + head) * 1024, r);
    STAMP(8);
    BARRIER();
    publish(ws, WS_F2 + ((rg1 * 2 + kh) * 32 + head) * 8, tag, lane);
    STAMP(9);
  }

  LAS char* bb1 = smem + L_B1 + (8 * h + q) * 192 + blk * 32 + p * 8;
  if (wave < 4) {
  const int rb = wave >> 1, kh = wave & 1;
  const int tile = rg * 2 + rb;
  LAS char* bsq[2];
#pragma unroll
  for (int u = 0; u < 2; u++) bsq[u] = smem + (4 * h + q) * 128 + (((4 * (u ^ kh) + 2 * blk + (p & 1)) ^ ((q >> 1) << 2)) << 4) + 8 * (p >> 1);
  auto nohook = [](auto) {};
  auto exchange = [&](f32x16 (&acc)[2], int lds) {
#pragma unroll
    for (int i = 0; i < 4; i++) *(LAS f32x4*)(smem + lds + (wave * 4 + i) * 1024 + lane * 16) = f32x4{acc[1][4 * i], acc[1][4 * i + 1], acc[1][4 * i + 2], acc[1][4 * i + 3]};
    BARRIER();
#pragma unroll
    for (int i = 0; i < 4; i++) {
      const f32x4 v = *(LAS f32x4*)(smem + lds + ((wave ^ 1) * 4 + i) * 1024 + lane * 16);
      for (int j = 0; j < 4; j++) acc[0][4 * i + j] += v[j];
    }
  };
  {
    f32x16 acc[2];
#pragma unroll
    for (int i = 0; i < 16; i++) { acc[0][i] = 0.f; acc[1][i] = 0.f; }
    wait_flags(ws, WS_F2 + (tile * 32 + kh * 16 + (lane & 15)) * 8, tag);
    STAMP(10);
    gemm_direct<16, 2, 0>(rs, lane * 16, [&](int pos) { return WS_O + (tile * 32 + kh * 16 + ((pos + off3) & 15)) * 1024; },
                          [&](int pos, int u) { return tr_frag(bsq[u] + kh * 32768 + pos * 2048, 1024); }, (const u32x4(*)[2]) nullptr, acc, nohook, nohook);
    STAMP(11);
    exchange(acc, L_Q);
    f32x2 t[8];
#pragma unroll
    for (int i = 0; i < 8; i++) t[i] = f32x2{acc[0][2 * i] + bflo(res[i >> 2][i & 3]), acc[0][2 * i + 1] + bfhi(res[i >> 2][i & 3])};
    FENCE();
#pragma unroll
    for (int i = 0; i < 8; i++) res[i >> 2][i & 3] = pack2v(t[i]);
    FENCE();
    st16(rs, lane * 16, WS_H + (tile * 32 + cs * 4 + kh * 2) * 1024, res[0]);
    st16(rs, lane * 16, WS_H + (tile * 32 + cs * 4 + kh * 2 + 1) * 1024, res[1]);
#pragma unroll
    for (int i = 0; i < 8; i++) t[i] = f32x2{bflo(res[i >> 2][i & 3]), bfhi(res[i >> 2][i & 3])};
    FENCE();
    f32x2 s2[4], q2[4];
#pragma unroll
    for (int i = 0; i < 4; i++) { s2[i] = t[i] + t[i + 4]; q2[i] = pfma(t[i], t[i], t[i + 4] * t[i + 4]); }
    const f32x2 sa = (s2[0] + s2[1]) + (s2[2] + s2[3]), qa = (q2[0] + q2[1]) + (q2[2] + q2[3]);
    const float sx = xhalf_sum(sa[0] + sa[1]), sxx = xhalf_sum(qa[0] + qa[1]);
    if (h == 0) __builtin_amdgcn_raw_buffer_store_b64(u32x2{asu(sx), asu(sxx)}, rs, r32 * 8, WS_ST + (tile * 16 + cs * 2 + kh) * 256, 16);
    STAMP(12);
    BARRIER();
#pragma unroll
    for (int i = 0; i < 12; i++) *(LAS u32x4*)(smem + L_B1 + lw * 12288 + i * 1024 + lane * 16) = stg(i);
    publish(ws, WS_F3 + (tile * 16 + cs * 2 + kh) * 8, tag, lane);
#pragma unroll
    for (int i = 0; i < 8; i++) {
      LAS char* dst = smem + (2 * ((lw >> 1) * 7 + i) + (lw & 1)) * 2048 + lane * 16;
      *(LAS u32x4*)dst = stg(12 + 2 * i); *(LAS u32x4*)(dst + 1024) = stg(13 + 2 * i);
    }
    STAMP(13);
  }
  u32x4 pre[4][2];
  {
    const int t4 = rp * 4 + wave;
    f32x16 acc[3];
#pragma unroll
    for (int u = 0; u < 3; u++) for (int i = 0; i < 16; i++) acc[u][i] = 0.f;
    BARRIER();
    STAMP(14);
    lds_spin(L_RES + 16 + p * 4, 36);
    u32x4 wx[3];
#pragma unroll
    for (int u = 0; u < 3; u++) wx[u] = *(LAS u32x4*)(smem + L_RES + 256 + u * 512 + r32 * 16);
    wait_flags(ws, WS_F3 + (t4 * 16 + (lane & 15)) * 8, tag);
    STAMP(16);
    u32x2 st[8];
    float rs2 = 0.f, sx = 0.f, sxx = 0.f, nm = 0.f, sd = 0.f;
    u32x4 ax = {0u, 0u, 0u, 0u};
    gemm_direct<32, 3, 0>(rs, lane * 16, [&](int pos) { return WS_H + (t4 * 32 + ((pos + off4) & 31)) * 1024; },
        [&](int pos, int u) { return tr_frag(bb1 + pos * 3072 + u * 64, 768); }, (const u32x4(*)[3]) nullptr, acc,
        [&](auto pp) {
          constexpr int pos = pp.value;
          if constexpr (pos < 8) st[pos] = __builtin_amdgcn_raw_buffer_load_b64(rs, h * 2048 + r32 * 8, WS_ST + (t4 * 16 + pos) * 256, 16);
          if constexpr (pos >= 12 && pos < 20) { sx = add1(asf(st[pos - 12][0]), sx); sxx = add1(asf(st[pos - 12][1]), sxx); }
          if constexpr (pos == 20) sx = xhalf_sum(sx);
          if constexpr (pos == 21) sxx = xhalf_sum(sxx);
          if constexpr (pos == 22) { nm = sx * (-1.f / D_); sd = fmaxf(fma1(sxx, 1.f / D_, -nm * nm), 0.f) + 1e-5f; }
          if constexpr (pos == 23) { rs2 = __builtin_amdgcn_rsqf(sd); asm("" : "+v"(rs2)); }
          if constexpr (pos == 24) { sd *= rs2; rs2 *= LOG2E; nm = h ? 0.f : nm; sd = h ? 0.f : sd; asm("" : "+v"(nm), "+v"(sd), "+v"(rs2)); }
          if constexpr (pos == 25) { ax[0] = pack2(nm, nm); ax[3] = pack2(sd, sd); }
          if constexpr (pos == 26) { nm = add1(nm, -bflo(ax[0])); sd = add1(sd, -bflo(ax[3])); }
          if constexpr (pos == 27) { ax[1] = (pack2(nm, 0.f) & 0xffffu) | (ax[3] & 0xffff0000u); ax[2] = pack2(sd, 0.f); ax[3] = 0u; }
        },
        nohook);
#pragma unroll
    for (int u = 0; u < 3; u++) acc[u] = mfma(wx[u], ax, acc[u]);
    STAMP(17);
#pragma unroll
    for (int i = 0; i < 4; i++) for (int u = 0; u < 2; u++) pre[i][u] = tr_frag(bsq[u] + (2 * i + kh) * 2048, 1024);
    FENCE();
    prog[wave] = 34;
    FENCE();
#pragma unroll
    for (int u = 0; u < 3; u++) {
      if (u < nunit) {
        f32x2 w[8], t[8];
#pragma unroll
        for (int i = 0; i < 8; i++) w[i] = f32x2{acc[u][2 * i], acc[u][2 * i + 1]} * pk(rs2);
        FENCE();
#pragma unroll
        for (int i = 0; i < 8; i++) t[i] = f32x2{__builtin_amdgcn_exp2f(w[i][0]), __builtin_amdgcn_exp2f(w[i][1])};
        FENCE();
        f32x2 a2[8];
#pragma unroll
        for (int i = 0; i < 8; i++) a2[i] = t[i] + pk(2.f);
        FENCE();
#pragma unroll
        for (int i = 0; i < 8; i++) t[i] = pfma(t[i], a2[i], pk(2.f));
        FENCE();
#pragma unroll
        for (int i = 0; i < 8; i++) t[i] = f32x2{__builtin_amdgcn_rcpf(t[i][0]), __builtin_amdgcn_rcpf(t[i][1])};
        FENCE();
#pragma unroll
        for (int i = 0; i < 8; i++) t[i] = pfma(t[i], pk(-2.f / LOG2E), pk(1.f / LOG2E));
        FENCE();
#pragma unroll
        for (int i = 0; i < 8; i++) t[i] = t[i] * w[i];
        FENCE();
        unsigned o[8];
#pragma unroll
        for (int i = 0; i < 8; i++) o[i] = pack2v(t[i]);
        FENCE();
        const unsigned so = WS_ACT + (t4 * 86 + (unit0 + u) * 2) * 1024;
        st16(rs, lane * 16, so, u32x4{o[0], o[1], o[2], o[3]});
        st16(rs, lane * 16, so + 1024, u32x4{o[4], o[5], o[6], o[7]});
      }
    }
    STAMP(18);
    publish(ws, WS_F4 + (t4 * 16 + sl) * 8, tag, lane);
    STAMP(19);
  }
  {
    f32x16 acc[2];
#pragma unroll
    for (int i = 0; i < 16; i++) { acc[0][i] = 0.f; acc[1][i] = 0.f; }
    BARRIER();
    wait_flags(ws, WS_F4 + (tile * 16 + (lane & 15)) * 8, tag);
    STAMP(20);
    gemm_direct<43, 2, 4>(rs, lane * 16, [&](int pos) { int ks = pos + off5; ks = ks >= 43 ? ks - 43 : ks; return WS_ACT + (tile * 86 + kh * 43 + ks) * 1024; },
                          [&](int pos, int u) { return tr_frag(bsq[u] + kh * 2048 + (pos >= 39 ? pos - 39 : pos) * 4096, 1024); }, pre, acc, nohook, nohook);
    STAMP(21);
    BARRIER();
    exchange(acc, 0);
#pragma unroll
    for (int s = 0; s < 2; s++) {
      u32x4 o;
#pragma unroll
      for (int j = 0; j < 4; j++) o[j] = pack2(acc[0][8 * s + 2 * j] + bflo(res[s][j]), acc[0][8 * s + 2 * j + 1] + bfhi(res[s][j]));
      *(u32x4*)(out + rowoff + 16 * s) = o;
    }
    if (b == 0 && tid == 0) *(u64*)(ws + WS_EP) = tag;
    STAMP(22);
    STAMP_FLUSH();
  }
  } else {
    const unsigned bv = b1[(unit0 + min(lw, 2)) * 32 + r32];
    const unsigned selv = ((lane & 15) == 4 * blk) ? 0x3f803f80u : 0u;
    const u32x4 sel = {selv, selv, selv, selv};
    f32x4 c4 = {0.f, 0.f, 0.f, 0.f};
    auto colsum = [&](int k0) {
      u32x4 f[16];
#pragma unroll
      for (int i = 0; i < 16; i++) f[i] = tr_frag(bb1 + (k0 + i) * 3072 + min(lw, 2) * 64, 768);
#pragma unroll
      for (int i = 0; i < 16; i++) c4 = mfma16(sel, f[i], c4);
    };
    colsum(16);
    BARRIER();
    BARRIER();
    BARRIER();
    STAMP(14);
    colsum(0);
    if (lw < 3 && h == 0) {
      const unsigned ch = pack2(c4[0], 0.f), cl = pack2(c4[0] - bflo(ch), 0.f);
      *(LAS u32x4*)(smem + L_RES + 256 + lw * 512 + r32 * 16) = u32x4{ch | (cl << 16), ch | (bv << 16), bv, 0u};
    }
    prog[wave] = 36;
    STAMP(15);
    lds_spin(L_RES + p * 4, 34);
    LAS char* dst = smem + (2 * (15 + (lw >> 1) * 14) + (lw & 1)) * 2048 + lane * 16;
    LAS char* dst2 = (lw >> 1) ? dst - 78 * 2048 : dst;
#pragma unroll
    for (int i = 0; i < 14; i++) { LAS char* d = (i >= 10 ? dst2 : dst) + i * 4096; *(LAS u32x4*)d = stg(2 * i); *(LAS u32x4*)(d + 1024) = stg(2 * i + 1); }
    STAMP(19);
    BARRIER();
    STAMP_FLUSH();
  }
}

extern "C" void kernel_launch(void* const* d_in, const int* in_sizes, int n_in, void* d_out, int out_size, void* d_ws, size_t ws_size, hipStream_t stream) {
  fused<<<256, 512, 0, stream>>>((const u16*)d_in[0], (const u16*)d_in[1], (const u16*)d_in[2], (const u16*)d_in[4], (const u16*)d_in[5], (const u16*)d_in[6], (char*)d_ws, (u16*)d_out);
}
```

```cpp
#include <hip/hip_runtime.h>
#include <hip/hip_bf16.h>
#include <stdint.h>

typedef __attribute__((ext_vector_type(16))) float f32x16;
typedef __attribute__((ext_vector_type(8))) float f32x8;
typedef __attribute__((ext_vector_type(4))) float f32x4;
typedef __attribute__((ext_vector_type(2))) float f32x2;
typedef __attribute__((ext_vector_type(8))) __bf16 bf16x8;
typedef __attribute__((ext_vector_type(16))) __bf16 bf16x16;
typedef __attribute__((ext_vector_type(2))) __bf16 bf16x2;
typedef __attribute__((ext_vector_type(4))) short s16x4;
typedef __attribute__((ext_vector_type(4))) unsigned u32x4;
typedef __attribute__((ext_vector_type(2))) unsigned u32x2;
typedef __attribute__((ext_vector_type(16))) unsigned u32x16;
typedef unsigned short u16;
typedef unsigned long long u64;
#define LAS __attribute__((address_space(3)))
#define GAS __attribute__((address_space(1)))
#define DEV __device__ __forceinline__

constexpr int D_ = 512, F_ = 1376;
constexpr float LOG2E = 1.4426950408889634f;

constexpr unsigned WS_K = 0, WS_V = 2u << 20, WS_O = 4u << 20, WS_H = 6u << 20, WS_ACT = 8u << 20, WS_ST = 14u << 20;
constexpr unsigned WS_EP = 15u << 20, WS_F1 = WS_EP + 4096, WS_F2 = WS_F1 + 32768, WS_F3 = WS_F2 + 32768, WS_F4 = WS_F3 + 32768, WS_END = 16u << 20;

DEV unsigned pack2(float a, float b) { f32x2 p = {a, b}; return __builtin_bit_cast(unsigned, __builtin_convertvector(p, bf16x2)); }
DEV float asf(unsigned u) { return __builtin_bit_cast(float, u); }
DEV unsigned asu(float f) { return __builtin_bit_cast(unsigned, f); }
DEV float bflo(unsigned u) { return __builtin_bit_cast(float, u << 16); }
DEV float bfhi(unsigned u) { return __builtin_bit_cast(float, u & 0xffff0000u); }
DEV f32x16 mfma(u32x4 a, u32x4 b, f32x16 c) { return __builtin_amdgcn_mfma_f32_32x32x16_bf16(__builtin_bit_cast(bf16x8, a), __builtin_bit_cast(bf16x8, b), c, 0, 0, 0); }
DEV f32x4 mfma16(u32x4 a, u32x4 b, f32x4 c) { return __builtin_amdgcn_mfma_f32_16x16x32_bf16(__builtin_bit_cast(bf16x8, a), __builtin_bit_cast(bf16x8, b), c, 0, 0, 0); }
DEV u32x2 tr_read(LAS char* p) { return __builtin_bit_cast(u32x2, __builtin_amdgcn_ds_read_tr16_b64_v4i16((LAS s16x4*)p)); }
DEV u32x4 tr_frag(LAS char* p, int step) { const u32x2 w0 = tr_read(p), w1 = tr_read(p + step); return u32x4{w0[0], w0[1], w1[0], w1[1]}; }
DEV float xhalf_sum(float v) {
  const unsigned u = __builtin_bit_cast(unsigned, v);
  const auto r = __builtin_amdgcn_permlane32_swap(u, u, false, false);
  return asf(r[0]) + asf(r[1]);
}
#define BARRIER() do { __builtin_amdgcn_sched_barrier(0); __builtin_amdgcn_s_waitcnt(0xC07F); asm volatile("s_barrier" ::: "memory"); __builtin_amdgcn_sched_barrier(0); } while (0)
template <int N> DEV void wait_vm() { asm volatile("s_waitcnt vmcnt(%0)" ::"n"(N) : "memory"); }
#define SGB(m, n) __builtin_amdgcn_sched_group_barrier(m, n, 0)
template <int I, int N, class Fn> DEV void static_for(Fn&& f) {
  if constexpr (I < N) { f(std::integral_constant<int, I>{}); static_for<I + 1, N>(f); }
}

#define STAMP(i)
#define STAMPC(i)
#define STAMP_DECL
#define STAMP_FLUSH()

DEV void dma_p1(u64 sw, unsigned vw0, unsigned vw1, int lds) {
#define P1(dm, v, off) "s_add_u32 m0, %[m], " #dm "\n\ts_nop 0\n\tglobal_load_lds_dwordx4 %[" #v "], %[sw] offset:" #off "\n\t"
  asm volatile(P1(0, vw0, 0) P1(0x400, vw1, 0) P1(0xc00, vw0, 1024) P1(0x1000, vw1, 1024) P1(0x1800, vw0, 2048) P1(0x1c00, vw1, 2048)
               :: [m] "s"(lds), [sw] "s"(sw), [vw0] "v"(vw0), [vw1] "v"(vw1) : "memory", "scc");
}
DEV void dma_p1h(u64 sw, unsigned vw0, int lds) {
  asm volatile(P1(0, vw0, 0) P1(0xc00, vw0, 1024) P1(0x1800, vw0, 2048) :: [m] "s"(lds), [sw] "s"(sw), [vw0] "v"(vw0) : "memory", "scc");
}
DEV void dma_k2(u64 sbase, unsigned v0, unsigned v1, int lds) {
  asm volatile("s_mov_b32 m0, %[m]\n\ts_add_u32 %[m], %[m], 0x400\n\tglobal_load_lds_dwordx4 %[v0], %[sb]\n\t"
               "s_mov_b32 m0, %[m]\n\ts_nop 0\n\tglobal_load_lds_dwordx4 %[v1], %[sb]"
               : [m] "+s"(lds) : [sb] "s"(sbase), [v0] "v"(v0), [v1] "v"(v1) : "memory", "scc");
}
DEV void dma_k3(u64 sbase, unsigned v0, unsigned v1, unsigned v2, int lds) {
  asm volatile("s_mov_b32 m0, %[m]\n\ts_add_u32 %[m], %[m], 0x400\n\tglobal_load_lds_dwordx4 %[v0], %[sb]\n\t"
               "s_mov_b32 m0, %[m]\n\ts_add_u32 %[m], %[m], 0x400\n\tglobal_load_lds_dwordx4 %[v1], %[sb]\n\t"
               "s_mov_b32 m0, %[m]\n\ts_nop 0\n\tglobal_load_lds_dwordx4 %[v2], %[sb]"
               : [m] "+s"(lds) : [sb] "s"(sbase), [v0] "v"(v0), [v1] "v"(v1), [v2] "v"(v2) : "memory", "scc");
}

#define RLX_AGENT __ATOMIC_RELAXED, __HIP_MEMORY_SCOPE_AGENT
DEV void publish(char* ws, unsigned off, u64 tag, int lane) {
  if (lane == 0) __hip_atomic_store((GAS u64*)(ws + off), tag, RLX_AGENT);
}
#define PSLEEP ""
DEV void wait_flags(char* ws, unsigned off, u64 tag) {
  u64 v; int n = 1 << 18;
  asm volatile("1:\n\tglobal_load_dwordx2 %[v], %[off], %[ws] sc1\n\ts_sub_u32 %[n], %[n], 1\n\ts_cbranch_scc1 2f\n\t"
               "s_waitcnt vmcnt(0)\n\tv_cmp_ne_u64 vcc, %[tag], %[v]\n\t" PSLEEP "s_cbranch_vccnz 1b\n\t2:\n\ts_waitcnt vmcnt(0)"
               : [v] "=&v"(v), [n] "+s"(n) : [off] "v"(off), [ws] "s"(ws), [tag] "s"(tag) : "memory", "vcc", "scc");
}
typedef __amdgpu_buffer_rsrc_t rsrc_t;
DEV void st16(rsrc_t r, unsigned voff, unsigned soff, u32x4 v) { __builtin_amdgcn_raw_buffer_store_b128(v, r, voff, soff, 16); }
DEV u32x4 ld16(rsrc_t r, unsigned voff, unsigned soff) { return __builtin_amdgcn_raw_buffer_load_b128(r, voff, soff, 16); }
#define LSLEEP 1
#define THR 6
#define W2POS 12
#define OUTAUX 0
#define FIRST 4
#define FSLEEP 6
#define WDELAY 4
#define DROPI 4
DEV unsigned min4(u32x4 v) { return __builtin_amdgcn_readfirstlane(min(min(v[0], v[1]), min(v[2], v[3]))); }
DEV void lds_wait(volatile LAS u32x4* p, unsigned have, unsigned need) {
#pragma unroll 1
  while (have < need) { have = min4(*p); if (have < need) __builtin_amdgcn_s_sleep(LSLEEP); }
  asm volatile("" ::: "memory");
}

#define LSP ""
DEV void lds_spin(unsigned addr, unsigned need) {
  unsigned a;
  asm volatile("1:\n\tds_read_b32 %[a], %[p]\n\ts_waitcnt lgkmcnt(0)\n\tv_cmp_gt_u32 vcc, %[n], %[a]\n\t" LSP "s_cbranch_vccnz 1b"
               : [a] "=&v"(a) : [p] "v"(addr), [n] "s"(need) : "memory", "vcc");
}
DEV f32x16 pv(u32x4 v, f32x8 pr, f32x16 o) {
  return __builtin_amdgcn_smfmac_f32_32x32x32_bf16(__builtin_bit_cast(bf16x8, v), __builtin_bit_cast(bf16x16, pr), o, 0xDDDD, 0, 0);
}
DEV f32x8 exp8(f32x16 s, int o) { f32x8 r; for (int i = 0; i < 8; i++) r[i] = __builtin_amdgcn_exp2f(s[o + i]); return r; }
DEV float fma1(float a, float b, float c) { float r = __builtin_fmaf(a, b, c); asm("" : "+v"(r)); return r; }
DEV float add1(float a, float b) { float r = a + b; asm("" : "+v"(r)); return r; }
DEV float dot2(unsigned a, unsigned b, float c) { return __builtin_amdgcn_fdot2_f32_bf16(__builtin_bit_cast(bf16x2, a), __builtin_bit_cast(bf16x2, b), c, false); }
DEV f32x2 pk(float a) { return f32x2{a, a}; }
DEV f32x2 pfma(f32x2 a, f32x2 b, f32x2 c) { return __builtin_elementwise_fma(a, b, c); }
DEV unsigned pack2v(f32x2 p) { return __builtin_bit_cast(unsigned, __builtin_convertvector(p, bf16x2)); }
#define FENCE() __builtin_amdgcn_sched_barrier(0)

struct WFrag { u32x4 a[4]; u32x4 w[4][3]; };
constexpr int L_RES = 159744;
constexpr int L_Q = 67584, L_RED = 75776, L_DUM = 94208;
constexpr int L_W1 = 65536, L_Q1 = 151552;
constexpr int L_B1 = 61440;
constexpr int CODE_BYTES = 33792;
#define RD_ 10
constexpr int RD = RD_;

template <int NK, int NU, int NPRE, class ASO, class BRD, class H1, class H2>
DEV void gemm_direct(rsrc_t rs, unsigned voff, ASO&& aso, BRD&& brd, const u32x4 (*pre)[NU], f32x16 (&acc)[NU], H1&& hook, H2&& hook2) {
  u32x4 a[RD], w[3][NU];
  static_for<0, RD>([&](auto i) { a[i.value] = ld16(rs, voff, aso(i.value)); });
  static_for<0, 2>([&](auto i) {
    if constexpr (i.value >= NPRE) { for (int u = 0; u < NU; u++) w[i.value][u] = brd(i.value, u); }
  });
  FENCE();
  static_for<0, NK>([&](auto pp) {
    constexpr int pos = pp.value;
    static_for<0, NU>([&](auto uu) {
      constexpr int u = uu.value;
      if constexpr (pos < NPRE) acc[u] = mfma(pre[pos][u], a[pos % RD], acc[u]); else acc[u] = mfma(w[pos % 3][u], a[pos % RD], acc[u]);
      FENCE();
      if constexpr (u == 0) hook(pp);
      if constexpr (pos + 2 < NK && pos + 2 >= NPRE) w[(pos + 2) % 3][u] = brd(pos + 2, u);
      if constexpr (u == NU - 1) {
        if constexpr (pos + RD < NK) a[pos % RD] = ld16(rs, voff, aso(pos + RD));
        hook2(pp);
      }
      FENCE();
    });
  });
}

__global__ __launch_bounds__(512, 2) void fused(const u16* __restrict__ x, const u16* __restrict__ wqkv, const u16* __restrict__ wo, const u16* __restrict__ w1,
                                                const u16* __restrict__ b1, const u16* __restrict__ w2, char* __restrict__ ws, u16* __restrict__ out) {
  __shared__ __attribute__((aligned(16))) char smem_[163840];
  LAS char* smem = (LAS char*)smem_;
  const int wave = __builtin_amdgcn_readfirstlane(threadIdx.x >> 6);
  int lane = threadIdx.x & 63;
  const int b = blockIdx.x, xcd = b & 7, bi = b >> 3;
  STAMP_DECL; STAMP(0);
  u64 pc0; asm volatile("s_getpc_b64 %0" : "=s"(pc0));
  asm volatile("" ::"s"(x), "s"(wqkv), "s"(wo), "s"(w1), "s"(b1), "s"(w2), "s"(ws), "s"(out));
  u64 tag = 0;
  auto load_tag = [&]() { asm volatile("s_load_dwordx2 %0, %1, 0x0\n\ts_waitcnt lgkmcnt(0)" : "=s"(tag) : "s"(ws + WS_EP) : "memory"); tag += 1; };
  const rsrc_t rs = __builtin_amdgcn_make_buffer_rsrc(ws, 0, WS_END, 0x00020000);
  int r32 = lane & 31, h = lane >> 5, q = (lane & 15) >> 2, p = lane & 3, blk = (lane >> 4) & 1;
  const int lw = wave & 3;
  const unsigned vrow128 = 16 * ((lane & 7) ^ (((lane >> 4) & 1) << 2));
  volatile LAS unsigned* prog = (volatile LAS unsigned*)(smem + L_RES);

#define MAPB 1
  constexpr int HS = 3 - MAPB;
  const int hg = ((xcd & ((1 << MAPB) - 1)) << HS) + (bi & ((1 << HS) - 1)), rg1 = (xcd >> MAPB) * (32 >> HS) + (bi >> HS);
  volatile LAS u32x4* landed = (volatile LAS u32x4*)(prog + 4);
  const int c0 = bi >> HS;
  const u64 sw0 = (u64)(wqkv + hg * 64);
  const unsigned vw0 = (lane >> 3) * (3 * D_ * 2) + vrow128, vw1 = vw0 + 8 * 3 * D_ * 2;
  {
    const int seg = lane & 7, xr = (8 >> HS) * (bi & ((1 << HS) - 1));
    const char* xb = (const char*)x + (size_t)(rg1 * 64 + wave * 8) * 1024;
    const unsigned xo = (lane >> 3) * 1024 + seg * 16;
    u32x4 xv[8];
#pragma unroll
    for (int i = 0; i < 8; i++) asm volatile("global_load_dwordx4 %0, %1, %2" : "=v"(xv[i]) : "v"(xo), "s"(xb + ((i + xr) & 7) * 128) : "memory");
    asm volatile("s_barrier" ::: "memory");
    dma_p1h(sw0 + (unsigned)(((wave + 8 * c0) & 63) * (8 * 3 * D_ * 2)), vw0, L_W1 + (wave >> 2) * 12288 + lw * 1024);
    unsigned tch;
    const char* tp = (const char*)(pc0 & ~127ull) + min(((bi * 8 + wave) * 2 + (lane & 1)) * 128, CODE_BYTES - 128);
    asm volatile("global_load_dword %0, %1, off" : "=v"(tch) : "v"(tp) : "memory");
    prog[wave] = wave;
    STAMP(1);
    float s4[4] = {0.f, 0.f, 0.f, 0.f}, q4[4] = {0.f, 0.f, 0.f, 0.f};
    static_for<0, 8>([&](auto ii) {
      constexpr int i = ii.value;
      asm volatile("s_waitcnt vmcnt(%1)" : "+v"(xv[i]) : "n"(11 - i));
#pragma unroll
      for (int j = 0; j < 4; j++) { s4[j] = dot2(xv[i][j], 0x3f803f80u, s4[j]); q4[j] = dot2(xv[i][j], xv[i][j], q4[j]); }
    });
    FENCE();
    dma_p1h(sw0 + (unsigned)(((wave + 8 + 8 * c0) & 63) * (8 * 3 * D_ * 2)), vw0, L_W1 + (2 + (wave >> 2)) * 12288 + lw * 1024);
    STAMP(25);
    float sx = (s4[0] + s4[1]) + (s4[2] + s4[3]), sxx = (q4[0] + q4[1]) + (q4[2] + q4[3]);
    sx += asf(__builtin_amdgcn_mov_dpp(asu(sx), 0xB1, 0xf, 0xf, true)); sxx += asf(__builtin_amdgcn_mov_dpp(asu(sxx), 0xB1, 0xf, 0xf, true));
    sx += asf(__builtin_amdgcn_mov_dpp(asu(sx), 0x4E, 0xf, 0xf, true)); sxx += asf(__builtin_amdgcn_mov_dpp(asu(sxx), 0x4E, 0xf, 0xf, true));
    sx += asf(__builtin_amdgcn_mov_dpp(asu(sx), 0x141, 0xf, 0xf, true)); sxx += asf(__builtin_amdgcn_mov_dpp(asu(sxx), 0x141, 0xf, 0xf, true));
    const float mean = sx * (1.f / D_);
    const float rstd = __builtin_amdgcn_rsqf(fmaxf(sxx * (1.f / D_) - mean * mean, 0.f) + 1e-5f), nmr = -mean * rstd;
    const int r = lw * 8 + (lane >> 3);
    LAS char* dst = smem + (wave >> 2) * 32768 + seg * 512 + ((r ^ seg) << 4);
#pragma unroll
    for (int i0 = 0; i0 < 8; i0 += 2) {
      f32x2 y[8];
#pragma unroll
      for (int j = 0; j < 8; j++) { const unsigned u = xv[i0 + (j >> 2)][j & 3]; y[j] = f32x2{bflo(u), bfhi(u)}; }
      FENCE();
#pragma unroll
      for (int j = 0; j < 8; j++) y[j] = pfma(y[j], pk(rstd), pk(nmr));
      FENCE();
      const u32x4 o0 = {pack2v(y[0]), pack2v(y[1]), pack2v(y[2]), pack2v(y[3])}, o1 = {pack2v(y[4]), pack2v(y[5]), pack2v(y[6]), pack2v(y[7])};
      *(LAS u32x4*)(dst + ((i0 + xr) & 7) * 4096) = o0;
      *(LAS u32x4*)(dst + ((i0 + 1 + xr) & 7) * 4096) = o1;
      FENCE();
    }
    if (wave < 4) prog[wave] = 0;
    STAMP(23);
    asm volatile("s_waitcnt vmcnt(0)" : "+v"(tch) :: "memory");
    BARRIER();
  }
  if (wave >= 4) {
#pragma unroll 1
    for (int c = lw + 4; c < 16; c += 4) {
      if (c >= 7) lds_spin(L_RES + p * 4, c - 6);
      const u64 src = sw0 + (unsigned)(((c + 2 * c0) & 15) * (32 * 3 * D_ * 2));
      const int slot = L_W1 + (c % 7) * 12288;
      {
      dma_p1(src, vw0, vw1, slot);
      dma_p1(src + 16 * 3 * D_ * 2, vw0, vw1, slot + 2048);
      }
      wait_vm<0>(); prog[wave] = c + 4;
    }
    STAMP(2);
    load_tag();
  } else {
    const int rb = wave >> 1, g = wave & 1;
    int aoff[4];
#pragma unroll
    for (int i = 0; i < 4; i++) aoff[i] = rb * 32768 + i * 1024 + h * 512 + ((r32 ^ (2 * i + h)) << 4);
    const int boff = L_W1 + (8 * h + q) * 128 + (((4 * g + 2 * blk + (p >> 1)) ^ ((q >> 1) << 2)) << 4) + 8 * (p & 1);
    f32x16 acc[3];
#pragma unroll
    for (int u = 0; u < 3; u++) for (int i = 0; i < 16; i++) acc[u][i] = 0.f;
    u32x4 fl;
    auto rd = [&](WFrag& f, int c) {
      const int ao = ((c + c0) & 7) * 4096;
#pragma unroll
      for (int i = 0; i < 4; i++) {
        f.a[i] = *(LAS u32x4*)(smem + aoff[i] + ao);
#pragma unroll
        for (int u = 0; u < 3; u++) f.w[i][u] = tr_frag(smem + boff + ((2 * c + (i >> 1)) % 7) * 12288 + (i & 1) * 2048 + u * 4096, 512);
        if (i & 1) prog[wave] = 2 * c + (i >> 1) + 1;
      }
      fl = *landed;
    };
    auto mm = [&](const WFrag& f) {
#pragma unroll
      for (int i = 0; i < 4; i++)
#pragma unroll
        for (int u = 0; u < 3; u++) { if (u == 2) acc[u] = mfma(f.a[i], f.w[i][u], acc[u]); else acc[u] = mfma(f.w[i][u], f.a[i], acc[u]); }
    };
    auto sched = [&]() {
      SGB(0x8, 1); SGB(0x2, 7);
#pragma unroll
      for (int i = 0; i < 11; i++) { SGB(0x80, 3); SGB(0x8, 1); }
    };
    auto step = [&](int c) { FENCE(); lds_wait(landed, min4(fl), 2 * c + 2); FENCE(); };
    WFrag fa, fb;
    STAMP(24);
    rd(fa, 0);
    static_for<0, 3>([&](auto cc) {
      constexpr int c = 2 * cc.value + 1;
      step(c);
      rd(fb, c); mm(fa); sched();
      step(c + 1);
      rd(fa, c + 1); mm(fb); sched();
    });
    step(7);
    STAMP(2);
    rd(fb, 7); mm(fa); sched();
    FENCE();
    mm(fb);
    STAMP(3);
    const int tile = rg1 * 2 + rb;
    const unsigned so = ((hg * 4 + 2 * g) * 64 + tile) * 1024;
    {
      unsigned o[8];
#pragma unroll
      for (int i = 0; i < 8; i++) o[i] = pack2(acc[1][2 * i], acc[1][2 * i + 1]);
      const unsigned pos = h * 32 + ((r32 & 19) | ((r32 & 4) << 1) | ((r32 & 8) >> 1));
      st16(rs, pos * 16, WS_K + so, u32x4{o[0], o[1], o[2], o[3]});
      st16(rs, pos * 16, WS_K + so + 65536, u32x4{o[4], o[5], o[6], o[7]});
#pragma unroll
      for (int i = 0; i < 8; i++) o[i] = pack2(acc[2][2 * i], acc[2][2 * i + 1]);
      const unsigned vo = (r32 >> 4) * 65536 + h * 256 + (r32 & 15) * 16;
      st16(rs, vo, WS_V + so, u32x4{o[0], o[1], o[2], o[3]});
      st16(rs, vo, WS_V + so + 512, u32x4{o[4], o[5], o[6], o[7]});
      STAMP(4);
#pragma unroll
      for (int i = 0; i < 8; i++) o[i] = pack2v(f32x2{acc[0][2 * i], acc[0][2 * i + 1]} * pk(0.25f * LOG2E));
      LAS char* d = smem + L_Q1 + (4 * g + rb) * 1024 + lane * 16;
      *(LAS u32x4*)d = u32x4{o[0], o[1], o[2], o[3]};
      *(LAS u32x4*)(d + 2048) = u32x4{o[4], o[5], o[6], o[7]};
    }
    wait_vm<0>();
    STAMP(5);
  }
  BARRIER();
  if (wave == 4) publish(ws, WS_F1 + (hg * 32 + rg1) * 128, tag, lane);

  const int rg = xcd * 4 + (bi >> 3), cs = bi & 7;
  const int rp = xcd * 2 + (bi >> 4), sl = bi & 15;
  const int unit0 = sl < 5 ? 2 * sl : 3 * sl - 5, nunit = sl < 5 ? 2 : 3;
  const int off3 = (cs & 3) * 4, off4 = (sl & 3) * 8, off5 = (0x080e161c00ull >> (8 * (cs % 5))) & 0xff;
  const unsigned vw = (lane >> 3) * (D_ * 2) + vrow128;
  auto v192 = [&](int j) { const int idx = j * 64 + lane, row = idx / 12; return (unsigned)(row * (F_ * 2) + (idx - row * 12) * 16); };

  const unsigned v30 = v192(0), v31 = v192(1), v32 = v192(2);
  u32x16 sg[7];
  u32x4 res[2];
  auto stg = [&](int i) { return u32x4{sg[i >> 2][(i & 3) * 4], sg[i >> 2][(i & 3) * 4 + 1], sg[i >> 2][(i & 3) * 4 + 2], sg[i >> 2][(i & 3) * 4 + 3]}; };
  auto w2at = [&](int pos) {
    int ks = pos + off5; ks = ks >= 43 ? ks - 43 : ks;
    return (const char*)(w2 + (size_t)((lw & 1) * 43 + ks) * 16 * D_ + cs * 64);
  };
  unsigned rowoff;

  {
    const int head = hg * 4 + (wave >> 1), kh = wave & 1;
    const u32x4 q0 = *(LAS u32x4*)(smem + L_Q1 + (wave >> 1) * 2048 + lane * 16), q1 = *(LAS u32x4*)(smem + L_Q1 + (wave >> 1) * 2048 + 1024 + lane * 16);
    const int drop = (wave >> 2) ^ 1;
    if (wave < 4) __builtin_amdgcn_s_setprio(2); else __builtin_amdgcn_s_setprio(1);
    f32x16 o0, o1;
#pragma unroll
    for (int i = 0; i < 16; i++) { o0[i] = 0.f; o1[i] = 0.f; }
    {
      const unsigned koff = lane * 16, voff = h * 256 + (lane & 15) * 16;
      const char* kb = ws + WS_K + (size_t)head * 65536 + kh * 32768;
      const char* vb = ws + WS_V + (size_t)head * 65536 + kh * 32768;
      const unsigned vinit = (r32 == 16 || r32 == 20) ? 0x3f803f80u : 0u;
      const unsigned a0 = wave < 4 ? v30 : vw, a1 = wave < 4 ? v31 : vw + 8 * D_ * 2, a2 = wave < 4 ? v32 : vw + 16 * D_ * 2;
      const char* sa = wave < 4 ? (const char*)(w1 + (size_t)((lw * 4 + off4) & 31) * 16 * F_ + unit0 * 32) : w2at(15 + (lw >> 1) * 14);
      const char* sb = w2at(wave < 4 ? (lw >> 1) * 7 : 21 + (lw >> 1) * 14);
      const int ga = wave < 4 ? 16 * F_ * 2 : 24 * D_ * 2;
      const char* so = (const char*)(wo + (size_t)((wave >> 2) * 256 + ((lw * 4 + off3) & 15) * 16) * D_ + cs * 64) - 8192;
      const char* sh = (const char*)(w1 + (size_t)((16 + wave * 2 + off4) & 31) * 16 * F_ + unit0 * 32);
      const int ol = wave * 8192, hl = L_B1 + (16 + wave * 2) * 3072;
      {
        const int l = min(lane, 26);
        const char* tp = wave >= 4 ? kb : l < 22 ? (const char*)((lw & 1) ? w2 : w1) + b * 5504 + ((lw >> 1) * 21 + l) * 128
                       : l < 26 ? (const char*)wo + b * 2048 + (lw * 4 + l - 22) * 128 : (const char*)b1 + ((b * 4 + lw) % 21) * 128;
        sg[6][14] = (unsigned)(u64)tp; sg[6][15] = (unsigned)((u64)tp >> 32);
      }
      asm volatile("" ::"v"(q0), "v"(q1), "v"(koff), "v"(voff), "v"(vinit), "v"(a0), "v"(a1), "v"(a2), "v"(v30), "v"(v31), "v"(v32), "v"(vw), "v"(sg[6]));
      asm volatile("" ::"s"(kb), "s"(vb), "s"(sa), "s"(sb), "s"(so), "s"(sh), "s"(ga), "s"(ol), "s"(hl));
    if (wave == 4) wait_flags(ws, WS_F1 + (hg * 32 + r32) * 128, tag);
    STAMP(6); STAMPC(26);
      asm volatile(
        "s_mov_b64 s[40:41], %[kin]\n\ts_mov_b64 s[42:43], %[vin]\n\ts_mov_b64 s[46:47], %[sa]\n\ts_mov_b64 s[48:49], %[sb]\n\ts_mov_b64 s[50:51], %[so]\n\ts_mov_b64 s[52:53], %[sh]\n\t"
        "v_mov_b32 v96, %[vinit]\n\tv_mov_b32 v112, %[vinit]\n\tv_mov_b32 v97, %[vinit]\n\tv_mov_b32 v113, %[vinit]\n\tv_mov_b32 v98, %[vinit]\n\tv_mov_b32 v114, %[vinit]\n\t"
        "v_mov_b32 v99, %[vinit]\n\tv_mov_b32 v115, %[vinit]\n\tv_mov_b32 v100, %[vinit]\n\tv_mov_b32 v116, %[vinit]\n\tv_mov_b32 v101, %[vinit]\n\tv_mov_b32 v117, %[vinit]\n\t"
        "v_mov_b32 v102, %[vinit]\n\tv_mov_b32 v118, %[vinit]\n\tv_mov_b32 v103, %[vinit]\n\tv_mov_b32 v119, %[vinit]\n\tv_mov_b32 v104, %[vinit]\n\tv_mov_b32 v120, %[vinit]\n\t"
        "v_mov_b32 v105, %[vinit]\n\tv_mov_b32 v121, %[vinit]\n\tv_mov_b32 v106, %[vinit]\n\tv_mov_b32 v122, %[vinit]\n\tv_mov_b32 v107, %[vinit]\n\tv_mov_b32 v123, %[vinit]\n\t"
        "v_mov_b32 v108, %[vinit]\n\tv_mov_b32 v124, %[vinit]\n\tv_mov_b32 v109, %[vinit]\n\tv_mov_b32 v125, %[vinit]\n\tv_mov_b32 v110, %[vinit]\n\tv_mov_b32 v126, %[vinit]\n\t"
        "v_mov_b32 v111, %[vinit]\n\tv_mov_b32 v127, %[vinit]\n\tv_mov_b32 v128, 0xdddd\n\ts_waitcnt lgkmcnt(0)\n\ts_barrier\n\t"
        "global_load_dwordx4 v[80:83], %[koff], s[40:41] offset:0 sc1\n\tglobal_load_dwordx4 v[84:87], %[koff], s[40:41] offset:1024 sc1\n\ts_mov_b64 exec, %[vm]\n\t"
        "global_load_dwordx4 v[96:99], %[voff], s[42:43] offset:0 sc1\n\tglobal_load_dwordx4 v[112:115], %[voff], s[42:43] offset:512 sc1\n\ts_mov_b64 exec, -1\n\t"
        "global_load_dwordx4 v[88:91], %[koff], s[40:41] offset:2048 sc1\n\ts_mov_b64 exec, %[vm]\n\tglobal_load_dwordx4 v[100:103], %[voff], s[42:43] offset:1024 sc1\n\t"
        "global_load_dwordx4 v[116:119], %[voff], s[42:43] offset:1536 sc1\n\ts_mov_b64 exec, -1\n\tglobal_load_dwordx4 v[92:95], %[koff], s[40:41] offset:3072 sc1\n\t"
        "s_mov_b64 exec, %[vm]\n\tglobal_load_dwordx4 v[104:107], %[voff], s[42:43] offset:2048 sc1\n\tglobal_load_dwordx4 v[120:123], %[voff], s[42:43] offset:2560 sc1\n\t"
        "s_mov_b64 exec, -1\n\ts_waitcnt vmcnt(9)\n\tv_mfma_f32_32x32x16_bf16 v[48:63], v[80:83], %[q0], 0\n\ts_nop 15\n\tv_exp_f32 v48, v48\n\tv_exp_f32 v49, v49\n\tv_exp_f32 v50, v50\n\t"
        "v_exp_f32 v51, v51\n\tv_exp_f32 v52, v52\n\tv_mfma_f32_32x32x16_bf16 v[64:79], v[80:83], %[q1], 0\n\ts_add_u32 s40, s40, 4096\n\ts_addc_u32 s41, s41, 0\n\t"
        "global_load_dwordx4 v[80:83], %[koff], s[40:41] offset:0 sc1\n\ts_mov_b64 exec, %[vm]\n\tglobal_load_dwordx4 v[108:111], %[voff], s[42:43] offset:3072 sc1\n\t"
        "global_load_dwordx4 v[124:127], %[voff], s[42:43] offset:3584 sc1\n\ts_mov_b64 exec, -1\n\tv_exp_f32 v53, v53\n\tv_exp_f32 v54, v54\n\tv_exp_f32 v55, v55\n\tv_exp_f32 v56, v56\n\t"
        "v_exp_f32 v57, v57\n\ts_waitcnt vmcnt(9)\n\tv_smfmac_f32_32x32x32_bf16 %[o0], v[96:99], v[48:55], v128\n\tv_exp_f32 v58, v58\n\tv_exp_f32 v59, v59\n\tv_exp_f32 v60, v60\n\t"
        "v_exp_f32 v61, v61\n\tv_exp_f32 v62, v62\n\tv_exp_f32 v63, v63\n\tv_exp_f32 v64, v64\n\tv_exp_f32 v65, v65\n\tv_smfmac_f32_32x32x32_bf16 %[o0], v[112:115], v[56:63], v128\n\t"
        "v_exp_f32 v66, v66\n\tv_exp_f32 v67, v67\n\tv_exp_f32 v68, v68\n\tv_mfma_f32_32x32x16_bf16 v[48:63], v[84:87], %[q0], 0\n\tv_exp_f32 v69, v69\n\tv_exp_f32 v70, v70\n\t"
        "v_exp_f32 v71, v71\n\tv_exp_f32 v72, v72\n\tv_exp_f32 v73, v73\n\tv_smfmac_f32_32x32x32_bf16 %[o1], v[96:99], v[64:71], v128\n\tv_exp_f32 v74, v74\n\tv_exp_f32 v75, v75\n\t"
        "v_exp_f32 v76, v76\n\tv_exp_f32 v77, v77\n\tv_exp_f32 v78, v78\n\tv_exp_f32 v79, v79\n\tv_exp_f32 v48, v48\n\tv_exp_f32 v49, v49\n\t"
        "v_smfmac_f32_32x32x32_bf16 %[o1], v[112:115], v[72:79], v128\n\tv_exp_f32 v50, v50\n\tv_exp_f32 v51, v51\n\tv_exp_f32 v52, v52\n\t"
        "v_mfma_f32_32x32x16_bf16 v[64:79], v[84:87], %[q1], 0\n\tglobal_load_dwordx4 v[84:87], %[koff], s[40:41] offset:1024 sc1\n\ts_add_u32 s42, s42, 4096\n\ts_addc_u32 s43, s43, 0\n\t"
        "s_mov_b64 exec, %[vm]\n\tglobal_load_dwordx4 v[96:99], %[voff], s[42:43] offset:0 sc1\n\tglobal_load_dwordx4 v[112:115], %[voff], s[42:43] offset:512 sc1\n\ts_mov_b64 exec, -1\n\t"
        "v_exp_f32 v53, v53\n\tv_exp_f32 v54, v54\n\tv_exp_f32 v55, v55\n\tv_exp_f32 v56, v56\n\tv_exp_f32 v57, v57\n\ts_waitcnt vmcnt(9)\n\t"
        "v_smfmac_f32_32x32x32_bf16 %[o0], v[100:103], v[48:55], v128\n\tv_exp_f32 v58, v58\n\tv_exp_f32 v59, v59\n\tv_exp_f32 v60, v60\n\tv_exp_f32 v61, v61\n\tv_exp_f32 v62, v62\n\t"
        "v_exp_f32 v63, v63\n\tv_exp_f32 v64, v64\n\tv_exp_f32 v65, v65\n\tv_smfmac_f32_32x32x32_bf16 %[o0], v[116:119], v[56:63], v128\n\tv_exp_f32 v66, v66\n\tv_exp_f32 v67, v67\n\t"
        "v_exp_f32 v68, v68\n\tv_mfma_f32_32x32x16_bf16 v[48:63], v[88:91], %[q0], 0\n\tv_exp_f32 v69, v69\n\tv_exp_f32 v70, v70\n\tv_exp_f32 v71, v71\n\tv_exp_f32 v72, v72\n\t"
        "v_exp_f32 v73, v73\n\tv_smfmac_f32_32x32x32_bf16 %[o1], v[100:103], v[64:71], v128\n\tv_exp_f32 v74, v74\n\tv_exp_f32 v75, v75\n\tv_exp_f32 v76, v76\n\tv_exp_f32 v77, v77\n\t"
        "v_exp_f32 v78, v78\n\tv_exp_f32 v79, v79\n\tv_exp_f32 v48, v48\n\tv_exp_f32 v49, v49\n\tv_smfmac_f32_32x32x32_bf16 %[o1], v[116:119], v[72:79], v128\n\tv_exp_f32 v50, v50\n\t"
        "v_exp_f32 v51, v51\n\tv_exp_f32 v52, v52\n\tv_mfma_f32_32x32x16_bf16 v[64:79], v[88:91], %[q1], 0\n\tglobal_load_dwordx4 v[88:91], %[koff], s[40:41] offset:2048 sc1\n\t"
        "s_mov_b64 exec, %[vm]\n\tglobal_load_dwordx4 v[100:103], %[voff], s[42:43] offset:1024 sc1\n\tglobal_load_dwordx4 v[116:119], %[voff], s[42:43] offset:1536 sc1\n\t"
        "s_mov_b64 exec, -1\n\tv_exp_f32 v53, v53\n\tv_exp_f32 v54, v54\n\tv_exp_f32 v55, v55\n\tv_exp_f32 v56, v56\n\tv_exp_f32 v57, v57\n\ts_waitcnt vmcnt(9)\n\t"
        "v_smfmac_f32_32x32x32_bf16 %[o0], v[104:107], v[48:55], v128\n\tv_exp_f32 v58, v58\n\tv_exp_f32 v59, v59\n\tv_exp_f32 v60, v60\n\tv_exp_f32 v61, v61\n\tv_exp_f32 v62, v62\n\t"
        "v_exp_f32 v63, v63\n\tv_exp_f32 v64, v64\n\tv_exp_f32 v65, v65\n\tv_smfmac_f32_32x32x32_bf16 %[o0], v[120:123], v[56:63], v128\n\tv_exp_f32 v66, v66\n\tv_exp_f32 v67, v67\n\t"
        "v_exp_f32 v68, v68\n\tv_mfma_f32_32x32x16_bf16 v[48:63], v[92:95], %[q0], 0\n\tv_exp_f32 v69, v69\n\tv_exp_f32 v70, v70\n\tv_exp_f32 v71, v71\n\tv_exp_f32 v72, v72\n\t"
        "v_exp_f32 v73, v73\n\tv_smfmac_f32_32x32x32_bf16 %[o1], v[104:107], v[64:71], v128\n\tv_exp_f32 v74, v74\n\tv_exp_f32 v75, v75\n\tv_exp_f32 v76, v76\n\tv_exp_f32 v77, v77\n\t"
        "v_exp_f32 v78, v78\n\tv_exp_f32 v79, v79\n\tv_exp_f32 v48, v48\n\tv_exp_f32 v49, v49\n\tv_smfmac_f32_32x32x32_bf16 %[o1], v[120:123], v[72:79], v128\n\tv_exp_f32 v50, v50\n\t"
        "v_exp_f32 v51, v51\n\tv_exp_f32 v52, v52\n\tv_mfma_f32_32x32x16_bf16 v[64:79], v[92:95], %[q1], 0\n\tglobal_load_dwordx4 v[92:95], %[koff], s[40:41] offset:3072 sc1\n\t"
        "s_mov_b64 exec, %[vm]\n\tglobal_load_dwordx4 v[104:107], %[voff], s[42:43] offset:2048 sc1\n\tglobal_load_dwordx4 v[120:123], %[voff], s[42:43] offset:2560 sc1\n\t"
        "s_mov_b64 exec, -1\n\tv_exp_f32 v53, v53\n\tv_exp_f32 v54, v54\n\tv_exp_f32 v55, v55\n\tv_exp_f32 v56, v56\n\tv_exp_f32 v57, v57\n\ts_waitcnt vmcnt(9)\n\t"
        "v_smfmac_f32_32x32x32_bf16 %[o0], v[108:111], v[48:55], v128\n\tv_exp_f32 v58, v58\n\tv_exp_f32 v59, v59\n\tv_exp_f32 v60, v60\n\tv_exp_f32 v61, v61\n\tv_exp_f32 v62, v62\n\t"
        "v_exp_f32 v63, v63\n\tv_exp_f32 v64, v64\n\tv_exp_f32 v65, v65\n\tv_smfmac_f32_32x32x32_bf16 %[o0], v[124:127], v[56:63], v128\n\tv_exp_f32 v66, v66\n\tv_exp_f32 v67, v67\n\t"
        "v_exp_f32 v68, v68\n\tv_mfma_f32_32x32x16_bf16 v[48:63], v[80:83], %[q0], 0\n\tglobal_load_dword v253, v[254:255], off\n\tv_exp_f32 v69, v69\n\tv_exp_f32 v70, v70\n\t"
        "v_exp_f32 v71, v71\n\tv_exp_f32 v72, v72\n\tv_exp_f32 v73, v73\n\tv_smfmac_f32_32x32x32_bf16 %[o1], v[108:111], v[64:71], v128\n\tv_exp_f32 v74, v74\n\tv_exp_f32 v75, v75\n\t"
        "v_exp_f32 v76, v76\n\tv_exp_f32 v77, v77\n\tv_exp_f32 v78, v78\n\tv_exp_f32 v79, v79\n\tv_exp_f32 v48, v48\n\tv_exp_f32 v49, v49\n\t"
        "v_smfmac_f32_32x32x32_bf16 %[o1], v[124:127], v[72:79], v128\n\tv_exp_f32 v50, v50\n\tv_exp_f32 v51, v51\n\tv_exp_f32 v52, v52\n\t"
        "v_mfma_f32_32x32x16_bf16 v[64:79], v[80:83], %[q1], 0\n\ts_add_u32 s40, s40, 4096\n\ts_addc_u32 s41, s41, 0\n\tglobal_load_dwordx4 v[80:83], %[koff], s[40:41] offset:0 sc1\n\t"
        "s_mov_b64 exec, %[vm]\n\tglobal_load_dwordx4 v[108:111], %[voff], s[42:43] offset:3072 sc1\n\tglobal_load_dwordx4 v[124:127], %[voff], s[42:43] offset:3584 sc1\n\t"
        "s_mov_b64 exec, -1\n\tv_exp_f32 v53, v53\n\tv_exp_f32 v54, v54\n\tv_exp_f32 v55, v55\n\tv_exp_f32 v56, v56\n\tv_exp_f32 v57, v57\n\ts_waitcnt vmcnt(10)\n\t"
        "v_smfmac_f32_32x32x32_bf16 %[o0], v[96:99], v[48:55], v128\n\tv_exp_f32 v58, v58\n\tv_exp_f32 v59, v59\n\tv_exp_f32 v60, v60\n\tv_exp_f32 v61, v61\n\tv_exp_f32 v62, v62\n\t"
        "v_exp_f32 v63, v63\n\tv_exp_f32 v64, v64\n\tv_exp_f32 v65, v65\n\tv_smfmac_f32_32x32x32_bf16 %[o0], v[112:115], v[56:63], v128\n\tv_exp_f32 v66, v66\n\tv_exp_f32 v67, v67\n\t"
        "v_exp_f32 v68, v68\n\tv_mfma_f32_32x32x16_bf16 v[48:63], v[84:87], %[q0], 0\n\tv_exp_f32 v69, v69\n\tv_exp_f32 v70, v70\n\tv_exp_f32 v71, v71\n\tv_exp_f32 v72, v72\n\t"
        "v_exp_f32 v73, v73\n\tv_smfmac_f32_32x32x32_bf16 %[o1], v[96:99], v[64:71], v128\n\tv_exp_f32 v74, v74\n\tv_exp_f32 v75, v75\n\tv_exp_f32 v76, v76\n\tv_exp_f32 v77, v77\n\t"
        "v_exp_f32 v78, v78\n\tv_exp_f32 v79, v79\n\tv_exp_f32 v48, v48\n\tv_exp_f32 v49, v49\n\tv_smfmac_f32_32x32x32_bf16 %[o1], v[112:115], v[72:79], v128\n\tv_exp_f32 v50, v50\n\t"
        "v_exp_f32 v51, v51\n\tv_exp_f32 v52, v52\n\tv_mfma_f32_32x32x16_bf16 v[64:79], v[84:87], %[q1], 0\n\tglobal_load_dwordx4 v[84:87], %[koff], s[40:41] offset:1024 sc1\n\t"
        "s_add_u32 s42, s42, 4096\n\ts_addc_u32 s43, s43, 0\n\ts_mov_b64 exec, %[vm]\n\tglobal_load_dwordx4 v[96:99], %[voff], s[42:43] offset:0 sc1\n\t"
        "global_load_dwordx4 v[112:115], %[voff], s[42:43] offset:512 sc1\n\ts_mov_b64 exec, -1\n\tv_exp_f32 v53, v53\n\tv_exp_f32 v54, v54\n\tv_exp_f32 v55, v55\n\tv_exp_f32 v56, v56\n\t"
        "v_exp_f32 v57, v57\n\ts_waitcnt vmcnt(10)\n\tv_smfmac_f32_32x32x32_bf16 %[o0], v[100:103], v[48:55], v128\n\tv_exp_f32 v58, v58\n\tv_exp_f32 v59, v59\n\tv_exp_f32 v60, v60\n\t"
        "v_exp_f32 v61, v61\n\tv_exp_f32 v62, v62\n\tv_exp_f32 v63, v63\n\tv_exp_f32 v64, v64\n\tv_exp_f32 v65, v65\n\tv_smfmac_f32_32x32x32_bf16 %[o0], v[116:119], v[56:63], v128\n\t"
        "v_exp_f32 v66, v66\n\tv_exp_f32 v67, v67\n\tv_exp_f32 v68, v68\n\tv_mfma_f32_32x32x16_bf16 v[48:63], v[88:91], %[q0], 0\n\tv_exp_f32 v69, v69\n\tv_exp_f32 v70, v70\n\t"
        "v_exp_f32 v71, v71\n\tv_exp_f32 v72, v72\n\tv_exp_f32 v73, v73\n\tv_smfmac_f32_32x32x32_bf16 %[o1], v[100:103], v[64:71], v128\n\tv_exp_f32 v74, v74\n\tv_exp_f32 v75, v75\n\t"
        "v_exp_f32 v76, v76\n\tv_exp_f32 v77, v77\n\tv_exp_f32 v78, v78\n\tv_exp_f32 v79, v79\n\tv_exp_f32 v48, v48\n\tv_exp_f32 v49, v49\n\t"
        "v_smfmac_f32_32x32x32_bf16 %[o1], v[116:119], v[72:79], v128\n\tv_exp_f32 v50, v50\n\tv_exp_f32 v51, v51\n\tv_exp_f32 v52, v52\n\t"
        "v_mfma_f32_32x32x16_bf16 v[64:79], v[88:91], %[q1], 0\n\tglobal_load_dwordx4 v[88:91], %[koff], s[40:41] offset:2048 sc1\n\ts_mov_b64 exec, %[vm]\n\t"
        "global_load_dwordx4 v[100:103], %[voff], s[42:43] offset:1024 sc1\n\tglobal_load_dwordx4 v[116:119], %[voff], s[42:43] offset:1536 sc1\n\ts_mov_b64 exec, -1\n\t"
        "v_exp_f32 v53, v53\n\tv_exp_f32 v54, v54\n\tv_exp_f32 v55, v55\n\tv_exp_f32 v56, v56\n\tv_exp_f32 v57, v57\n\ts_waitcnt vmcnt(10)\n\t"
        "v_smfmac_f32_32x32x32_bf16 %[o0], v[104:107], v[48:55], v128\n\tv_exp_f32 v58, v58\n\tv_exp_f32 v59, v59\n\tv_exp_f32 v60, v60\n\tv_exp_f32 v61, v61\n\tv_exp_f32 v62, v62\n\t"
        "v_exp_f32 v63, v63\n\tv_exp_f32 v64, v64\n\tv_exp_f32 v65, v65\n\tv_smfmac_f32_32x32x32_bf16 %[o0], v[120:123], v[56:63], v128\n\tv_exp_f32 v66, v66\n\tv_exp_f32 v67, v67\n\t"
        "v_exp_f32 v68, v68\n\tv_mfma_f32_32x32x16_bf16 v[48:63], v[92:95], %[q0], 0\n\tv_exp_f32 v69, v69\n\tv_exp_f32 v70, v70\n\tv_exp_f32 v71, v71\n\tv_exp_f32 v72, v72\n\t"
        "v_exp_f32 v73, v73\n\tv_smfmac_f32_32x32x32_bf16 %[o1], v[104:107], v[64:71], v128\n\tv_exp_f32 v74, v74\n\tv_exp_f32 v75, v75\n\tv_exp_f32 v76, v76\n\tv_exp_f32 v77, v77\n\t"
        "v_exp_f32 v78, v78\n\tv_exp_f32 v79, v79\n\tv_exp_f32 v48, v48\n\tv_exp_f32 v49, v49\n\tv_smfmac_f32_32x32x32_bf16 %[o1], v[120:123], v[72:79], v128\n\tv_exp_f32 v50, v50\n\t"
        "v_exp_f32 v51, v51\n\tv_exp_f32 v52, v52\n\tv_mfma_f32_32x32x16_bf16 v[64:79], v[92:95], %[q1], 0\n\tglobal_load_dwordx4 v[92:95], %[koff], s[40:41] offset:3072 sc1\n\t"
        "s_mov_b64 exec, %[vm]\n\tglobal_load_dwordx4 v[104:107], %[voff], s[42:43] offset:2048 sc1\n\tglobal_load_dwordx4 v[120:123], %[voff], s[42:43] offset:2560 sc1\n\t"
        "s_mov_b64 exec, -1\n\tv_exp_f32 v53, v53\n\tv_exp_f32 v54, v54\n\tv_exp_f32 v55, v55\n\tv_exp_f32 v56, v56\n\tv_exp_f32 v57, v57\n\ts_waitcnt vmcnt(9)\n\t"
        "v_smfmac_f32_32x32x32_bf16 %[o0], v[108:111], v[48:55], v128\n\tv_exp_f32 v58, v58\n\tv_exp_f32 v59, v59\n\tv_exp_f32 v60, v60\n\tv_exp_f32 v61, v61\n\tv_exp_f32 v62, v62\n\t"
        "v_exp_f32 v63, v63\n\tv_exp_f32 v64, v64\n\tv_exp_f32 v65, v65\n\tv_smfmac_f32_32x32x32_bf16 %[o0], v[124:127], v[56:63], v128\n\tv_exp_f32 v66, v66\n\tv_exp_f32 v67, v67\n\t"
        "v_exp_f32 v68, v68\n\tv_mfma_f32_32x32x16_bf16 v[48:63], v[80:83], %[q0], 0\n\tv_exp_f32 v69, v69\n\tv_exp_f32 v70, v70\n\tv_exp_f32 v71, v71\n\tv_exp_f32 v72, v72\n\t"
        "v_exp_f32 v73, v73\n\tv_smfmac_f32_32x32x32_bf16 %[o1], v[108:111], v[64:71], v128\n\tv_exp_f32 v74, v74\n\tv_exp_f32 v75, v75\n\tv_exp_f32 v76, v76\n\tv_exp_f32 v77, v77\n\t"
        "v_exp_f32 v78, v78\n\tv_exp_f32 v79, v79\n\tv_exp_f32 v48, v48\n\tv_exp_f32 v49, v49\n\tv_smfmac_f32_32x32x32_bf16 %[o1], v[124:127], v[72:79], v128\n\tv_exp_f32 v50, v50\n\t"
        "v_exp_f32 v51, v51\n\tv_exp_f32 v52, v52\n\tv_mfma_f32_32x32x16_bf16 v[64:79], v[80:83], %[q1], 0\n\ts_add_u32 s40, s40, 4096\n\ts_addc_u32 s41, s41, 0\n\t"
        "global_load_dwordx4 v[80:83], %[koff], s[40:41] offset:0 sc1\n\ts_mov_b64 exec, %[vm]\n\tglobal_load_dwordx4 v[108:111], %[voff], s[42:43] offset:3072 sc1\n\t"
        "global_load_dwordx4 v[124:127], %[voff], s[42:43] offset:3584 sc1\n\ts_mov_b64 exec, -1\n\tv_exp_f32 v53, v53\n\tv_exp_f32 v54, v54\n\tv_exp_f32 v55, v55\n\tv_exp_f32 v56, v56\n\t"
        "v_exp_f32 v57, v57\n\ts_waitcnt vmcnt(9)\n\tv_smfmac_f32_32x32x32_bf16 %[o0], v[96:99], v[48:55], v128\n\tv_exp_f32 v58, v58\n\tv_exp_f32 v59, v59\n\tv_exp_f32 v60, v60\n\t"
        "v_exp_f32 v61, v61\n\tv_exp_f32 v62, v62\n\tv_exp_f32 v63, v63\n\tv_exp_f32 v64, v64\n\tv_exp_f32 v65, v65\n\tv_smfmac_f32_32x32x32_bf16 %[o0], v[112:115], v[56:63], v128\n\t"
        "v_exp_f32 v66, v66\n\tv_exp_f32 v67, v67\n\tv_exp_f32 v68, v68\n\tv_mfma_f32_32x32x16_bf16 v[48:63], v[84:87], %[q0], 0\n\tglobal_load_dwordx4 v[144:147], %[a0], s[46:47]\n\t"
        "v_exp_f32 v69, v69\n\tv_exp_f32 v70, v70\n\tv_exp_f32 v71, v71\n\tv_exp_f32 v72, v72\n\tv_exp_f32 v73, v73\n\tv_smfmac_f32_32x32x32_bf16 %[o1], v[96:99], v[64:71], v128\n\t"
        "v_exp_f32 v74, v74\n\tv_exp_f32 v75, v75\n\tv_exp_f32 v76, v76\n\tv_exp_f32 v77, v77\n\tv_exp_f32 v78, v78\n\tv_exp_f32 v79, v79\n\tv_exp_f32 v48, v48\n\tv_exp_f32 v49, v49\n\t"
        "v_smfmac_f32_32x32x32_bf16 %[o1], v[112:115], v[72:79], v128\n\tv_exp_f32 v50, v50\n\tv_exp_f32 v51, v51\n\tv_exp_f32 v52, v52\n\t"
        "v_mfma_f32_32x32x16_bf16 v[64:79], v[84:87], %[q1], 0\n\tglobal_load_dwordx4 v[84:87], %[koff], s[40:41] offset:1024 sc1\n\ts_add_u32 s42, s42, 4096\n\ts_addc_u32 s43, s43, 0\n\t"
        "s_mov_b64 exec, %[vm]\n\tglobal_load_dwordx4 v[96:99], %[voff], s[42:43] offset:0 sc1\n\tglobal_load_dwordx4 v[112:115], %[voff], s[42:43] offset:512 sc1\n\ts_mov_b64 exec, -1\n\t"
        "v_exp_f32 v53, v53\n\tv_exp_f32 v54, v54\n\tv_exp_f32 v55, v55\n\tv_exp_f32 v56, v56\n\tv_exp_f32 v57, v57\n\ts_waitcnt vmcnt(10)\n\t"
        "v_smfmac_f32_32x32x32_bf16 %[o0], v[100:103], v[48:55], v128\n\tv_exp_f32 v58, v58\n\tv_exp_f32 v59, v59\n\tv_exp_f32 v60, v60\n\tv_exp_f32 v61, v61\n\tv_exp_f32 v62, v62\n\t"
        "v_exp_f32 v63, v63\n\tv_exp_f32 v64, v64\n\tv_exp_f32 v65, v65\n\tv_smfmac_f32_32x32x32_bf16 %[o0], v[116:119], v[56:63], v128\n\tv_exp_f32 v66, v66\n\tv_exp_f32 v67, v67\n\t"
        "v_exp_f32 v68, v68\n\tv_mfma_f32_32x32x16_bf16 v[48:63], v[88:91], %[q0], 0\n\ts_add_u32 m0, %[ol], 0\n\ts_add_u32 s50, s50, 8192\n\ts_addc_u32 s51, s51, 0\n\t"
        "global_load_lds_dwordx4 %[vw], s[50:51]\n\tglobal_load_dwordx4 v[148:151], %[a1], s[46:47]\n\tv_exp_f32 v69, v69\n\tv_exp_f32 v70, v70\n\tv_exp_f32 v71, v71\n\t"
        "v_exp_f32 v72, v72\n\tv_exp_f32 v73, v73\n\tv_smfmac_f32_32x32x32_bf16 %[o1], v[100:103], v[64:71], v128\n\tv_exp_f32 v74, v74\n\tv_exp_f32 v75, v75\n\tv_exp_f32 v76, v76\n\t"
        "v_exp_f32 v77, v77\n\tv_exp_f32 v78, v78\n\tv_exp_f32 v79, v79\n\tv_exp_f32 v48, v48\n\tv_exp_f32 v49, v49\n\tv_smfmac_f32_32x32x32_bf16 %[o1], v[116:119], v[72:79], v128\n\t"
        "v_exp_f32 v50, v50\n\tv_exp_f32 v51, v51\n\tv_exp_f32 v52, v52\n\tv_mfma_f32_32x32x16_bf16 v[64:79], v[88:91], %[q1], 0\n\t"
        "global_load_dwordx4 v[88:91], %[koff], s[40:41] offset:2048 sc1\n\ts_mov_b64 exec, %[vm]\n\tglobal_load_dwordx4 v[100:103], %[voff], s[42:43] offset:1024 sc1\n\t"
        "global_load_dwordx4 v[116:119], %[voff], s[42:43] offset:1536 sc1\n\ts_mov_b64 exec, -1\n\tv_exp_f32 v53, v53\n\tv_exp_f32 v54, v54\n\tv_exp_f32 v55, v55\n\tv_exp_f32 v56, v56\n\t"
        "v_exp_f32 v57, v57\n\ts_waitcnt vmcnt(12)\n\tv_smfmac_f32_32x32x32_bf16 %[o0], v[104:107], v[48:55], v128\n\tv_exp_f32 v58, v58\n\tv_exp_f32 v59, v59\n\tv_exp_f32 v60, v60\n\t"
        "v_exp_f32 v61, v61\n\tv_exp_f32 v62, v62\n\tv_exp_f32 v63, v63\n\tv_exp_f32 v64, v64\n\tv_exp_f32 v65, v65\n\tv_smfmac_f32_32x32x32_bf16 %[o0], v[120:123], v[56:63], v128\n\t"
        "v_exp_f32 v66, v66\n\tv_exp_f32 v67, v67\n\tv_exp_f32 v68, v68\n\tv_mfma_f32_32x32x16_bf16 v[48:63], v[92:95], %[q0], 0\n\tglobal_load_dwordx4 v[152:155], %[a2], s[46:47]\n\t"
        "s_add_u32 s46, s46, %[ga]\n\ts_addc_u32 s47, s47, 0\n\ts_add_u32 m0, %[ol], 1024\n\ts_add_u32 s50, s50, 8192\n\ts_addc_u32 s51, s51, 0\n\t"
        "global_load_lds_dwordx4 %[vw], s[50:51]\n\tv_exp_f32 v69, v69\n\tv_exp_f32 v70, v70\n\tv_exp_f32 v71, v71\n\tv_exp_f32 v72, v72\n\tv_exp_f32 v73, v73\n\t"
        "v_smfmac_f32_32x32x32_bf16 %[o1], v[104:107], v[64:71], v128\n\tv_exp_f32 v74, v74\n\tv_exp_f32 v75, v75\n\tv_exp_f32 v76, v76\n\tv_exp_f32 v77, v77\n\tv_exp_f32 v78, v78\n\t"
        "v_exp_f32 v79, v79\n\tv_exp_f32 v48, v48\n\tv_exp_f32 v49, v49\n\tv_smfmac_f32_32x32x32_bf16 %[o1], v[120:123], v[72:79], v128\n\tv_exp_f32 v50, v50\n\tv_exp_f32 v51, v51\n\t"
        "v_exp_f32 v52, v52\n\tv_mfma_f32_32x32x16_bf16 v[64:79], v[92:95], %[q1], 0\n\tglobal_load_dwordx4 v[92:95], %[koff], s[40:41] offset:3072 sc1\n\ts_mov_b64 exec, %[vm]\n\t"
        "global_load_dwordx4 v[104:107], %[voff], s[42:43] offset:2048 sc1\n\tglobal_load_dwordx4 v[120:123], %[voff], s[42:43] offset:2560 sc1\n\ts_mov_b64 exec, -1\n\t"
        "v_exp_f32 v53, v53\n\tv_exp_f32 v54, v54\n\tv_exp_f32 v55, v55\n\tv_exp_f32 v56, v56\n\tv_exp_f32 v57, v57\n\ts_waitcnt vmcnt(14)\n\t"
        "v_smfmac_f32_32x32x32_bf16 %[o0], v[108:111], v[48:55], v128\n\tv_exp_f32 v58, v58\n\tv_exp_f32 v59, v59\n\tv_exp_f32 v60, v60\n\tv_exp_f32 v61, v61\n\tv_exp_f32 v62, v62\n\t"
        "v_exp_f32 v63, v63\n\tv_exp_f32 v64, v64\n\tv_exp_f32 v65, v65\n\tv_smfmac_f32_32x32x32_bf16 %[o0], v[124:127], v[56:63], v128\n\tv_exp_f32 v66, v66\n\tv_exp_f32 v67, v67\n\t"
        "v_exp_f32 v68, v68\n\tv_mfma_f32_32x32x16_bf16 v[48:63], v[80:83], %[q0], 0\n\tglobal_load_dwordx4 v[156:159], %[a0], s[46:47]\n\t"
        "global_load_dwordx4 v[160:163], %[a1], s[46:47]\n\tv_exp_f32 v69, v69\n\tv_exp_f32 v70, v70\n\tv_exp_f32 v71, v71\n\tv_exp_f32 v72, v72\n\tv_exp_f32 v73, v73\n\t"
        "v_smfmac_f32_32x32x32_bf16 %[o1], v[108:111], v[64:71], v128\n\tv_exp_f32 v74, v74\n\tv_exp_f32 v75, v75\n\tv_exp_f32 v76, v76\n\tv_exp_f32 v77, v77\n\tv_exp_f32 v78, v78\n\t"
        "v_exp_f32 v79, v79\n\tv_exp_f32 v48, v48\n\tv_exp_f32 v49, v49\n\tv_smfmac_f32_32x32x32_bf16 %[o1], v[124:127], v[72:79], v128\n\tv_exp_f32 v50, v50\n\tv_exp_f32 v51, v51\n\t"
        "v_exp_f32 v52, v52\n\tv_mfma_f32_32x32x16_bf16 v[64:79], v[80:83], %[q1], 0\n\ts_add_u32 s40, s40, 4096\n\ts_addc_u32 s41, s41, 0\n\t"
        "global_load_dwordx4 v[80:83], %[koff], s[40:41] offset:0 sc1\n\ts_mov_b64 exec, %[vm]\n\tglobal_load_dwordx4 v[108:111], %[voff], s[42:43] offset:3072 sc1\n\t"
        "global_load_dwordx4 v[124:127], %[voff], s[42:43] offset:3584 sc1\n\ts_mov_b64 exec, -1\n\tv_exp_f32 v53, v53\n\tv_exp_f32 v54, v54\n\tv_exp_f32 v55, v55\n\tv_exp_f32 v56, v56\n\t"
        "v_exp_f32 v57, v57\n\ts_waitcnt vmcnt(15)\n\tv_smfmac_f32_32x32x32_bf16 %[o0], v[96:99], v[48:55], v128\n\tv_exp_f32 v58, v58\n\tv_exp_f32 v59, v59\n\tv_exp_f32 v60, v60\n\t"
        "v_exp_f32 v61, v61\n\tv_exp_f32 v62, v62\n\tv_exp_f32 v63, v63\n\tv_exp_f32 v64, v64\n\tv_exp_f32 v65, v65\n\tv_smfmac_f32_32x32x32_bf16 %[o0], v[112:115], v[56:63], v128\n\t"
        "v_exp_f32 v66, v66\n\tv_exp_f32 v67, v67\n\tv_exp_f32 v68, v68\n\tv_mfma_f32_32x32x16_bf16 v[48:63], v[84:87], %[q0], 0\n\ts_add_u32 m0, %[ol], 2048\n\ts_add_u32 s50, s50, 8192\n\t"
        "s_addc_u32 s51, s51, 0\n\tglobal_load_lds_dwordx4 %[vw], s[50:51]\n\tglobal_load_dwordx4 v[164:167], %[a2], s[46:47]\n\ts_add_u32 s46, s46, %[ga]\n\ts_addc_u32 s47, s47, 0\n\t"
        "v_exp_f32 v69, v69\n\tv_exp_f32 v70, v70\n\tv_exp_f32 v71, v71\n\tv_exp_f32 v72, v72\n\tv_exp_f32 v73, v73\n\tv_smfmac_f32_32x32x32_bf16 %[o1], v[96:99], v[64:71], v128\n\t"
        "v_exp_f32 v74, v74\n\tv_exp_f32 v75, v75\n\tv_exp_f32 v76, v76\n\tv_exp_f32 v77, v77\n\tv_exp_f32 v78, v78\n\tv_exp_f32 v79, v79\n\tv_exp_f32 v48, v48\n\tv_exp_f32 v49, v49\n\t"
        "v_smfmac_f32_32x32x32_bf16 %[o1], v[112:115], v[72:79], v128\n\tv_exp_f32 v50, v50\n\tv_exp_f32 v51, v51\n\tv_exp_f32 v52, v52\n\t"
        "v_mfma_f32_32x32x16_bf16 v[64:79], v[84:87], %[q1], 0\n\tglobal_load_dwordx4 v[84:87], %[koff], s[40:41] offset:1024 sc1\n\ts_add_u32 s42, s42, 4096\n\ts_addc_u32 s43, s43, 0\n\t"
        "s_mov_b64 exec, %[vm]\n\tglobal_load_dwordx4 v[96:99], %[voff], s[42:43] offset:0 sc1\n\tglobal_load_dwordx4 v[112:115], %[voff], s[42:43] offset:512 sc1\n\ts_mov_b64 exec, -1\n\t"
        "v_exp_f32 v53, v53\n\tv_exp_f32 v54, v54\n\tv_exp_f32 v55, v55\n\tv_exp_f32 v56, v56\n\tv_exp_f32 v57, v57\n\ts_waitcnt vmcnt(15)\n\t"
        "v_smfmac_f32_32x32x32_bf16 %[o0], v[100:103], v[48:55], v128\n\tv_exp_f32 v58, v58\n\tv_exp_f32 v59, v59\n\tv_exp_f32 v60, v60\n\tv_exp_f32 v61, v61\n\tv_exp_f32 v62, v62\n\t"
        "v_exp_f32 v63, v63\n\tv_exp_f32 v64, v64\n\tv_exp_f32 v65, v65\n\tv_smfmac_f32_32x32x32_bf16 %[o0], v[116:119], v[56:63], v128\n\tv_exp_f32 v66, v66\n\tv_exp_f32 v67, v67\n\t"
        "v_exp_f32 v68, v68\n\tv_mfma_f32_32x32x16_bf16 v[48:63], v[88:91], %[q0], 0\n\tglobal_load_dwordx4 v[168:171], %[a0], s[46:47]\n\ts_add_u32 m0, %[ol], 3072\n\t"
        "s_add_u32 s50, s50, 8192\n\ts_addc_u32 s51, s51, 0\n\tglobal_load_lds_dwordx4 %[vw], s[50:51]\n\tv_exp_f32 v69, v69\n\tv_exp_f32 v70, v70\n\tv_exp_f32 v71, v71\n\t"
        "v_exp_f32 v72, v72\n\tv_exp_f32 v73, v73\n\tv_smfmac_f32_32x32x32_bf16 %[o1], v[100:103], v[64:71], v128\n\tv_exp_f32 v74, v74\n\tv_exp_f32 v75, v75\n\tv_exp_f32 v76, v76\n\t"
        "v_exp_f32 v77, v77\n\tv_exp_f32 v78, v78\n\tv_exp_f32 v79, v79\n\tv_exp_f32 v48, v48\n\tv_exp_f32 v49, v49\n\tv_smfmac_f32_32x32x32_bf16 %[o1], v[116:119], v[72:79], v128\n\t"
        "v_exp_f32 v50, v50\n\tv_exp_f32 v51, v51\n\tv_exp_f32 v52, v52\n\tv_mfma_f32_32x32x16_bf16 v[64:79], v[88:91], %[q1], 0\n\t"
        "global_load_dwordx4 v[88:91], %[koff], s[40:41] offset:2048 sc1\n\ts_mov_b64 exec, %[vm]\n\tglobal_load_dwordx4 v[100:103], %[voff], s[42:43] offset:1024 sc1\n\t"
        "global_load_dwordx4 v[116:119], %[voff], s[42:43] offset:1536 sc1\n\ts_mov_b64 exec, -1\n\tv_exp_f32 v53, v53\n\tv_exp_f32 v54, v54\n\tv_exp_f32 v55, v55\n\tv_exp_f32 v56, v56\n\t"
        "v_exp_f32 v57, v57\n\ts_waitcnt vmcnt(15)\n\tv_smfmac_f32_32x32x32_bf16 %[o0], v[104:107], v[48:55], v128\n\tv_exp_f32 v58, v58\n\tv_exp_f32 v59, v59\n\tv_exp_f32 v60, v60\n\t"
        "v_exp_f32 v61, v61\n\tv_exp_f32 v62, v62\n\tv_exp_f32 v63, v63\n\tv_exp_f32 v64, v64\n\tv_exp_f32 v65, v65\n\tv_smfmac_f32_32x32x32_bf16 %[o0], v[120:123], v[56:63], v128\n\t"
        "v_exp_f32 v66, v66\n\tv_exp_f32 v67, v67\n\tv_exp_f32 v68, v68\n\tv_mfma_f32_32x32x16_bf16 v[48:63], v[92:95], %[q0], 0\n\tglobal_load_dwordx4 v[172:175], %[a1], s[46:47]\n\t"
        "global_load_dwordx4 v[176:179], %[a2], s[46:47]\n\ts_add_u32 s46, s46, %[ga]\n\ts_addc_u32 s47, s47, 0\n\tv_exp_f32 v69, v69\n\tv_exp_f32 v70, v70\n\tv_exp_f32 v71, v71\n\t"
        "v_exp_f32 v72, v72\n\tv_exp_f32 v73, v73\n\tv_smfmac_f32_32x32x32_bf16 %[o1], v[104:107], v[64:71], v128\n\tv_exp_f32 v74, v74\n\tv_exp_f32 v75, v75\n\tv_exp_f32 v76, v76\n\t"
        "v_exp_f32 v77, v77\n\tv_exp_f32 v78, v78\n\tv_exp_f32 v79, v79\n\tv_exp_f32 v48, v48\n\tv_exp_f32 v49, v49\n\tv_smfmac_f32_32x32x32_bf16 %[o1], v[120:123], v[72:79], v128\n\t"
        "v_exp_f32 v50, v50\n\tv_exp_f32 v51, v51\n\tv_exp_f32 v52, v52\n\tv_mfma_f32_32x32x16_bf16 v[64:79], v[92:95], %[q1], 0\n\t"
        "global_load_dwordx4 v[92:95], %[koff], s[40:41] offset:3072 sc1\n\ts_mov_b64 exec, %[vm]\n\tglobal_load_dwordx4 v[104:107], %[voff], s[42:43] offset:2048 sc1\n\t"
        "global_load_dwordx4 v[120:123], %[voff], s[42:43] offset:2560 sc1\n\ts_mov_b64 exec, -1\n\tv_exp_f32 v53, v53\n\tv_exp_f32 v54, v54\n\tv_exp_f32 v55, v55\n\tv_exp_f32 v56, v56\n\t"
        "v_exp_f32 v57, v57\n\ts_waitcnt vmcnt(15)\n\tv_smfmac_f32_32x32x32_bf16 %[o0], v[108:111], v[48:55], v128\n\tv_exp_f32 v58, v58\n\tv_exp_f32 v59, v59\n\tv_exp_f32 v60, v60\n\t"
        "v_exp_f32 v61, v61\n\tv_exp_f32 v62, v62\n\tv_exp_f32 v63, v63\n\tv_exp_f32 v64, v64\n\tv_exp_f32 v65, v65\n\tv_smfmac_f32_32x32x32_bf16 %[o0], v[124:127], v[56:63], v128\n\t"
        "v_exp_f32 v66, v66\n\tv_exp_f32 v67, v67\n\tv_exp_f32 v68, v68\n\tv_mfma_f32_32x32x16_bf16 v[48:63], v[80:83], %[q0], 0\n\ts_add_u32 m0, %[ol], 4096\n\ts_add_u32 s50, s50, 8192\n\t"
        "s_addc_u32 s51, s51, 0\n\tglobal_load_lds_dwordx4 %[vw], s[50:51]\n\tglobal_load_dwordx4 v[180:183], %[a0], s[46:47]\n\tv_exp_f32 v69, v69\n\tv_exp_f32 v70, v70\n\t"
        "v_exp_f32 v71, v71\n\tv_exp_f32 v72, v72\n\tv_exp_f32 v73, v73\n\tv_smfmac_f32_32x32x32_bf16 %[o1], v[108:111], v[64:71], v128\n\tv_exp_f32 v74, v74\n\tv_exp_f32 v75, v75\n\t"
        "v_exp_f32 v76, v76\n\tv_exp_f32 v77, v77\n\tv_exp_f32 v78, v78\n\tv_exp_f32 v79, v79\n\tv_exp_f32 v48, v48\n\tv_exp_f32 v49, v49\n\t"
        "v_smfmac_f32_32x32x32_bf16 %[o1], v[124:127], v[72:79], v128\n\tv_exp_f32 v50, v50\n\tv_exp_f32 v51, v51\n\tv_exp_f32 v52, v52\n\t"
        "v_mfma_f32_32x32x16_bf16 v[64:79], v[80:83], %[q1], 0\n\ts_add_u32 s40, s40, 4096\n\ts_addc_u32 s41, s41, 0\n\tglobal_load_dwordx4 v[80:83], %[koff], s[40:41] offset:0 sc1\n\t"
        "s_mov_b64 exec, %[vm]\n\tglobal_load_dwordx4 v[108:111], %[voff], s[42:43] offset:3072 sc1\n\tglobal_load_dwordx4 v[124:127], %[voff], s[42:43] offset:3584 sc1\n\t"
        "s_mov_b64 exec, -1\n\tv_exp_f32 v53, v53\n\tv_exp_f32 v54, v54\n\tv_exp_f32 v55, v55\n\tv_exp_f32 v56, v56\n\tv_exp_f32 v57, v57\n\ts_waitcnt vmcnt(15)\n\t"
        "v_smfmac_f32_32x32x32_bf16 %[o0], v[96:99], v[48:55], v128\n\tv_exp_f32 v58, v58\n\tv_exp_f32 v59, v59\n\tv_exp_f32 v60, v60\n\tv_exp_f32 v61, v61\n\tv_exp_f32 v62, v62\n\t"
        "v_exp_f32 v63, v63\n\tv_exp_f32 v64, v64\n\tv_exp_f32 v65, v65\n\tv_smfmac_f32_32x32x32_bf16 %[o0], v[112:115], v[56:63], v128\n\tv_exp_f32 v66, v66\n\tv_exp_f32 v67, v67\n\t"
        "v_exp_f32 v68, v68\n\tv_mfma_f32_32x32x16_bf16 v[48:63], v[84:87], %[q0], 0\n\tglobal_load_dwordx4 v[184:187], %[a1], s[46:47]\n\ts_add_u32 m0, %[ol], 5120\n\t"
        "s_add_u32 s50, s50, 8192\n\ts_addc_u32 s51, s51, 0\n\tglobal_load_lds_dwordx4 %[vw], s[50:51]\n\tv_exp_f32 v69, v69\n\tv_exp_f32 v70, v70\n\tv_exp_f32 v71, v71\n\t"
        "v_exp_f32 v72, v72\n\tv_exp_f32 v73, v73\n\tv_smfmac_f32_32x32x32_bf16 %[o1], v[96:99], v[64:71], v128\n\tv_exp_f32 v74, v74\n\tv_exp_f32 v75, v75\n\tv_exp_f32 v76, v76\n\t"
        "v_exp_f32 v77, v77\n\tv_exp_f32 v78, v78\n\tv_exp_f32 v79, v79\n\tv_exp_f32 v48, v48\n\tv_exp_f32 v49, v49\n\tv_smfmac_f32_32x32x32_bf16 %[o1], v[112:115], v[72:79], v128\n\t"
        "v_exp_f32 v50, v50\n\tv_exp_f32 v51, v51\n\tv_exp_f32 v52, v52\n\tv_mfma_f32_32x32x16_bf16 v[64:79], v[84:87], %[q1], 0\n\t"
        "global_load_dwordx4 v[84:87], %[koff], s[40:41] offset:1024 sc1\n\ts_add_u32 s42, s42, 4096\n\ts_addc_u32 s43, s43, 0\n\ts_mov_b64 exec, %[vm]\n\t"
        "global_load_dwordx4 v[96:99], %[voff], s[42:43] offset:0 sc1\n\tglobal_load_dwordx4 v[112:115], %[voff], s[42:43] offset:512 sc1\n\ts_mov_b64 exec, -1\n\tv_exp_f32 v53, v53\n\t"
        "v_exp_f32 v54, v54\n\tv_exp_f32 v55, v55\n\tv_exp_f32 v56, v56\n\tv_exp_f32 v57, v57\n\ts_waitcnt vmcnt(15)\n\tv_smfmac_f32_32x32x32_bf16 %[o0], v[100:103], v[48:55], v128\n\t"
        "v_exp_f32 v58, v58\n\tv_exp_f32 v59, v59\n\tv_exp_f32 v60, v60\n\tv_exp_f32 v61, v61\n\tv_exp_f32 v62, v62\n\tv_exp_f32 v63, v63\n\tv_exp_f32 v64, v64\n\tv_exp_f32 v65, v65\n\t"
        "v_smfmac_f32_32x32x32_bf16 %[o0], v[116:119], v[56:63], v128\n\tv_exp_f32 v66, v66\n\tv_exp_f32 v67, v67\n\tv_exp_f32 v68, v68\n\t"
        "v_mfma_f32_32x32x16_bf16 v[48:63], v[88:91], %[q0], 0\n\tglobal_load_dwordx4 v[188:191], %[a2], s[46:47]\n\ts_add_u32 s46, s46, %[ga]\n\ts_addc_u32 s47, s47, 0\n\t"
        "global_load_dwordx4 v[192:195], %[vw], s[48:49]\n\ts_add_u32 s48, s48, 8192\n\ts_addc_u32 s49, s49, 0\n\tv_exp_f32 v69, v69\n\tv_exp_f32 v70, v70\n\tv_exp_f32 v71, v71\n\t"
        "v_exp_f32 v72, v72\n\tv_exp_f32 v73, v73\n\tv_smfmac_f32_32x32x32_bf16 %[o1], v[100:103], v[64:71], v128\n\tv_exp_f32 v74, v74\n\tv_exp_f32 v75, v75\n\tv_exp_f32 v76, v76\n\t"
        "v_exp_f32 v77, v77\n\tv_exp_f32 v78, v78\n\tv_exp_f32 v79, v79\n\tv_exp_f32 v48, v48\n\tv_exp_f32 v49, v49\n\tv_smfmac_f32_32x32x32_bf16 %[o1], v[116:119], v[72:79], v128\n\t"
        "v_exp_f32 v50, v50\n\tv_exp_f32 v51, v51\n\tv_exp_f32 v52, v52\n\tv_mfma_f32_32x32x16_bf16 v[64:79], v[88:91], %[q1], 0\n\t"
        "global_load_dwordx4 v[88:91], %[koff], s[40:41] offset:2048 sc1\n\ts_mov_b64 exec, %[vm]\n\tglobal_load_dwordx4 v[100:103], %[voff], s[42:43] offset:1024 sc1\n\t"
        "global_load_dwordx4 v[116:119], %[voff], s[42:43] offset:1536 sc1\n\ts_mov_b64 exec, -1\n\tv_exp_f32 v53, v53\n\tv_exp_f32 v54, v54\n\tv_exp_f32 v55, v55\n\tv_exp_f32 v56, v56\n\t"
        "v_exp_f32 v57, v57\n\ts_waitcnt vmcnt(15)\n\tv_smfmac_f32_32x32x32_bf16 %[o0], v[104:107], v[48:55], v128\n\tv_exp_f32 v58, v58\n\tv_exp_f32 v59, v59\n\tv_exp_f32 v60, v60\n\t"
        "v_exp_f32 v61, v61\n\tv_exp_f32 v62, v62\n\tv_exp_f32 v63, v63\n\tv_exp_f32 v64, v64\n\tv_exp_f32 v65, v65\n\tv_smfmac_f32_32x32x32_bf16 %[o0], v[120:123], v[56:63], v128\n\t"
        "v_exp_f32 v66, v66\n\tv_exp_f32 v67, v67\n\tv_exp_f32 v68, v68\n\tv_mfma_f32_32x32x16_bf16 v[48:63], v[92:95], %[q0], 0\n\ts_add_u32 m0, %[ol], 6144\n\ts_add_u32 s50, s50, 8192\n\t"
        "s_addc_u32 s51, s51, 0\n\tglobal_load_lds_dwordx4 %[vw], s[50:51]\n\tglobal_load_dwordx4 v[196:199], %[vw], s[48:49]\n\ts_add_u32 s48, s48, 8192\n\ts_addc_u32 s49, s49, 0\n\t"
        "v_exp_f32 v69, v69\n\tv_exp_f32 v70, v70\n\tv_exp_f32 v71, v71\n\tv_exp_f32 v72, v72\n\tv_exp_f32 v73, v73\n\tv_smfmac_f32_32x32x32_bf16 %[o1], v[104:107], v[64:71], v128\n\t"
        "v_exp_f32 v74, v74\n\tv_exp_f32 v75, v75\n\tv_exp_f32 v76, v76\n\tv_exp_f32 v77, v77\n\tv_exp_f32 v78, v78\n\tv_exp_f32 v79, v79\n\tv_exp_f32 v48, v48\n\tv_exp_f32 v49, v49\n\t"
        "v_smfmac_f32_32x32x32_bf16 %[o1], v[120:123], v[72:79], v128\n\tv_exp_f32 v50, v50\n\tv_exp_f32 v51, v51\n\tv_exp_f32 v52, v52\n\t"
        "v_mfma_f32_32x32x16_bf16 v[64:79], v[92:95], %[q1], 0\n\tglobal_load_dwordx4 v[92:95], %[koff], s[40:41] offset:3072 sc1\n\ts_mov_b64 exec, %[vm]\n\t"
        "global_load_dwordx4 v[104:107], %[voff], s[42:43] offset:2048 sc1\n\tglobal_load_dwordx4 v[120:123], %[voff], s[42:43] offset:2560 sc1\n\ts_mov_b64 exec, -1\n\t"
        "v_exp_f32 v53, v53\n\tv_exp_f32 v54, v54\n\tv_exp_f32 v55, v55\n\tv_exp_f32 v56, v56\n\tv_exp_f32 v57, v57\n\ts_waitcnt vmcnt(15)\n\t"
        "v_smfmac_f32_32x32x32_bf16 %[o0], v[108:111], v[48:55], v128\n\tv_exp_f32 v58, v58\n\tv_exp_f32 v59, v59\n\tv_exp_f32 v60, v60\n\tv_exp_f32 v61, v61\n\tv_exp_f32 v62, v62\n\t"
        "v_exp_f32 v63, v63\n\tv_exp_f32 v64, v64\n\tv_exp_f32 v65, v65\n\tv_smfmac_f32_32x32x32_bf16 %[o0], v[124:127], v[56:63], v128\n\tv_exp_f32 v66, v66\n\tv_exp_f32 v67, v67\n\t"
        "v_exp_f32 v68, v68\n\tv_mfma_f32_32x32x16_bf16 v[48:63], v[80:83], %[q0], 0\n\tglobal_load_dwordx4 v[200:203], %[vw], s[48:49]\n\ts_add_u32 s48, s48, 8192\n\t"
        "s_addc_u32 s49, s49, 0\n\tv_exp_f32 v69, v69\n\tv_exp_f32 v70, v70\n\tv_exp_f32 v71, v71\n\tv_exp_f32 v72, v72\n\tv_exp_f32 v73, v73\n\t"
        "v_smfmac_f32_32x32x32_bf16 %[o1], v[108:111], v[64:71], v128\n\tv_exp_f32 v74, v74\n\tv_exp_f32 v75, v75\n\tv_exp_f32 v76, v76\n\tv_exp_f32 v77, v77\n\tv_exp_f32 v78, v78\n\t"
        "v_exp_f32 v79, v79\n\tv_exp_f32 v48, v48\n\tv_exp_f32 v49, v49\n\tv_smfmac_f32_32x32x32_bf16 %[o1], v[124:127], v[72:79], v128\n\tv_exp_f32 v50, v50\n\tv_exp_f32 v51, v51\n\t"
        "v_exp_f32 v52, v52\n\tv_mfma_f32_32x32x16_bf16 v[64:79], v[80:83], %[q1], 0\n\ts_add_u32 s40, s40, 4096\n\ts_addc_u32 s41, s41, 0\n\t"
        "global_load_dwordx4 v[80:83], %[koff], s[40:41] offset:0 sc1\n\ts_mov_b64 exec, %[vm]\n\tglobal_load_dwordx4 v[108:111], %[voff], s[42:43] offset:3072 sc1\n\t"
        "global_load_dwordx4 v[124:127], %[voff], s[42:43] offset:3584 sc1\n\ts_mov_b64 exec, -1\n\tv_exp_f32 v53, v53\n\tv_exp_f32 v54, v54\n\tv_exp_f32 v55, v55\n\tv_exp_f32 v56, v56\n\t"
        "v_exp_f32 v57, v57\n\ts_waitcnt vmcnt(14)\n\tv_smfmac_f32_32x32x32_bf16 %[o0], v[96:99], v[48:55], v128\n\tv_exp_f32 v58, v58\n\tv_exp_f32 v59, v59\n\tv_exp_f32 v60, v60\n\t"
        "v_exp_f32 v61, v61\n\tv_exp_f32 v62, v62\n\tv_exp_f32 v63, v63\n\tv_exp_f32 v64, v64\n\tv_exp_f32 v65, v65\n\tv_smfmac_f32_32x32x32_bf16 %[o0], v[112:115], v[56:63], v128\n\t"
        "v_exp_f32 v66, v66\n\tv_exp_f32 v67, v67\n\tv_exp_f32 v68, v68\n\tv_mfma_f32_32x32x16_bf16 v[48:63], v[84:87], %[q0], 0\n\ts_add_u32 m0, %[ol], 7168\n\ts_add_u32 s50, s50, 8192\n\t"
        "s_addc_u32 s51, s51, 0\n\tglobal_load_lds_dwordx4 %[vw], s[50:51]\n\tglobal_load_dwordx4 v[204:207], %[vw], s[48:49]\n\ts_add_u32 s48, s48, 8192\n\ts_addc_u32 s49, s49, 0\n\t"
        "v_exp_f32 v69, v69\n\tv_exp_f32 v70, v70\n\tv_exp_f32 v71, v71\n\tv_exp_f32 v72, v72\n\tv_exp_f32 v73, v73\n\tv_smfmac_f32_32x32x32_bf16 %[o1], v[96:99], v[64:71], v128\n\t"
        "v_exp_f32 v74, v74\n\tv_exp_f32 v75, v75\n\tv_exp_f32 v76, v76\n\tv_exp_f32 v77, v77\n\tv_exp_f32 v78, v78\n\tv_exp_f32 v79, v79\n\tv_exp_f32 v48, v48\n\tv_exp_f32 v49, v49\n\t"
        "v_smfmac_f32_32x32x32_bf16 %[o1], v[112:115], v[72:79], v128\n\tv_exp_f32 v50, v50\n\tv_exp_f32 v51, v51\n\tv_exp_f32 v52, v52\n\t"
        "v_mfma_f32_32x32x16_bf16 v[64:79], v[84:87], %[q1], 0\n\tglobal_load_dwordx4 v[84:87], %[koff], s[40:41] offset:1024 sc1\n\ts_add_u32 s42, s42, 4096\n\ts_addc_u32 s43, s43, 0\n\t"
        "s_mov_b64 exec, %[vm]\n\tglobal_load_dwordx4 v[96:99], %[voff], s[42:43] offset:0 sc1\n\tglobal_load_dwordx4 v[112:115], %[voff], s[42:43] offset:512 sc1\n\ts_mov_b64 exec, -1\n\t"
        "v_exp_f32 v53, v53\n\tv_exp_f32 v54, v54\n\tv_exp_f32 v55, v55\n\tv_exp_f32 v56, v56\n\tv_exp_f32 v57, v57\n\ts_waitcnt vmcnt(14)\n\t"
        "v_smfmac_f32_32x32x32_bf16 %[o0], v[100:103], v[48:55], v128\n\tv_exp_f32 v58, v58\n\tv_exp_f32 v59, v59\n\tv_exp_f32 v60, v60\n\tv_exp_f32 v61, v61\n\tv_exp_f32 v62, v62\n\t"
        "v_exp_f32 v63, v63\n\tv_exp_f32 v64, v64\n\tv_exp_f32 v65, v65\n\tv_smfmac_f32_32x32x32_bf16 %[o0], v[116:119], v[56:63], v128\n\tv_exp_f32 v66, v66\n\tv_exp_f32 v67, v67\n\t"
        "v_exp_f32 v68, v68\n\tv_mfma_f32_32x32x16_bf16 v[48:63], v[88:91], %[q0], 0\n\tglobal_load_dwordx4 v[208:211], %[vw], s[48:49]\n\ts_add_u32 s48, s48, 8192\n\t"
        "s_addc_u32 s49, s49, 0\n\ts_add_u32 m0, %[hl], 0\n\ts_nop 0\n\tglobal_load_lds_dwordx4 %[b0], s[52:53]\n\tv_exp_f32 v69, v69\n\tv_exp_f32 v70, v70\n\tv_exp_f32 v71, v71\n\t"
        "v_exp_f32 v72, v72\n\tv_exp_f32 v73, v73\n\tv_smfmac_f32_32x32x32_bf16 %[o1], v[100:103], v[64:71], v128\n\tv_exp_f32 v74, v74\n\tv_exp_f32 v75, v75\n\tv_exp_f32 v76, v76\n\t"
        "v_exp_f32 v77, v77\n\tv_exp_f32 v78, v78\n\tv_exp_f32 v79, v79\n\tv_exp_f32 v48, v48\n\tv_exp_f32 v49, v49\n\tv_smfmac_f32_32x32x32_bf16 %[o1], v[116:119], v[72:79], v128\n\t"
        "v_exp_f32 v50, v50\n\tv_exp_f32 v51, v51\n\tv_exp_f32 v52, v52\n\tv_mfma_f32_32x32x16_bf16 v[64:79], v[88:91], %[q1], 0\n\t"
        "global_load_dwordx4 v[88:91], %[koff], s[40:41] offset:2048 sc1\n\ts_mov_b64 exec, %[vm]\n\tglobal_load_dwordx4 v[100:103], %[voff], s[42:43] offset:1024 sc1\n\t"
        "global_load_dwordx4 v[116:119], %[voff], s[42:43] offset:1536 sc1\n\ts_mov_b64 exec, -1\n\tv_exp_f32 v53, v53\n\tv_exp_f32 v54, v54\n\tv_exp_f32 v55, v55\n\tv_exp_f32 v56, v56\n\t"
        "v_exp_f32 v57, v57\n\ts_waitcnt vmcnt(14)\n\tv_smfmac_f32_32x32x32_bf16 %[o0], v[104:107], v[48:55], v128\n\tv_exp_f32 v58, v58\n\tv_exp_f32 v59, v59\n\tv_exp_f32 v60, v60\n\t"
        "v_exp_f32 v61, v61\n\tv_exp_f32 v62, v62\n\tv_exp_f32 v63, v63\n\tv_exp_f32 v64, v64\n\tv_exp_f32 v65, v65\n\tv_smfmac_f32_32x32x32_bf16 %[o0], v[120:123], v[56:63], v128\n\t"
        "v_exp_f32 v66, v66\n\tv_exp_f32 v67, v67\n\tv_exp_f32 v68, v68\n\tv_mfma_f32_32x32x16_bf16 v[48:63], v[92:95], %[q0], 0\n\tglobal_load_dwordx4 v[212:215], %[vw], s[48:49]\n\t"
        "s_add_u32 s48, s48, 8192\n\ts_addc_u32 s49, s49, 0\n\tglobal_load_dwordx4 v[216:219], %[vw], s[48:49]\n\ts_add_u32 s48, s48, 8192\n\ts_addc_u32 s49, s49, 0\n\t"
        "v_exp_f32 v69, v69\n\tv_exp_f32 v70, v70\n\tv_exp_f32 v71, v71\n\tv_exp_f32 v72, v72\n\tv_exp_f32 v73, v73\n\tv_smfmac_f32_32x32x32_bf16 %[o1], v[104:107], v[64:71], v128\n\t"
        "v_exp_f32 v74, v74\n\tv_exp_f32 v75, v75\n\tv_exp_f32 v76, v76\n\tv_exp_f32 v77, v77\n\tv_exp_f32 v78, v78\n\tv_exp_f32 v79, v79\n\tv_exp_f32 v48, v48\n\tv_exp_f32 v49, v49\n\t"
        "v_smfmac_f32_32x32x32_bf16 %[o1], v[120:123], v[72:79], v128\n\tv_exp_f32 v50, v50\n\tv_exp_f32 v51, v51\n\tv_exp_f32 v52, v52\n\t"
        "v_mfma_f32_32x32x16_bf16 v[64:79], v[92:95], %[q1], 0\n\tglobal_load_dwordx4 v[92:95], %[koff], s[40:41] offset:3072 sc1\n\ts_mov_b64 exec, %[vm]\n\t"
        "global_load_dwordx4 v[104:107], %[voff], s[42:43] offset:2048 sc1\n\tglobal_load_dwordx4 v[120:123], %[voff], s[42:43] offset:2560 sc1\n\ts_mov_b64 exec, -1\n\t"
        "v_exp_f32 v53, v53\n\tv_exp_f32 v54, v54\n\tv_exp_f32 v55, v55\n\tv_exp_f32 v56, v56\n\tv_exp_f32 v57, v57\n\ts_waitcnt vmcnt(15)\n\t"
        "v_smfmac_f32_32x32x32_bf16 %[o0], v[108:111], v[48:55], v128\n\tv_exp_f32 v58, v58\n\tv_exp_f32 v59, v59\n\tv_exp_f32 v60, v60\n\tv_exp_f32 v61, v61\n\tv_exp_f32 v62, v62\n\t"
        "v_exp_f32 v63, v63\n\tv_exp_f32 v64, v64\n\tv_exp_f32 v65, v65\n\tv_smfmac_f32_32x32x32_bf16 %[o0], v[124:127], v[56:63], v128\n\tv_exp_f32 v66, v66\n\tv_exp_f32 v67, v67\n\t"
        "v_exp_f32 v68, v68\n\tv_mfma_f32_32x32x16_bf16 v[48:63], v[80:83], %[q0], 0\n\ts_add_u32 m0, %[hl], 1024\n\ts_nop 0\n\tglobal_load_lds_dwordx4 %[b1], s[52:53]\n\t"
        "global_load_dwordx4 v[220:223], %[vw], s[48:49]\n\ts_add_u32 s48, s48, 8192\n\ts_addc_u32 s49, s49, 0\n\tv_exp_f32 v69, v69\n\tv_exp_f32 v70, v70\n\tv_exp_f32 v71, v71\n\t"
        "v_exp_f32 v72, v72\n\tv_exp_f32 v73, v73\n\tv_smfmac_f32_32x32x32_bf16 %[o1], v[108:111], v[64:71], v128\n\tv_exp_f32 v74, v74\n\tv_exp_f32 v75, v75\n\tv_exp_f32 v76, v76\n\t"
        "v_exp_f32 v77, v77\n\tv_exp_f32 v78, v78\n\tv_exp_f32 v79, v79\n\tv_exp_f32 v48, v48\n\tv_exp_f32 v49, v49\n\tv_smfmac_f32_32x32x32_bf16 %[o1], v[124:127], v[72:79], v128\n\t"
        "v_exp_f32 v50, v50\n\tv_exp_f32 v51, v51\n\tv_exp_f32 v52, v52\n\tv_mfma_f32_32x32x16_bf16 v[64:79], v[80:83], %[q1], 0\n\ts_add_u32 s40, s40, 4096\n\ts_addc_u32 s41, s41, 0\n\t"
        "global_load_dwordx4 v[80:83], %[koff], s[40:41] offset:0 sc1\n\ts_mov_b64 exec, %[vm]\n\tglobal_load_dwordx4 v[108:111], %[voff], s[42:43] offset:3072 sc1\n\t"
        "global_load_dwordx4 v[124:127], %[voff], s[42:43] offset:3584 sc1\n\ts_mov_b64 exec, -1\n\tv_exp_f32 v53, v53\n\tv_exp_f32 v54, v54\n\tv_exp_f32 v55, v55\n\tv_exp_f32 v56, v56\n\t"
        "v_exp_f32 v57, v57\n\ts_waitcnt vmcnt(15)\n\tv_smfmac_f32_32x32x32_bf16 %[o0], v[96:99], v[48:55], v128\n\tv_exp_f32 v58, v58\n\tv_exp_f32 v59, v59\n\tv_exp_f32 v60, v60\n\t"
        "v_exp_f32 v61, v61\n\tv_exp_f32 v62, v62\n\tv_exp_f32 v63, v63\n\tv_exp_f32 v64, v64\n\tv_exp_f32 v65, v65\n\tv_smfmac_f32_32x32x32_bf16 %[o0], v[112:115], v[56:63], v128\n\t"
        "v_exp_f32 v66, v66\n\tv_exp_f32 v67, v67\n\tv_exp_f32 v68, v68\n\tv_mfma_f32_32x32x16_bf16 v[48:63], v[84:87], %[q0], 0\n\tglobal_load_dwordx4 v[224:227], %[vw], s[48:49]\n\t"
        "s_add_u32 s48, s48, 8192\n\ts_addc_u32 s49, s49, 0\n\ts_add_u32 m0, %[hl], 2048\n\ts_nop 0\n\tglobal_load_lds_dwordx4 %[b2], s[52:53]\n\tv_exp_f32 v69, v69\n\t"
        "v_exp_f32 v70, v70\n\tv_exp_f32 v71, v71\n\tv_exp_f32 v72, v72\n\tv_exp_f32 v73, v73\n\tv_smfmac_f32_32x32x32_bf16 %[o1], v[96:99], v[64:71], v128\n\tv_exp_f32 v74, v74\n\t"
        "v_exp_f32 v75, v75\n\tv_exp_f32 v76, v76\n\tv_exp_f32 v77, v77\n\tv_exp_f32 v78, v78\n\tv_exp_f32 v79, v79\n\tv_exp_f32 v48, v48\n\tv_exp_f32 v49, v49\n\t"
        "v_smfmac_f32_32x32x32_bf16 %[o1], v[112:115], v[72:79], v128\n\tv_exp_f32 v50, v50\n\tv_exp_f32 v51, v51\n\tv_exp_f32 v52, v52\n\t"
        "v_mfma_f32_32x32x16_bf16 v[64:79], v[84:87], %[q1], 0\n\tglobal_load_dwordx4 v[84:87], %[koff], s[40:41] offset:1024 sc1\n\ts_add_u32 s42, s42, 4096\n\ts_addc_u32 s43, s43, 0\n\t"
        "s_mov_b64 exec, %[vm]\n\tglobal_load_dwordx4 v[96:99], %[voff], s[42:43] offset:0 sc1\n\tglobal_load_dwordx4 v[112:115], %[voff], s[42:43] offset:512 sc1\n\ts_mov_b64 exec, -1\n\t"
        "v_exp_f32 v53, v53\n\tv_exp_f32 v54, v54\n\tv_exp_f32 v55, v55\n\tv_exp_f32 v56, v56\n\tv_exp_f32 v57, v57\n\ts_waitcnt vmcnt(15)\n\t"
        "v_smfmac_f32_32x32x32_bf16 %[o0], v[100:103], v[48:55], v128\n\tv_exp_f32 v58, v58\n\tv_exp_f32 v59, v59\n\tv_exp_f32 v60, v60\n\tv_exp_f32 v61, v61\n\tv_exp_f32 v62, v62\n\t"
        "v_exp_f32 v63, v63\n\tv_exp_f32 v64, v64\n\tv_exp_f32 v65, v65\n\tv_smfmac_f32_32x32x32_bf16 %[o0], v[116:119], v[56:63], v128\n\tv_exp_f32 v66, v66\n\tv_exp_f32 v67, v67\n\t"
        "v_exp_f32 v68, v68\n\tv_mfma_f32_32x32x16_bf16 v[48:63], v[88:91], %[q0], 0\n\tglobal_load_dwordx4 v[228:231], %[vw], s[48:49]\n\ts_add_u32 s48, s48, 8192\n\t"
        "s_addc_u32 s49, s49, 0\n\tglobal_load_dwordx4 v[232:235], %[vw], s[48:49]\n\ts_add_u32 s48, s48, 8192\n\ts_addc_u32 s49, s49, 0\n\tv_exp_f32 v69, v69\n\tv_exp_f32 v70, v70\n\t"
        "v_exp_f32 v71, v71\n\tv_exp_f32 v72, v72\n\tv_exp_f32 v73, v73\n\tv_smfmac_f32_32x32x32_bf16 %[o1], v[100:103], v[64:71], v128\n\tv_exp_f32 v74, v74\n\tv_exp_f32 v75, v75\n\t"
        "v_exp_f32 v76, v76\n\tv_exp_f32 v77, v77\n\tv_exp_f32 v78, v78\n\tv_exp_f32 v79, v79\n\tv_exp_f32 v48, v48\n\tv_exp_f32 v49, v49\n\t"
        "v_smfmac_f32_32x32x32_bf16 %[o1], v[116:119], v[72:79], v128\n\tv_exp_f32 v50, v50\n\tv_exp_f32 v51, v51\n\tv_exp_f32 v52, v52\n\t"
        "v_mfma_f32_32x32x16_bf16 v[64:79], v[88:91], %[q1], 0\n\tglobal_load_dwordx4 v[88:91], %[koff], s[40:41] offset:2048 sc1\n\ts_mov_b64 exec, %[vm]\n\t"
        "global_load_dwordx4 v[100:103], %[voff], s[42:43] offset:1024 sc1\n\tglobal_load_dwordx4 v[116:119], %[voff], s[42:43] offset:1536 sc1\n\ts_mov_b64 exec, -1\n\t"
        "v_exp_f32 v53, v53\n\tv_exp_f32 v54, v54\n\tv_exp_f32 v55, v55\n\tv_exp_f32 v56, v56\n\tv_exp_f32 v57, v57\n\ts_waitcnt vmcnt(15)\n\t"
        "v_smfmac_f32_32x32x32_bf16 %[o0], v[104:107], v[48:55], v128\n\tv_exp_f32 v58, v58\n\tv_exp_f32 v59, v59\n\tv_exp_f32 v60, v60\n\tv_exp_f32 v61, v61\n\tv_exp_f32 v62, v62\n\t"
        "v_exp_f32 v63, v63\n\tv_exp_f32 v64, v64\n\tv_exp_f32 v65, v65\n\tv_smfmac_f32_32x32x32_bf16 %[o0], v[120:123], v[56:63], v128\n\tv_exp_f32 v66, v66\n\tv_exp_f32 v67, v67\n\t"
        "v_exp_f32 v68, v68\n\tv_mfma_f32_32x32x16_bf16 v[48:63], v[92:95], %[q0], 0\n\ts_add_u32 m0, %[hl], 3072\n\ts_add_u32 s52, s52, 44032\n\ts_addc_u32 s53, s53, 0\n\t"
        "global_load_lds_dwordx4 %[b0], s[52:53]\n\tglobal_load_dwordx4 v[236:239], %[vw], s[48:49]\n\ts_add_u32 s48, s48, 8192\n\ts_addc_u32 s49, s49, 0\n\ts_cmp_eq_u32 %[drop], 0\n\t"
        "s_cbranch_scc1 2f\n\ts_setprio 0\n\t2:\n\tv_exp_f32 v69, v69\n\tv_exp_f32 v70, v70\n\tv_exp_f32 v71, v71\n\tv_exp_f32 v72, v72\n\tv_exp_f32 v73, v73\n\t"
        "v_smfmac_f32_32x32x32_bf16 %[o1], v[104:107], v[64:71], v128\n\tv_exp_f32 v74, v74\n\tv_exp_f32 v75, v75\n\tv_exp_f32 v76, v76\n\tv_exp_f32 v77, v77\n\tv_exp_f32 v78, v78\n\t"
        "v_exp_f32 v79, v79\n\tv_exp_f32 v48, v48\n\tv_exp_f32 v49, v49\n\tv_smfmac_f32_32x32x32_bf16 %[o1], v[120:123], v[72:79], v128\n\tv_exp_f32 v50, v50\n\tv_exp_f32 v51, v51\n\t"
        "v_exp_f32 v52, v52\n\tv_mfma_f32_32x32x16_bf16 v[64:79], v[92:95], %[q1], 0\n\tglobal_load_dwordx4 v[92:95], %[koff], s[40:41] offset:3072 sc1\n\ts_mov_b64 exec, %[vm]\n\t"
        "global_load_dwordx4 v[104:107], %[voff], s[42:43] offset:2048 sc1\n\tglobal_load_dwordx4 v[120:123], %[voff], s[42:43] offset:2560 sc1\n\ts_mov_b64 exec, -1\n\t"
        "v_exp_f32 v53, v53\n\tv_exp_f32 v54, v54\n\tv_exp_f32 v55, v55\n\tv_exp_f32 v56, v56\n\tv_exp_f32 v57, v57\n\ts_waitcnt vmcnt(15)\n\t"
        "v_smfmac_f32_32x32x32_bf16 %[o0], v[108:111], v[48:55], v128\n\tv_exp_f32 v58, v58\n\tv_exp_f32 v59, v59\n\tv_exp_f32 v60, v60\n\tv_exp_f32 v61, v61\n\tv_exp_f32 v62, v62\n\t"
        "v_exp_f32 v63, v63\n\tv_exp_f32 v64, v64\n\tv_exp_f32 v65, v65\n\tv_smfmac_f32_32x32x32_bf16 %[o0], v[124:127], v[56:63], v128\n\tv_exp_f32 v66, v66\n\tv_exp_f32 v67, v67\n\t"
        "v_exp_f32 v68, v68\n\tv_mfma_f32_32x32x16_bf16 v[48:63], v[80:83], %[q0], 0\n\tglobal_load_dwordx4 v[240:243], %[vw], s[48:49]\n\ts_add_u32 s48, s48, 8192\n\t"
        "s_addc_u32 s49, s49, 0\n\ts_add_u32 m0, %[hl], 4096\n\ts_nop 0\n\tglobal_load_lds_dwordx4 %[b1], s[52:53]\n\tv_exp_f32 v69, v69\n\tv_exp_f32 v70, v70\n\tv_exp_f32 v71, v71\n\t"
        "v_exp_f32 v72, v72\n\tv_exp_f32 v73, v73\n\tv_smfmac_f32_32x32x32_bf16 %[o1], v[108:111], v[64:71], v128\n\tv_exp_f32 v74, v74\n\tv_exp_f32 v75, v75\n\tv_exp_f32 v76, v76\n\t"
        "v_exp_f32 v77, v77\n\tv_exp_f32 v78, v78\n\tv_exp_f32 v79, v79\n\tv_exp_f32 v48, v48\n\tv_exp_f32 v49, v49\n\tv_smfmac_f32_32x32x32_bf16 %[o1], v[124:127], v[72:79], v128\n\t"
        "v_exp_f32 v50, v50\n\tv_exp_f32 v51, v51\n\tv_exp_f32 v52, v52\n\tv_mfma_f32_32x32x16_bf16 v[64:79], v[80:83], %[q1], 0\n\ts_mov_b64 exec, %[vm]\n\t"
        "global_load_dwordx4 v[108:111], %[voff], s[42:43] offset:3072 sc1\n\tglobal_load_dwordx4 v[124:127], %[voff], s[42:43] offset:3584 sc1\n\ts_mov_b64 exec, -1\n\t"
        "v_exp_f32 v53, v53\n\tv_exp_f32 v54, v54\n\tv_exp_f32 v55, v55\n\tv_exp_f32 v56, v56\n\tv_exp_f32 v57, v57\n\ts_waitcnt vmcnt(14)\n\t"
        "v_smfmac_f32_32x32x32_bf16 %[o0], v[96:99], v[48:55], v128\n\tv_exp_f32 v58, v58\n\tv_exp_f32 v59, v59\n\tv_exp_f32 v60, v60\n\tv_exp_f32 v61, v61\n\tv_exp_f32 v62, v62\n\t"
        "v_exp_f32 v63, v63\n\tv_exp_f32 v64, v64\n\tv_exp_f32 v65, v65\n\tv_smfmac_f32_32x32x32_bf16 %[o0], v[112:115], v[56:63], v128\n\tv_exp_f32 v66, v66\n\tv_exp_f32 v67, v67\n\t"
        "v_exp_f32 v68, v68\n\tv_mfma_f32_32x32x16_bf16 v[48:63], v[84:87], %[q0], 0\n\tglobal_load_dwordx4 v[244:247], %[vw], s[48:49]\n\ts_add_u32 s48, s48, 8192\n\t"
        "s_addc_u32 s49, s49, 0\n\tglobal_load_dwordx4 v[248:251], %[vw], s[48:49]\n\ts_add_u32 s48, s48, 8192\n\ts_addc_u32 s49, s49, 0\n\tv_exp_f32 v69, v69\n\tv_exp_f32 v70, v70\n\t"
        "v_exp_f32 v71, v71\n\tv_exp_f32 v72, v72\n\tv_exp_f32 v73, v73\n\tv_smfmac_f32_32x32x32_bf16 %[o1], v[96:99], v[64:71], v128\n\tv_exp_f32 v74, v74\n\tv_exp_f32 v75, v75\n\t"
        "v_exp_f32 v76, v76\n\tv_exp_f32 v77, v77\n\tv_exp_f32 v78, v78\n\tv_exp_f32 v79, v79\n\tv_exp_f32 v48, v48\n\tv_exp_f32 v49, v49\n\t"
        "v_smfmac_f32_32x32x32_bf16 %[o1], v[112:115], v[72:79], v128\n\tv_exp_f32 v50, v50\n\tv_exp_f32 v51, v51\n\tv_exp_f32 v52, v52\n\t"
        "v_mfma_f32_32x32x16_bf16 v[64:79], v[84:87], %[q1], 0\n\tv_exp_f32 v53, v53\n\tv_exp_f32 v54, v54\n\tv_exp_f32 v55, v55\n\tv_exp_f32 v56, v56\n\tv_exp_f32 v57, v57\n\t"
        "s_waitcnt vmcnt(11)\n\tv_smfmac_f32_32x32x32_bf16 %[o0], v[100:103], v[48:55], v128\n\tv_exp_f32 v58, v58\n\tv_exp_f32 v59, v59\n\tv_exp_f32 v60, v60\n\tv_exp_f32 v61, v61\n\t"
        "v_exp_f32 v62, v62\n\tv_exp_f32 v63, v63\n\tv_exp_f32 v64, v64\n\tv_exp_f32 v65, v65\n\tv_smfmac_f32_32x32x32_bf16 %[o0], v[116:119], v[56:63], v128\n\tv_exp_f32 v66, v66\n\t"
        "v_exp_f32 v67, v67\n\tv_exp_f32 v68, v68\n\tv_mfma_f32_32x32x16_bf16 v[48:63], v[88:91], %[q0], 0\n\ts_add_u32 m0, %[hl], 5120\n\ts_nop 0\n\t"
        "global_load_lds_dwordx4 %[b2], s[52:53]\n\tglobal_load_dwordx4 v[252:255], %[vw], s[48:49]\n\ts_add_u32 s48, s48, 8192\n\ts_addc_u32 s49, s49, 0\n\tv_exp_f32 v69, v69\n\t"
        "v_exp_f32 v70, v70\n\tv_exp_f32 v71, v71\n\tv_exp_f32 v72, v72\n\tv_exp_f32 v73, v73\n\tv_smfmac_f32_32x32x32_bf16 %[o1], v[100:103], v[64:71], v128\n\tv_exp_f32 v74, v74\n\t"
        "v_exp_f32 v75, v75\n\tv_exp_f32 v76, v76\n\tv_exp_f32 v77, v77\n\tv_exp_f32 v78, v78\n\tv_exp_f32 v79, v79\n\tv_exp_f32 v48, v48\n\tv_exp_f32 v49, v49\n\t"
        "v_smfmac_f32_32x32x32_bf16 %[o1], v[116:119], v[72:79], v128\n\tv_exp_f32 v50, v50\n\tv_exp_f32 v51, v51\n\tv_exp_f32 v52, v52\n\t"
        "v_mfma_f32_32x32x16_bf16 v[64:79], v[88:91], %[q1], 0\n\tv_exp_f32 v53, v53\n\tv_exp_f32 v54, v54\n\tv_exp_f32 v55, v55\n\tv_exp_f32 v56, v56\n\tv_exp_f32 v57, v57\n\t"
        "s_waitcnt vmcnt(8)\n\tv_smfmac_f32_32x32x32_bf16 %[o0], v[104:107], v[48:55], v128\n\tv_exp_f32 v58, v58\n\tv_exp_f32 v59, v59\n\tv_exp_f32 v60, v60\n\tv_exp_f32 v61, v61\n\t"
        "v_exp_f32 v62, v62\n\tv_exp_f32 v63, v63\n\tv_exp_f32 v64, v64\n\tv_exp_f32 v65, v65\n\tv_smfmac_f32_32x32x32_bf16 %[o0], v[120:123], v[56:63], v128\n\tv_exp_f32 v66, v66\n\t"
        "v_exp_f32 v67, v67\n\tv_exp_f32 v68, v68\n\tv_mfma_f32_32x32x16_bf16 v[48:63], v[92:95], %[q0], 0\n\tv_exp_f32 v69, v69\n\tv_exp_f32 v70, v70\n\tv_exp_f32 v71, v71\n\t"
        "v_exp_f32 v72, v72\n\tv_exp_f32 v73, v73\n\tv_smfmac_f32_32x32x32_bf16 %[o1], v[104:107], v[64:71], v128\n\tv_exp_f32 v74, v74\n\tv_exp_f32 v75, v75\n\tv_exp_f32 v76, v76\n\t"
        "v_exp_f32 v77, v77\n\tv_exp_f32 v78, v78\n\tv_exp_f32 v79, v79\n\tv_exp_f32 v48, v48\n\tv_exp_f32 v49, v49\n\tv_smfmac_f32_32x32x32_bf16 %[o1], v[120:123], v[72:79], v128\n\t"
        "v_exp_f32 v50, v50\n\tv_exp_f32 v51, v51\n\tv_exp_f32 v52, v52\n\tv_mfma_f32_32x32x16_bf16 v[64:79], v[92:95], %[q1], 0\n\tv_exp_f32 v53, v53\n\tv_exp_f32 v54, v54\n\t"
        "v_exp_f32 v55, v55\n\tv_exp_f32 v56, v56\n\tv_exp_f32 v57, v57\n\ts_waitcnt vmcnt(4)\n\tv_smfmac_f32_32x32x32_bf16 %[o0], v[108:111], v[48:55], v128\n\tv_exp_f32 v58, v58\n\t"
        "v_exp_f32 v59, v59\n\tv_exp_f32 v60, v60\n\tv_exp_f32 v61, v61\n\tv_exp_f32 v62, v62\n\tv_exp_f32 v63, v63\n\tv_exp_f32 v64, v64\n\tv_exp_f32 v65, v65\n\t"
        "v_smfmac_f32_32x32x32_bf16 %[o0], v[124:127], v[56:63], v128\n\tv_exp_f32 v66, v66\n\tv_exp_f32 v67, v67\n\tv_exp_f32 v68, v68\n\tv_exp_f32 v69, v69\n\tv_exp_f32 v70, v70\n\t"
        "v_exp_f32 v71, v71\n\tv_exp_f32 v72, v72\n\tv_exp_f32 v73, v73\n\tv_smfmac_f32_32x32x32_bf16 %[o1], v[108:111], v[64:71], v128\n\tv_exp_f32 v74, v74\n\tv_exp_f32 v75, v75\n\t"
        "v_exp_f32 v76, v76\n\tv_exp_f32 v77, v77\n\tv_exp_f32 v78, v78\n\tv_exp_f32 v79, v79\n\ts_setprio 0\n\ts_nop 0\n\tv_smfmac_f32_32x32x32_bf16 %[o1], v[124:127], v[72:79], v128\n\t"
        "s_waitcnt vmcnt(0)\n\ts_nop 15\n\t"
        : [o0] "+v"(o0), [o1] "+v"(o1), "=&{v[144:159]}"(sg[0]), "=&{v[160:175]}"(sg[1]), "=&{v[176:191]}"(sg[2]), "=&{v[192:207]}"(sg[3]), "=&{v[208:223]}"(sg[4]), "=&{v[224:239]}"(sg[5]), "+{v[240:255]}"(sg[6])
        : [q0] "v"(q0), [q1] "v"(q1), [koff] "v"(koff), [voff] "v"(voff), [vinit] "v"(vinit), [vm] "s"(0x0000ffff0000ffffull), [kin] "s"(kb), [vin] "s"(vb), [drop] "s"(drop),
          [a0] "v"(a0), [a1] "v"(a1), [a2] "v"(a2), [b0] "v"(v30), [b1] "v"(v31), [b2] "v"(v32), [vw] "v"(vw), [sa] "s"(sa), [sb] "s"(sb), [so] "s"(so), [sh] "s"(sh), [ga] "s"(ga), [ol] "s"(ol), [hl] "s"(hl)
        : "memory", "scc", "s40", "s41", "s42", "s43", "s46", "s47", "s48", "s49", "s50", "s51", "s52", "s53",
          "v48","v49","v50","v51","v52","v53","v54","v55","v56","v57","v58","v59","v60","v61","v62","v63","v64","v65","v66","v67","v68","v69","v70","v71","v72","v73","v74","v75","v76","v77","v78","v79",
          "v80","v81","v82","v83","v84","v85","v86","v87","v88","v89","v90","v91","v92","v93","v94","v95","v96","v97","v98","v99","v100","v101","v102","v103","v104","v105","v106","v107","v108","v109","v110","v111",
          "v112","v113","v114","v115","v116","v117","v118","v119","v120","v121","v122","v123","v124","v125","v126","v127","v128");
    }
    STAMP(7); STAMPC(27);
    lane = __builtin_amdgcn_mbcnt_hi(~0u, __builtin_amdgcn_mbcnt_lo(~0u, 0u)); asm volatile("" : "+v"(lane));
    r32 = lane & 31; h = lane >> 5; q = (lane & 15) >> 2; p = lane & 3; blk = (lane >> 4) & 1;
    rowoff = ((rg * 2 + (lw >> 1)) * 32 + r32) * D_ + cs * 64 + (lw & 1) * 32 + 8 * h;
    if (wave < 4) { res[0] = *(const u32x4*)(x + rowoff); res[1] = *(const u32x4*)(x + rowoff + 16); }
    LAS float* red = (LAS float*)(smem + L_RED);
#pragma unroll
    for (int i = 0; i < 9; i++) red[(wave * 9 + i) * 64 + lane] = kh ? o0[i] : o1[i];
    BARRIER();
    float o[9];
#pragma unroll
    for (int i = 0; i < 9; i++) o[i] = (kh ? o1[i] : o0[i]) + red[((wave ^ 1) * 9 + i) * 64 + lane];
    const bool ok = o[8] >= 0x1p-40f && o[8] <= 0x1p40f;
    if (!__all(ok)) {
      const u32x4 qq = kh ? q1 : q0;
      f32x16 z, oc;
#pragma unroll
      for (int i = 0; i < 16; i++) { z[i] = 0.f; oc[i] = 0.f; }
      float m = -__builtin_inff();
      const bool vl = (lane & 16) == 0;
      const u32x4 ones = {0x3f803f80u, 0x3f803f80u, 0x3f803f80u, 0x3f803f80u};
#pragma unroll 1
      for (int t = 0; t < 64; t++) {
        const u32x4 kk = ld16(rs, lane * 16, WS_K + head * 65536 + t * 1024);
        u32x4 wa = ld16(rs, h * 256 + (lane & 15) * 16, WS_V + head * 65536 + t * 1024), wb = ld16(rs, h * 256 + (lane & 15) * 16, WS_V + head * 65536 + t * 1024 + 512);
        wa = vl ? wa : ones; wb = vl ? wb : ones;
        f32x16 s = mfma(kk, qq, z);
        float tm = s[0];
#pragma unroll
        for (int i = 1; i < 16; i++) tm = fmaxf(tm, s[i]);
        tm = fmaxf(tm, __shfl_xor(tm, 32));
        const float mn = fmaxf(m, tm), al = __builtin_amdgcn_exp2f(m - mn);
        m = mn;
#pragma unroll
        for (int i = 0; i < 16; i++) { oc[i] *= al; s[i] -= mn; }
        oc = pv(wa, exp8(s, 0), oc); oc = pv(wb, exp8(s, 8), oc);
      }
#pragma unroll
      for (int i = 0; i < 9; i++) o[i] = oc[i];
    }
    const float rl = __builtin_amdgcn_rcpf(o[8]);
    u32x4 r;
#pragma unroll
    for (int j = 0; j < 4; j++) r[j] = pack2(o[2 * j] * rl, o[2 * j + 1] * rl);
    st16(rs, lane * 16, WS_O + ((rg1 * 2 + kh) * 32 + head) * 1024, r);
    STAMP(8);
    wait_vm<0>();
    BARRIER();
    if (wave == 4) publish(ws, WS_F2 + (rg1 * 8 + hg) * 128, tag, lane);
    STAMP(9);
  }

  LAS char* bb1 = smem + L_B1 + (8 * h + q) * 192 + blk * 32 + p * 8;
  if (wave < 4) {
  const int rb = wave >> 1, kh = wave & 1;
  const int tile = rg * 2 + rb;
  LAS char* bsq[2];
#pragma unroll
  for (int u = 0; u < 2; u++) bsq[u] = smem + (4 * h + q) * 128 + (((4 * (u ^ kh) + 2 * blk + (p & 1)) ^ ((q >> 1) << 2)) << 4) + 8 * (p >> 1);
  auto nohook = [](auto) {};
  auto exchange = [&](f32x16 (&acc)[2], int lds) {
#pragma unroll
    for (int i = 0; i < 4; i++) *(LAS f32x4*)(smem + lds + (wave * 4 + i) * 1024 + lane * 16) = f32x4{acc[1][4 * i], acc[1][4 * i + 1], acc[1][4 * i + 2], acc[1][4 * i + 3]};
    BARRIER();
#pragma unroll
    for (int i = 0; i < 4; i++) {
      const f32x4 v = *(LAS f32x4*)(smem + lds + ((wave ^ 1) * 4 + i) * 1024 + lane * 16);
      for (int j = 0; j < 4; j++) acc[0][4 * i + j] += v[j];
    }
  };
  {
    f32x16 acc[2];
#pragma unroll
    for (int i = 0; i < 16; i++) { acc[0][i] = 0.f; acc[1][i] = 0.f; }
    BARRIER();
    STAMP(10);
    gemm_direct<16, 2, 0>(rs, lane * 16, [&](int pos) { return WS_O + (tile * 32 + kh * 16 + ((pos + off3) & 15)) * 1024; },
                          [&](int pos, int u) { return tr_frag(bsq[u] + kh * 32768 + pos * 2048, 1024); }, (const u32x4(*)[2]) nullptr, acc, nohook, nohook);
    STAMP(11);
    exchange(acc, L_Q);
    f32x2 t[8];
#pragma unroll
    for (int i = 0; i < 8; i++) t[i] = f32x2{acc[0][2 * i] + bflo(res[i >> 2][i & 3]), acc[0][2 * i + 1] + bfhi(res[i >> 2][i & 3])};
    FENCE();
#pragma unroll
    for (int i = 0; i < 8; i++) res[i >> 2][i & 3] = pack2v(t[i]);
    FENCE();
    st16(rs, lane * 16, WS_H + (tile * 32 + cs * 4 + kh * 2) * 1024, res[0]);
    st16(rs, lane * 16, WS_H + (tile * 32 + cs * 4 + kh * 2 + 1) * 1024, res[1]);
#pragma unroll
    for (int i = 0; i < 8; i++) t[i] = f32x2{bflo(res[i >> 2][i & 3]), bfhi(res[i >> 2][i & 3])};
    FENCE();
    f32x2 s2[4], q2[4];
#pragma unroll
    for (int i = 0; i < 4; i++) { s2[i] = t[i] + t[i + 4]; q2[i] = pfma(t[i], t[i], t[i + 4] * t[i + 4]); }
    const f32x2 sa = (s2[0] + s2[1]) + (s2[2] + s2[3]), qa = (q2[0] + q2[1]) + (q2[2] + q2[3]);
    const float sx = xhalf_sum(sa[0] + sa[1]), sxx = xhalf_sum(qa[0] + qa[1]);
    if (h == 0) __builtin_amdgcn_raw_buffer_store_b64(u32x2{asu(sx), asu(sxx)}, rs, r32 * 8, WS_ST + (tile * 16 + cs * 2 + kh) * 256, 16);
    STAMP(12);
    BARRIER();
#pragma unroll
    for (int i = 0; i < 12; i++) *(LAS u32x4*)(smem + L_B1 + lw * 12288 + i * 1024 + lane * 16) = stg(i);
    wait_vm<0>();
    BARRIER();
#pragma unroll
    for (int i = 0; i < 8; i++) {
      LAS char* dst = smem + (2 * ((lw >> 1) * 7 + i) + (lw & 1)) * 2048 + lane * 16;
      *(LAS u32x4*)dst = stg(12 + 2 * i); *(LAS u32x4*)(dst + 1024) = stg(13 + 2 * i);
    }
    STAMP(13);
  }
  u32x4 pre[4][2];
  {
    const int t4 = rp * 4 + wave;
    f32x16 acc[3];
#pragma unroll
    for (int u = 0; u < 3; u++) for (int i = 0; i < 16; i++) acc[u][i] = 0.f;
    BARRIER();
    STAMP(16);
    u32x4 wx[3];
    u32x2 st[8];
    float rs2 = 0.f, sx = 0.f, sxx = 0.f, nm = 0.f, sd = 0.f;
    u32x4 ax = {0u, 0u, 0u, 0u};
    gemm_direct<32, 3, 0>(rs, lane * 16, [&](int pos) { return WS_H + (t4 * 32 + ((pos + off4) & 31)) * 1024; },
        [&](int pos, int u) { return tr_frag(bb1 + pos * 3072 + u * 64, 768); }, (const u32x4(*)[3]) nullptr, acc,
        [&](auto pp) {
          constexpr int pos = pp.value;
          if constexpr (pos < 8) st[pos] = __builtin_amdgcn_raw_buffer_load_b64(rs, h * 2048 + r32 * 8, WS_ST + (t4 * 16 + pos) * 256, 16);
          if constexpr (pos >= 12 && pos < 20) { sx = add1(asf(st[pos - 12][0]), sx); sxx = add1(asf(st[pos - 12][1]), sxx); }
          if constexpr (pos == 20) sx = xhalf_sum(sx);
          if constexpr (pos == 21) sxx = xhalf_sum(sxx);
          if constexpr (pos == 22) { nm = sx * (-1.f / D_); sd = fmaxf(fma1(sxx, 1.f / D_, -nm * nm), 0.f) + 1e-5f; }
          if constexpr (pos == 23) { rs2 = __builtin_amdgcn_rsqf(sd); asm("" : "+v"(rs2)); }
          if constexpr (pos == 24) { sd *= rs2; rs2 *= LOG2E; nm = h ? 0.f : nm; sd = h ? 0.f : sd; asm("" : "+v"(nm), "+v"(sd), "+v"(rs2)); }
          if constexpr (pos == 25) { ax[0] = pack2(nm, nm); ax[3] = pack2(sd, sd); }
          if constexpr (pos == 26) { nm = add1(nm, -bflo(ax[0])); sd = add1(sd, -bflo(ax[3])); }
          if constexpr (pos >= 28 && pos < 31) wx[pos - 28] = *(LAS u32x4*)(smem + L_RES + 256 + (pos - 28) * 512 + r32 * 16);
          if constexpr (pos == 27) { ax[1] = (pack2(nm, 0.f) & 0xffffu) | (ax[3] & 0xffff0000u); ax[2] = pack2(sd, 0.f); ax[3] = 0u; }
        },
        nohook);
#pragma unroll
    for (int u = 0; u < 3; u++) acc[u] = mfma(wx[u], ax, acc[u]);
    STAMP(17);
#pragma unroll
    for (int i = 0; i < 4; i++) for (int u = 0; u < 2; u++) pre[i][u] = tr_frag(bsq[u] + (2 * i + kh) * 2048, 1024);
    FENCE();
    prog[wave] = 34;
    FENCE();
#pragma unroll
    for (int u = 0; u < 3; u++) {
      if (u < nunit) {
        f32x2 w[8], t[8];
#pragma unroll
        for (int i = 0; i < 8; i++) w[i] = f32x2{acc[u][2 * i], acc[u][2 * i + 1]} * pk(rs2);
        FENCE();
#pragma unroll
        for (int i = 0; i < 8; i++) t[i] = f32x2{__builtin_amdgcn_exp2f(w[i][0]), __builtin_amdgcn_exp2f(w[i][1])};
        FENCE();
        f32x2 a2[8];
#pragma unroll
        for (int i = 0; i < 8; i++) a2[i] = t[i] + pk(2.f);
        FENCE();
#pragma unroll
        for (int i = 0; i < 8; i++) t[i] = pfma(t[i], a2[i], pk(2.f));
        FENCE();
#pragma unroll
        for (int i = 0; i < 8; i++) t[i] = f32x2{__builtin_amdgcn_rcpf(t[i][0]), __builtin_amdgcn_rcpf(t[i][1])};
        FENCE();
#pragma unroll
        for (int i = 0; i < 8; i++) t[i] = pfma(t[i], pk(-2.f / LOG2E), pk(1.f / LOG2E));
        FENCE();
#pragma unroll
        for (int i = 0; i < 8; i++) t[i] = t[i] * w[i];
        FENCE();
        unsigned o[8];
#pragma unroll
        for (int i = 0; i < 8; i++) o[i] = pack2v(t[i]);
        FENCE();
        const unsigned so = WS_ACT + (t4 * 86 + (unit0 + u) * 2) * 1024;
        st16(rs, lane * 16, so, u32x4{o[0], o[1], o[2], o[3]});
        st16(rs, lane * 16, so + 1024, u32x4{o[4], o[5], o[6], o[7]});
      }
    }
    STAMP(18);
    wait_vm<0>();
    STAMP(19);
  }
  {
    f32x16 acc[2];
#pragma unroll
    for (int i = 0; i < 16; i++) { acc[0][i] = 0.f; acc[1][i] = 0.f; }
    BARRIER();
    BARRIER();
    STAMP(20);
    gemm_direct<43, 2, 4>(rs, lane * 16, [&](int pos) { int ks = pos + off5; ks = ks >= 43 ? ks - 43 : ks; return WS_ACT + (tile * 86 + kh * 43 + ks) * 1024; },
                          [&](int pos, int u) { return tr_frag(bsq[u] + kh * 2048 + (pos >= 39 ? pos - 39 : pos) * 4096, 1024); }, pre, acc, nohook, nohook);
    STAMP(21);
    BARRIER();
    exchange(acc, 0);
#pragma unroll
    for (int s = 0; s < 2; s++) {
      u32x4 o;
#pragma unroll
      for (int j = 0; j < 4; j++) o[j] = pack2(acc[0][8 * s + 2 * j] + bflo(res[s][j]), acc[0][8 * s + 2 * j + 1] + bfhi(res[s][j]));
      __builtin_amdgcn_raw_buffer_store_b128(o, __builtin_amdgcn_make_buffer_rsrc(out, 0, 2048 * D_ * 2, 0x00020000), (unsigned)rowoff * 2 + 32 * s, 0, OUTAUX);
    }
    STAMP(22);
    STAMP_FLUSH();
  }
  } else {
    const unsigned bv = b1[(unit0 + max(lw - 1, 0)) * 32 + r32];
    const unsigned selv = ((lane & 15) == 4 * blk) ? 0x3f803f80u : 0u;
    const u32x4 sel = {selv, selv, selv, selv};
    f32x4 c4 = {0.f, 0.f, 0.f, 0.f};
    auto colsum = [&](int k0) {
      u32x4 f[16];
#pragma unroll
      for (int i = 0; i < 16; i++) f[i] = tr_frag(bb1 + (k0 + i) * 3072 + max(lw - 1, 0) * 64, 768);
#pragma unroll
      for (int i = 0; i < 16; i++) c4 = mfma16(sel, f[i], c4);
    };
    if (wave == 4) wait_flags(ws, WS_F2 + (rg * 8 + (lane & 7)) * 128, tag); else colsum(16);
    BARRIER();
    BARRIER();
    BARRIER();
    BARRIER();
    STAMP(14);
    if (wave == 4) { publish(ws, WS_F3 + (rg * 8 + cs) * 128, tag, lane); wait_flags(ws, WS_F3 + (rp * 16 + (lane & 15)) * 128, tag); }
    else colsum(0);
    if (lw > 0 && h == 0) {
      const unsigned ch = pack2(c4[0], 0.f), cl = pack2(c4[0] - bflo(ch), 0.f);
      *(LAS u32x4*)(smem + L_RES + 256 + (lw - 1) * 512 + r32 * 16) = u32x4{ch | (cl << 16), ch | (bv << 16), bv, 0u};
    }
    STAMP(15);
    BARRIER();
    lds_spin(L_RES + p * 4, 34);
    LAS char* dst = smem + (2 * (15 + (lw >> 1) * 14) + (lw & 1)) * 2048 + lane * 16;
    LAS char* dst2 = (lw >> 1) ? dst - 78 * 2048 : dst;
#pragma unroll
    for (int i = 0; i < 14; i++) { LAS char* d = (i >= 10 ? dst2 : dst) + i * 4096; *(LAS u32x4*)d = stg(2 * i); *(LAS u32x4*)(d + 1024) = stg(2 * i + 1); }
    STAMP(19);
    BARRIER();
    if (wave == 4) {
      publish(ws, WS_F4 + (rp * 16 + sl) * 128, tag, lane); wait_flags(ws, WS_F4 + ((rg >> 1) * 16 + (lane & 15)) * 128, tag);
      if (b == 0 && lane == 0) *(u64*)(ws + WS_EP) = tag;
    }
    BARRIER();
    STAMP_FLUSH();
  }
}

extern "C" void kernel_launch(void* const* d_in, const int* in_sizes, int n_in, void* d_out, int out_size, void* d_ws, size_t ws_size, hipStream_t stream) {
  fused<<<256, 512, 0, stream>>>((const u16*)d_in[0], (const u16*)d_in[1], (const u16*)d_in[2], (const u16*)d_in[4], (const u16*)d_in[5], (const u16*)d_in[6], (char*)d_ws, (u16*)d_out);
}
```

```cpp
#define DBG_DUMP
#define CW 6
#include <hip/hip_runtime.h>
#include <hip/hip_bf16.h>
#include <stdint.h>

typedef __attribute__((ext_vector_type(16))) float f32x16;
typedef __attribute__((ext_vector_type(8))) float f32x8;
typedef __attribute__((ext_vector_type(4))) float f32x4;
typedef __attribute__((ext_vector_type(2))) float f32x2;
typedef __attribute__((ext_vector_type(8))) __bf16 bf16x8;
typedef __attribute__((ext_vector_type(16))) __bf16 bf16x16;
typedef __attribute__((ext_vector_type(2))) __bf16 bf16x2;
typedef __attribute__((ext_vector_type(4))) short s16x4;
typedef __attribute__((ext_vector_type(4))) unsigned u32x4;
typedef __attribute__((ext_vector_type(2))) unsigned u32x2;
typedef __attribute__((ext_vector_type(16))) unsigned u32x16;
typedef unsigned short u16;
typedef unsigned long long u64;
#define LAS __attribute__((address_space(3)))
#define GAS __attribute__((address_space(1)))
#define DEV __device__ __forceinline__

constexpr int D_ = 512, F_ = 1376;
constexpr float LOG2E = 1.4426950408889634f;

constexpr unsigned WS_K = 0, WS_V = 2u << 20, WS_O = 4u << 20, WS_H = 6u << 20, WS_ACT = 8u << 20, WS_ST = 14u << 20;
constexpr unsigned WS_EP = 15u << 20, WS_F1 = WS_EP + 4096, WS_F2 = WS_F1 + 32768, WS_F3 = WS_F2 + 32768, WS_F4 = WS_F3 + 32768, WS_F5 = WS_F4 + 32768, WS_END = 16u << 20;

DEV unsigned pack2(float a, float b) { f32x2 p = {a, b}; return __builtin_bit_cast(unsigned, __builtin_convertvector(p, bf16x2)); }
DEV float asf(unsigned u) { return __builtin_bit_cast(float, u); }
DEV unsigned asu(float f) { return __builtin_bit_cast(unsigned, f); }
DEV float bflo(unsigned u) { return __builtin_bit_cast(float, u << 16); }
DEV float bfhi(unsigned u) { return __builtin_bit_cast(float, u & 0xffff0000u); }
DEV f32x16 mfma(u32x4 a, u32x4 b, f32x16 c) { return __builtin_amdgcn_mfma_f32_32x32x16_bf16(__builtin_bit_cast(bf16x8, a), __builtin_bit_cast(bf16x8, b), c, 0, 0, 0); }
DEV f32x4 mfma16(u32x4 a, u32x4 b, f32x4 c) { return __builtin_amdgcn_mfma_f32_16x16x32_bf16(__builtin_bit_cast(bf16x8, a), __builtin_bit_cast(bf16x8, b), c, 0, 0, 0); }
DEV u32x2 tr_read(LAS char* p) { return __builtin_bit_cast(u32x2, __builtin_amdgcn_ds_read_tr16_b64_v4i16((LAS s16x4*)p)); }
DEV u32x4 tr_frag(LAS char* p, int step) { const u32x2 w0 = tr_read(p), w1 = tr_read(p + step); return u32x4{w0[0], w0[1], w1[0], w1[1]}; }
DEV float xhalf_sum(float v) {
  const unsigned u = __builtin_bit_cast(unsigned, v);
  const auto r = __builtin_amdgcn_permlane32_swap(u, u, false, false);
  return asf(r[0]) + asf(r[1]);
}
#define BARRIER() do { __builtin_amdgcn_sched_barrier(0); __builtin_amdgcn_s_waitcnt(0xC07F); asm volatile("s_barrier" ::: "memory"); __builtin_amdgcn_sched_barrier(0); } while (0)
template <int N> DEV void wait_vm() { asm volatile("s_waitcnt vmcnt(%0)" ::"n"(N) : "memory"); }
#define SGB(m, n) __builtin_amdgcn_sched_group_barrier(m, n, 0)
template <int I, int N, class Fn> DEV void static_for(Fn&& f) {
  if constexpr (I < N) { f(std::integral_constant<int, I>{}); static_for<I + 1, N>(f); }
}

#include <cstdio>
#include <vector>
#include <algorithm>
constexpr size_t WS_DBG = 64u << 20;
constexpr int NST = 28;
#define STAMP_DECL unsigned st_t[NST]; _Pragma("unroll") for (int i_ = 0; i_ < NST; i_++) st_t[i_] = 0
#define STAMP(i) do { __builtin_amdgcn_sched_barrier(0); st_t[i] = (unsigned)__builtin_amdgcn_s_memrealtime(); __builtin_amdgcn_sched_barrier(0); } while (0)
#define STAMPC(i) do { __builtin_amdgcn_sched_barrier(0); st_t[i] = (unsigned)__builtin_amdgcn_s_memtime(); __builtin_amdgcn_sched_barrier(0); } while (0)
#define STAMP_FLUSH() do { if ((threadIdx.x & 63) == 0) { unsigned* p_ = (unsigned*)(ws + WS_DBG) + (blockIdx.x * 8 + (threadIdx.x >> 6)) * 32; \
  _Pragma("unroll") for (int i_ = 0; i_ < NST; i_++) p_[i_] = st_t[i_]; } } while (0)
static void dbg_dump(void* d_ws) {
  std::vector<unsigned> h(256 * 8 * 32);
  (void)hipMemcpy(h.data(), (char*)d_ws + WS_DBG, h.size() * 4, hipMemcpyDeviceToHost);
  unsigned g0 = ~0u; for (int b = 0; b < 256 * 8; b++) g0 = std::min(g0, h[b * 32]);
  for (int i = 0; i < NST - 2; i++) {
    fprintf(stderr, "[%2d]", i);
    for (int wsel : {0, 3, 4, 7}) {
      std::vector<double> v;
      for (int b = 0; b < 256; b++) { unsigned t = h[(b * 8 + wsel) * 32 + i]; if (t && t - g0 < 100000) v.push_back((t - g0) * 0.01); }
      if (v.size() < 100) { fprintf(stderr, "  w%d: %-20s", wsel, "-"); continue; }
      std::sort(v.begin(), v.end());
      fprintf(stderr, "  w%d: %6.2f/%6.2f/%6.2f", wsel, v[0], v[v.size() / 2], v.back());
    }
    fprintf(stderr, "\n");
  }
  std::vector<double> c; for (int b = 0; b < 256; b++) c.push_back(h[(b * 8 + CW) * 32 + NST - 1] - h[(b * 8 + CW) * 32 + NST - 2]);
  std::sort(c.begin(), c.end()); fprintf(stderr, "cyc26-27: %.0f\n", c[128]);
}

DEV void dma_kv(u64 sw, unsigned v, int lds) {
  asm volatile("s_mov_b32 m0, %[m]\n\ts_nop 0\n\tglobal_load_lds_dwordx4 %[v], %[sw]\n\t"
               "s_add_u32 m0, %[m], 0x1c00\n\ts_nop 0\n\tglobal_load_lds_dwordx4 %[v], %[sw] offset:1024"
               :: [m] "s"(lds), [sw] "s"(sw), [v] "v"(v) : "memory", "scc");
}
DEV void dma_slot(u64 sw, unsigned v, int lds) {
#define P1(dm) "s_add_u32 m0, %[m], " #dm "\n\ts_add_u32 s44, s44, 0x6000\n\ts_addc_u32 s45, s45, 0\n\tglobal_load_lds_dwordx4 %[v], s[44:45]\n\t"
  asm volatile("s_mov_b64 s[44:45], %[sw]\n\t" P1(0) P1(0x400) P1(0x800) P1(0xc00) P1(0x1000) P1(0x1400) P1(0x1800) P1(0x1c00)
               :: [m] "s"(lds), [sw] "s"(sw - 0x6000), [v] "v"(v) : "memory", "scc", "s44", "s45");
}
#define RLX_AGENT __ATOMIC_RELAXED, __HIP_MEMORY_SCOPE_AGENT
DEV void publish(char* ws, unsigned off, u64 tag, int lane) {
  if (lane == 0) __hip_atomic_store((GAS u64*)(ws + off), tag, RLX_AGENT);
}
#define PD1 14
#define PD2 14
#define PD3 14
#define PD4 14
#define PSL 2
template <int PDLY> DEV void wait_flags(char* ws, unsigned off, u64 tag) {
  u64 v; int n = 1 << 18;
  asm volatile("s_sleep %[d]\n\t1:\n\tglobal_load_dwordx2 %[v], %[off], %[ws] sc1\n\ts_waitcnt vmcnt(0)\n\tv_cmp_ne_u64 vcc, %[tag], %[v]\n\ts_cbranch_vccz 2f\n\t"
               "s_mov_b64 exec, vcc\n\ts_sleep %[s]\n\ts_sub_u32 %[n], %[n], 1\n\ts_cbranch_scc0 1b\n\t2:\n\ts_mov_b64 exec, -1"
               : [v] "=&v"(v), [n] "+s"(n) : [off] "v"(off), [ws] "s"(ws), [tag] "s"(tag), [s] "n"(PSL), [d] "n"(PDLY) : "memory", "vcc", "scc");
}
typedef __amdgpu_buffer_rsrc_t rsrc_t;
DEV void st16(rsrc_t r, unsigned voff, unsigned soff, u32x4 v) { __builtin_amdgcn_raw_buffer_store_b128(v, r, voff, soff, 16); }
DEV u32x4 ld16(rsrc_t r, unsigned voff, unsigned soff) { return __builtin_amdgcn_raw_buffer_load_b128(r, voff, soff, 16); }
#define LSLEEP 1
#define THR 6
#define W2POS 12
#define OUTAUX 0
#define FIRST 4
#define FSLEEP 6
#define FLPOS 1
#define EARLY 0
#define DRAIN(m) do { if (!(EARLY & m)) wait_vm<0>(); } while (0)
#define SL1 1
#define SL4 1
#define QREL 0
#define XRX 0
#define QT 0
#define BX 0
#define MIX 1
#define NINIT 3
#define WDELAY 4
#define DROPI 4
template <int SL> DEV void lds_spin(unsigned addr, unsigned need) {
  unsigned a;
  asm volatile("1:\n\tds_read_b32 %[a], %[p]\n\ts_waitcnt lgkmcnt(0)\n\tv_cmp_gt_u32 vcc, %[n], %[a]\n\ts_cbranch_vccz 2f\n\ts_sleep %[s]\n\ts_branch 1b\n\t2:"
               : [a] "=&v"(a) : [p] "v"(addr), [n] "s"(need), [s] "n"(SL) : "memory", "vcc");
}
DEV f32x16 pv(u32x4 v, f32x8 pr, f32x16 o) {
  return __builtin_amdgcn_smfmac_f32_32x32x32_bf16(__builtin_bit_cast(bf16x8, v), __builtin_bit_cast(bf16x16, pr), o, 0xDDDD, 0, 0);
}
DEV f32x8 exp8(f32x16 s, int o) { f32x8 r; for (int i = 0; i < 8; i++) r[i] = __builtin_amdgcn_exp2f(s[o + i]); return r; }
DEV float fma1(float a, float b, float c) { float r = __builtin_fmaf(a, b, c); asm("" : "+v"(r)); return r; }
DEV float add1(float a, float b) { float r = a + b; asm("" : "+v"(r)); return r; }
DEV float dot2(unsigned a, unsigned b, float c) { return __builtin_amdgcn_fdot2_f32_bf16(__builtin_bit_cast(bf16x2, a), __builtin_bit_cast(bf16x2, b), c, false); }
DEV float rbf(float a) { return bflo(pack2(a, 0.f)); }
DEV f32x2 pk(float a) { return f32x2{a, a}; }
DEV f32x2 pfma(f32x2 a, f32x2 b, f32x2 c) { return __builtin_elementwise_fma(a, b, c); }
DEV unsigned pack2v(f32x2 p) { return __builtin_bit_cast(unsigned, __builtin_convertvector(p, bf16x2)); }
#define FENCE() __builtin_amdgcn_sched_barrier(0)

struct WFrag { u32x4 a[8]; u32x4 w[8]; };
constexpr int L_RES = 159744;
constexpr int L_Q = 67584, L_RED = 75776, L_DUM = 94208;
constexpr int L_W1 = 65536, L_Q1 = 98304;
constexpr int L_B1 = 61440;
constexpr int CODE_BYTES = 30720;
#define RD_ 10
constexpr int RD = RD_;

struct Run4 { static constexpr int start(int pos) { return pos & ~3; } };
struct Run5 { static constexpr int start(int pos) { const int s0 = pos >= 35 ? 35 : pos >= 29 ? 29 : pos >= 21 ? 21 : pos >= 15 ? 15 : 0; return s0 + ((pos - s0) & ~3); } };
template <int NK, int NU, int NPRE, class GS, class ASO, class BRD, class RDY, class H1>
DEV void gemm_direct(rsrc_t rs, unsigned voff, ASO&& aso, BRD&& brd, const u32x4 (*pre)[NU], f32x16 (&acc)[NU], RDY&& ready, H1&& hook) {
  u32x4 a[RD], w[3][NU];
  unsigned so[NK];
  static_for<0, NK>([&](auto i) { if constexpr (GS::start(i.value) == i.value) { so[i.value] = aso(i.value); asm volatile("" : "+s"(so[i.value])); } });
  auto lda = [&](auto i) { return ld16(rs, voff + (i.value - GS::start(i.value)) * 1024, so[GS::start(i.value)]); };
  static_for<0, 2>([&](auto i) {
    if constexpr (i.value >= NPRE) { for (int u = 0; u < NU; u++) w[i.value][u] = brd(i.value, u); }
  });
  ready();
  static_for<0, RD>([&](auto i) { a[i.value] = lda(i); FENCE(); });
  FENCE();
  static_for<0, NK>([&](auto pp) {
    constexpr int pos = pp.value;
    static_for<0, NU>([&](auto uu) {
      constexpr int u = uu.value;
      if constexpr (pos < NPRE) acc[u] = mfma(pre[pos][u], a[pos % RD], acc[u]); else acc[u] = mfma(w[pos % 3][u], a[pos % RD], acc[u]);
      FENCE();
      if constexpr (u == 0) hook(pp);
      if constexpr (pos + 2 < NK && pos + 2 >= NPRE) w[(pos + 2) % 3][u] = brd(pos + 2, u);
      if constexpr (u == NU - 1) {
        if constexpr (pos + RD < NK) a[pos % RD] = lda(std::integral_constant<int, pos + RD>{});
      }
      FENCE();
    });
  });
}

__global__ __launch_bounds__(512, 2) void fused(const u16* __restrict__ x, const u16* __restrict__ wqkv, const u16* __restrict__ wo, const u16* __restrict__ w1,
                                                const u16* __restrict__ b1, const u16* __restrict__ w2, char* __restrict__ ws, u16* __restrict__ out) {
  __shared__ __attribute__((aligned(16))) char smem_[163840];
  LAS char* smem = (LAS char*)smem_;
  const int wave = __builtin_amdgcn_readfirstlane(threadIdx.x >> 6);
  int lane = threadIdx.x & 63;
  const int b = blockIdx.x ^ BX, xcd = b & 7, bi = b >> 3;
  STAMP_DECL; STAMP(0);
  u64 pc0; asm volatile("s_getpc_b64 %0" : "=s"(pc0));
  asm volatile("" ::"s"(x), "s"(wqkv), "s"(wo), "s"(w1), "s"(b1), "s"(w2), "s"(ws), "s"(out));
  u64 tag = 0;
  auto load_tag = [&]() { asm volatile("s_load_dwordx2 %0, %1, 0x0\n\ts_waitcnt lgkmcnt(0)" : "=s"(tag) : "s"(ws + WS_EP) : "memory"); tag += 1; };
  const rsrc_t rs = __builtin_amdgcn_make_buffer_rsrc(ws, 0, WS_END, 0x00020000);
  int r32 = lane & 31, h = lane >> 5, q = (lane & 15) >> 2, p = lane & 3, blk = (lane >> 4) & 1;
  const int lw = wave & 3;
  const unsigned vrow128 = 16 * ((lane & 7) ^ (((lane >> 4) & 1) << 2));
  volatile LAS unsigned* prog = (volatile LAS unsigned*)(smem + L_RES);

#define MAPB 1
  constexpr int HS = 3 - MAPB;
  const int hg = ((xcd & ((1 << MAPB) - 1)) << HS) + (bi & ((1 << HS) - 1)), rg1 = (xcd >> MAPB) * (32 >> HS) + (bi >> HS);
  const int c0 = bi >> HS;
  const u64 sw0 = (u64)(wqkv + hg * 64);
  const unsigned vw0 = (lane >> 3) * (3 * D_ * 2) + vrow128;
  const int seg = lane & 7, xr = (8 >> HS) * (bi & ((1 << HS) - 1)) + XRX * (xcd & 1);
  u32x4 xv[8];
  auto kv8 = [&](int c) { dma_kv(sw0 + 1024 + (unsigned)((((c + c0) & 7) * 8 + wave) * (8 * 3 * D_ * 2)), vw0, L_W1 + c * 16384 + wave * 1024); };
  {
    const char* xb = (const char*)x + (size_t)(rg1 * 64 + wave * 8) * 1024;
    const unsigned xo = (lane >> 3) * 1024 + seg * 16;
#pragma unroll
    for (int i = 0; i < 8; i++) asm volatile("global_load_dwordx4 %0, %1, %2" : "=v"(xv[i]) : "v"(xo), "s"(xb + ((i + xr) & 7) * 128) : "memory");
    asm volatile("s_barrier" ::: "memory");
    kv8(0);
    const char* tp = lane == 2 ? ws + WS_F1 + lw * 32768 + b * 128 : (const char*)(pc0 & ~127ull) + min(((bi * 8 + wave) * 2 + (lane & 1)) * 128, CODE_BYTES - 128);
    asm volatile("s_mov_b32 m0, %1\n\ts_nop 0\n\tglobal_load_lds_dword %0, off" ::"v"(tp), "s"(L_RES + 2048) : "memory");
  }

  const int rg = xcd * 4 + (bi >> 3), cs = bi & 7;
  const int rp = xcd * 2 + (bi >> 4), sl = bi & 15;
  const int unit0 = sl < 5 ? 2 * sl : 3 * sl - 5, nunit = sl < 5 ? 2 : 3;
  const int mx = MIX * (bi >> 3);
  const int off3 = ((cs + mx) & 3) * 4, off4 = ((sl + (mx & 2)) & 3) * 8, off5 = (0x080e161c00ull >> (8 * ((cs + mx) % 5))) & 0xff;
  u32x16 sg[7];
  u32x4 res[2];
  auto stg = [&](int i) { return u32x4{sg[i >> 2][(i & 3) * 4], sg[i >> 2][(i & 3) * 4 + 1], sg[i >> 2][(i & 3) * 4 + 2], sg[i >> 2][(i & 3) * 4 + 3]}; };
  unsigned rowoff;
  const int head = hg * 4 + (wave >> 1), kh = wave & 1;
  unsigned koff, voff, vinit, a0, a1, a2, v30, v31, v32, vw, tpl, tph;
  const char *kb, *vb, *sa, *sb, *so, *sh;
  auto setup = [&](auto first) {
    auto v192 = [&](int j) { const int idx = j * 64 + lane, row = idx / 12; return (unsigned)(row * (F_ * 2) + (idx - row * 12) * 16); };
    auto w2at = [&](int pos) {
      int ks = pos + off5; ks = ks >= 43 ? ks - 43 : ks;
      return (const char*)w2 + (unsigned)(((lw & 1) * 43 + ks) * 16 * D_ * 2 + cs * 128);
    };
    auto w1at = [&](int pos) { return (const char*)w1 + (unsigned)(((pos + off4) & 31) * 16 * F_ * 2 + unit0 * 64); };
    vw = (lane >> 3) * (D_ * 2) + vrow128;
    v30 = v192(0); v31 = v192(1); v32 = v192(2);
    koff = lane * 16; voff = h * 256 + (lane & 15) * 16;
    kb = ws + WS_K + (unsigned)(head * 65536 + kh * 32768); vb = kb + (WS_V - WS_K);
    vinit = (r32 == 16 || r32 == 20) ? 0x3f803f80u : 0u;
    so = (const char*)wo + (unsigned)(((wave >> 2) * 256 + ((lw * 4 + off3) & 15) * 16) * D_ * 2 + cs * 128) - 8192;
    sh = w1at(16 + wave * 2);
    const char* tp;
    if constexpr (first.value) {
      a0 = v30; a1 = v31; a2 = v32; sa = w1at(lw * 4); sb = w2at((lw >> 1) * 7);
      const char* t0 = (const char*)((lw & 1) ? w2 : w1) + (unsigned)(b * 5504 + (lw >> 1) * 21 * 128);
      const char* t1 = (const char*)wo + (unsigned)(b * 2048 + lw * 512) - 22 * 128;
      const char* t2 = (const char*)b1 + (unsigned)(((b * 4 + lw) & 15) * 168) - 26 * 128;
      const int l = min(lane, 26);
      tp = (l < 22 ? t0 : l < 26 ? t1 : t2) + l * 128;
    } else {
      a0 = vw; a1 = vw + 8 * D_ * 2; a2 = vw + 16 * D_ * 2; sa = w2at(15 + (lw >> 1) * 14); sb = w2at(21 + (lw >> 1) * 14);
      tp = kb;
    }
    tpl = (unsigned)(u64)tp; tph = (unsigned)((u64)tp >> 32);
    asm volatile("" : "+v"(koff), "+v"(voff), "+v"(vinit), "+v"(a0), "+v"(a1), "+v"(a2), "+v"(v30), "+v"(v31), "+v"(v32), "+v"(vw), "+v"(tpl), "+v"(tph));
    asm volatile("" : "+s"(kb), "+s"(vb), "+s"(sa), "+s"(sb), "+s"(so), "+s"(sh));
  };
  {
    prog[wave] = wave < 4 ? 0 : 2 * NINIT + lw; prog[8 + lw] = 0;
    STAMP(1);
    float s4[4] = {0.f, 0.f, 0.f, 0.f}, q4[4] = {0.f, 0.f, 0.f, 0.f};
    static_for<0, 8>([&](auto ii) {
      constexpr int i = ii.value;
      asm volatile("s_waitcnt vmcnt(%1)" : "+v"(xv[i]) : "n"(10 - i));
#pragma unroll
      for (int j = 0; j < 4; j++) { s4[j] = dot2(xv[i][j], 0x3f803f80u, s4[j]); q4[j] = dot2(xv[i][j], xv[i][j], q4[j]); }
    });
    FENCE();
#pragma unroll
    for (int c = 1; c < NINIT; c++) kv8(c);
    asm volatile("s_mov_b32 m0, %2\n\ts_nop 0\n\tglobal_load_lds_dword %0, %1" ::"v"((lane & 7) * (3 * D_ * 2)), "s"(sw0 + (unsigned)(((c0 & 7) * 8 + wave) * (8 * 3 * D_ * 2))), "s"(L_RES + 2048) : "memory");
    STAMP(25);
    float sx = (s4[0] + s4[1]) + (s4[2] + s4[3]), sxx = (q4[0] + q4[1]) + (q4[2] + q4[3]);
    sx += asf(__builtin_amdgcn_mov_dpp(asu(sx), 0xB1, 0xf, 0xf, true)); sxx += asf(__builtin_amdgcn_mov_dpp(asu(sxx), 0xB1, 0xf, 0xf, true));
    sx += asf(__builtin_amdgcn_mov_dpp(asu(sx), 0x4E, 0xf, 0xf, true)); sxx += asf(__builtin_amdgcn_mov_dpp(asu(sxx), 0x4E, 0xf, 0xf, true));
    sx += asf(__builtin_amdgcn_mov_dpp(asu(sx), 0x141, 0xf, 0xf, true)); sxx += asf(__builtin_amdgcn_mov_dpp(asu(sxx), 0x141, 0xf, 0xf, true));
    const float mean = sx * (1.f / D_);
    const float var = rbf(fmaxf(sxx * (1.f / D_) - mean * mean, 0.f));
    const float rstd = __builtin_amdgcn_rcpf(rbf(__builtin_amdgcn_sqrtf(rbf(var + 1e-5f)))), nmr = -rbf(mean) * rstd;
    const int r = lw * 8 + (lane >> 3);
    LAS char* dst = smem + (wave >> 2) * 32768 + seg * 512 + ((r ^ seg) << 4);
#pragma unroll
    for (int i0 = 0; i0 < 8; i0 += 2) {
      f32x2 y[8];
#pragma unroll
      for (int j = 0; j < 8; j++) { const unsigned u = xv[i0 + (j >> 2)][j & 3]; y[j] = f32x2{bflo(u), bfhi(u)}; }
      FENCE();
#pragma unroll
      for (int j = 0; j < 8; j++) y[j] = pfma(y[j], pk(rstd), pk(nmr));
      FENCE();
      const u32x4 o0 = {pack2v(y[0]), pack2v(y[1]), pack2v(y[2]), pack2v(y[3])}, o1 = {pack2v(y[4]), pack2v(y[5]), pack2v(y[6]), pack2v(y[7])};
      *(LAS u32x4*)(dst + ((i0 + xr) & 7) * 4096) = o0;
      *(LAS u32x4*)(dst + ((i0 + 1 + xr) & 7) * 4096) = o1;
      FENCE();
    }
    STAMP(23);
    if (wave < 4) setup(std::true_type{});
    wait_vm<(QT < 2)>();
    BARRIER();
  }
  if (wave >= 4) {
    load_tag();
#pragma unroll 1
    for (int n = lw + 2 * NINIT; n < 24; n += 4) {
      if (n >= 10) lds_spin<SL1>(L_RES + p * 4, n == 23 ? max(15, QREL) : n >= 16 ? max(n - 9, QREL) : n - 9);
      const int c = n < 16 ? n >> 1 : n - 16;
      dma_slot(sw0 + (unsigned)(((c + c0) & 7) * (64 * 3 * D_ * 2) + (n < 16 ? 1024 + (n & 1) * 1024 : 0)), vw0, L_W1 + (n % 10) * 8192);
      wait_vm<0>(); prog[wave] = n + 4;
    }
    STAMP(2);
    setup(std::false_type{});
    if (wave == 4 + ((20 - 2 * NINIT) & 3)) {
      lds_spin<SL1>(L_RES + 32 + p * 4, 1);
      publish(ws, WS_F1 + (hg * 32 + rg1) * 128, tag, lane);
      STAMP(5);
      wait_flags<PD1>(ws, WS_F1 + (hg * 32 + r32) * 128, tag);
    }
  } else {
    const int rb = wave >> 1, g = wave & 1;
    int aoff[4];
#pragma unroll
    for (int i = 0; i < 4; i++) aoff[i] = rb * 32768 + i * 1024 + h * 512 + ((r32 ^ (2 * i + h)) << 4);
    int boff[2];
    boff[0] = L_W1 + (8 * h + q) * 128 + (((4 * g + 2 * blk + (p >> 1)) ^ ((q >> 1) << 2)) << 4) + 8 * (p & 1); boff[1] = boff[0] + 40960;
    asm volatile("" : "+v"(boff[0]), "+v"(boff[1]));
    f32x16 acc[3];
#pragma unroll
    for (int u = 0; u < 3; u++) for (int i = 0; i < 16; i++) acc[u][i] = 0.f;
    unsigned fl;
    auto rd = [&](WFrag& f, int st) {
#pragma unroll
      for (int i = 0; i < 8; i++) {
        if (st >= 8) f.a[i] = *(LAS u32x4*)(smem + aoff[i & 3] + ((2 * st + (i >> 2) + c0) & 7) * 4096);
        else if (i < 4) f.a[i] = *(LAS u32x4*)(smem + aoff[i] + ((st + c0) & 7) * 4096);
        f.w[i] = tr_frag(smem + boff[((2 * st + (i >> 2)) % 10) / 5] + ((2 * st + (i >> 2)) % 5) * 8192 + (i & 3) * 2048, 512);
        if ((i & 3) == 3) prog[wave] = 2 * st + (i >> 2) + 1;
        if (i == 3) fl = *(volatile LAS unsigned*)(smem + L_RES + 16 + p * 4);
      }
    };
    auto mm = [&](const WFrag& f, int st) {
#pragma unroll
      for (int i = 0; i < 8; i++) {
        if (st >= 8) acc[0] = mfma(f.w[i], f.a[i], acc[0]);
        else if (i < 4) acc[1] = mfma(f.w[i], f.a[i], acc[1]);
        else acc[2] = mfma(f.a[i & 3], f.w[i], acc[2]);
      }
    };
    auto sched = [&]() {
      SGB(0x8, 1); SGB(0x2, 6);
#pragma unroll
      for (int i = 0; i < 7; i++) { SGB(0x80, 3); SGB(0x8, 1); }
    };
    auto step = [&](int st) { FENCE(); if (st >= NINIT && __builtin_expect(__any(fl < 2 * st + 2), 0)) lds_spin<0>(L_RES + 16 + p * 4, 2 * st + 2); FENCE(); };
    WFrag fa, fb;
    STAMP(24);
    rd(fa, 0);
    static_for<0, 3>([&](auto cc) {
      constexpr int c = 2 * cc.value + 1;
      step(c);
      rd(fb, c); mm(fa, c - 1); sched();
      step(c + 1);
      rd(fa, c + 1); mm(fb, c); sched();
    });
    step(7);
    STAMP(2);
    rd(fb, 7); mm(fa, 6); sched();
    FENCE();
    mm(fb, 7);
    STAMP(3);
    const int tile = rg1 * 2 + rb;
    const unsigned so1 = ((hg * 4 + 2 * g) * 64 + tile) * 1024;
    {
      unsigned o[8];
#pragma unroll
      for (int i = 0; i < 8; i++) o[i] = pack2(acc[1][2 * i], acc[1][2 * i + 1]);
      const unsigned pos = h * 32 + ((r32 & 19) | ((r32 & 4) << 1) | ((r32 & 8) >> 1));
      st16(rs, pos * 16, WS_K + so1, u32x4{o[0], o[1], o[2], o[3]});
      st16(rs, pos * 16, WS_K + so1 + 65536, u32x4{o[4], o[5], o[6], o[7]});
#pragma unroll
      for (int i = 0; i < 8; i++) o[i] = pack2(acc[2][2 * i], acc[2][2 * i + 1]);
      const unsigned vo = (r32 >> 4) * 65536 + h * 256 + (r32 & 15) * 16;
      st16(rs, vo, WS_V + so1, u32x4{o[0], o[1], o[2], o[3]});
      st16(rs, vo, WS_V + so1 + 512, u32x4{o[4], o[5], o[6], o[7]});
      STAMP(4);
      step(8); rd(fa, 8);
      step(9); rd(fb, 9); mm(fa, 8); sched();
      FENCE(); wait_vm<0>(); prog[8 + wave] = 1;
      STAMP(5);
      step(10); rd(fa, 10); mm(fb, 9); sched();
      step(11); rd(fb, 11); mm(fa, 10); sched();
      FENCE();
      mm(fb, 11);
#pragma unroll
      for (int i = 0; i < 8; i++) o[i] = pack2v(f32x2{acc[0][2 * i], acc[0][2 * i + 1]} * pk(0.25f * LOG2E));
      LAS char* d = smem + L_Q1 + (4 * g + rb) * 1024 + lane * 16;
      *(LAS u32x4*)d = u32x4{o[0], o[1], o[2], o[3]};
      *(LAS u32x4*)(d + 2048) = u32x4{o[4], o[5], o[6], o[7]};
    }
  }

  {
    u32x4 q0, q1;
    const unsigned qa = L_Q1 + wave * 1024 + lane * 16, qb = qa ^ 1024;
    const int drop = (wave >> 2) ^ 1;
    if (wave < 4) __builtin_amdgcn_s_setprio(2); else __builtin_amdgcn_s_setprio(1);
    f32x16 o0, o1;
#pragma unroll
    for (int i = 0; i < 16; i++) { o0[i] = 0.f; o1[i] = 0.f; }
    {
      const int ol = wave * 8192, hl = L_B1 + (16 + wave * 2) * 3072, ga = 16 * F_ * 2 - (wave >> 2) * (16 * F_ * 2 - 24 * D_ * 2);
      sg[6][14] = tpl; sg[6][15] = tph;
      STAMP(6); STAMPC(26);
      asm volatile(
        "s_mov_b64 s[40:41], %[kin]\n\ts_mov_b64 s[42:43], %[vin]\n\ts_mov_b64 s[46:47], %[sa]\n\ts_mov_b64 s[48:49], %[sb]\n\ts_mov_b64 s[50:51], %[so]\n\ts_mov_b64 s[52:53], %[sh]\n\t"
        "v_mov_b32 v96, %[vinit]\n\tv_mov_b32 v112, %[vinit]\n\tv_mov_b32 v97, %[vinit]\n\tv_mov_b32 v113, %[vinit]\n\tv_mov_b32 v98, %[vinit]\n\tv_mov_b32 v114, %[vinit]\n\t"
        "v_mov_b32 v99, %[vinit]\n\tv_mov_b32 v115, %[vinit]\n\tv_mov_b32 v100, %[vinit]\n\tv_mov_b32 v116, %[vinit]\n\tv_mov_b32 v101, %[vinit]\n\tv_mov_b32 v117, %[vinit]\n\t"
        "v_mov_b32 v102, %[vinit]\n\tv_mov_b32 v118, %[vinit]\n\tv_mov_b32 v103, %[vinit]\n\tv_mov_b32 v119, %[vinit]\n\tv_mov_b32 v104, %[vinit]\n\tv_mov_b32 v120, %[vinit]\n\t"
        "v_mov_b32 v105, %[vinit]\n\tv_mov_b32 v121, %[vinit]\n\tv_mov_b32 v106, %[vinit]\n\tv_mov_b32 v122, %[vinit]\n\tv_mov_b32 v107, %[vinit]\n\tv_mov_b32 v123, %[vinit]\n\t"
        "v_mov_b32 v108, %[vinit]\n\tv_mov_b32 v124, %[vinit]\n\tv_mov_b32 v109, %[vinit]\n\tv_mov_b32 v125, %[vinit]\n\tv_mov_b32 v110, %[vinit]\n\tv_mov_b32 v126, %[vinit]\n\t"
        "v_mov_b32 v111, %[vinit]\n\tv_mov_b32 v127, %[vinit]\n\tv_mov_b32 v128, 0xdddd\n\ts_waitcnt lgkmcnt(0)\n\ts_barrier\n\t"
        "global_load_dwordx4 v[80:83], %[koff], s[40:41] offset:0 sc1\n\tds_read_b128 %[q0], %[qa]\n\tds_read_b128 %[q1], %[qb]\n\t"
        "global_load_dwordx4 v[84:87], %[koff], s[40:41] offset:1024 sc1\n\ts_mov_b64 exec, %[vm]\n\tglobal_load_dwordx4 v[96:99], %[voff], s[42:43] offset:0 sc1\n\t"
        "global_load_dwordx4 v[112:115], %[voff], s[42:43] offset:512 sc1\n\ts_mov_b64 exec, -1\n\tglobal_load_dwordx4 v[88:91], %[koff], s[40:41] offset:2048 sc1\n\t"
        "s_mov_b64 exec, %[vm]\n\tglobal_load_dwordx4 v[100:103], %[voff], s[42:43] offset:1024 sc1\n\tglobal_load_dwordx4 v[116:119], %[voff], s[42:43] offset:1536 sc1\n\t"
        "s_mov_b64 exec, -1\n\tglobal_load_dwordx4 v[92:95], %[koff], s[40:41] offset:3072 sc1\n\ts_mov_b64 exec, %[vm]\n\t"
        "global_load_dwordx4 v[104:107], %[voff], s[42:43] offset:2048 sc1\n\tglobal_load_dwordx4 v[120:123], %[voff], s[42:43] offset:2560 sc1\n\ts_mov_b64 exec, -1\n\t"
        "s_waitcnt lgkmcnt(0)\n\ts_waitcnt vmcnt(9)\n\tv_mfma_f32_32x32x16_bf16 v[48:63], v[80:83], %[q0], 0\n\ts_nop 15\n\tv_exp_f32 v48, v48\n\tv_exp_f32 v49, v49\n\t"
        "v_exp_f32 v50, v50\n\tv_exp_f32 v51, v51\n\tv_exp_f32 v52, v52\n\tv_mfma_f32_32x32x16_bf16 v[64:79], v[80:83], %[q1], 0\n\ts_add_u32 s40, s40, 4096\n\ts_addc_u32 s41, s41, 0\n\t"
        "global_load_dwordx4 v[80:83], %[koff], s[40:41] offset:0 sc1\n\ts_mov_b64 exec, %[vm]\n\tglobal_load_dwordx4 v[108:111], %[voff], s[42:43] offset:3072 sc1\n\t"
        "global_load_dwordx4 v[124:127], %[voff], s[42:43] offset:3584 sc1\n\ts_mov_b64 exec, -1\n\tv_exp_f32 v53, v53\n\tv_exp_f32 v54, v54\n\tv_exp_f32 v55, v55\n\tv_exp_f32 v56, v56\n\t"
        "v_exp_f32 v57, v57\n\ts_waitcnt vmcnt(9)\n\tv_smfmac_f32_32x32x32_bf16 %[o0], v[96:99], v[48:55], v128\n\tv_exp_f32 v58, v58\n\tv_exp_f32 v59, v59\n\tv_exp_f32 v60, v60\n\t"
        "v_exp_f32 v61, v61\n\tv_exp_f32 v62, v62\n\tv_exp_f32 v63, v63\n\tv_exp_f32 v64, v64\n\tv_exp_f32 v65, v65\n\tv_smfmac_f32_32x32x32_bf16 %[o0], v[112:115], v[56:63], v128\n\t"
        "v_exp_f32 v66, v66\n\tv_exp_f32 v67, v67\n\tv_exp_f32 v68, v68\n\tv_mfma_f32_32x32x16_bf16 v[48:63], v[84:87], %[q0], 0\n\tv_exp_f32 v69, v69\n\tv_exp_f32 v70, v70\n\t"
        "v_exp_f32 v71, v71\n\tv_exp_f32 v72, v72\n\tv_exp_f32 v73, v73\n\tv_smfmac_f32_32x32x32_bf16 %[o1], v[96:99], v[64:71], v128\n\tv_exp_f32 v74, v74\n\tv_exp_f32 v75, v75\n\t"
        "v_exp_f32 v76, v76\n\tv_exp_f32 v77, v77\n\tv_exp_f32 v78, v78\n\tv_exp_f32 v79, v79\n\tv_exp_f32 v48, v48\n\tv_exp_f32 v49, v49\n\t"
        "v_smfmac_f32_32x32x32_bf16 %[o1], v[112:115], v[72:79], v128\n\tv_exp_f32 v50, v50\n\tv_exp_f32 v51, v51\n\tv_exp_f32 v52, v52\n\t"
        "v_mfma_f32_32x32x16_bf16 v[64:79], v[84:87], %[q1], 0\n\tglobal_load_dwordx4 v[84:87], %[koff], s[40:41] offset:1024 sc1\n\ts_add_u32 s42, s42, 4096\n\ts_addc_u32 s43, s43, 0\n\t"
        "s_mov_b64 exec, %[vm]\n\tglobal_load_dwordx4 v[96:99], %[voff], s[42:43] offset:0 sc1\n\tglobal_load_dwordx4 v[112:115], %[voff], s[42:43] offset:512 sc1\n\ts_mov_b64 exec, -1\n\t"
        "v_exp_f32 v53, v53\n\tv_exp_f32 v54, v54\n\tv_exp_f32 v55, v55\n\tv_exp_f32 v56, v56\n\tv_exp_f32 v57, v57\n\ts_waitcnt vmcnt(9)\n\t"
        "v_smfmac_f32_32x32x32_bf16 %[o0], v[100:103], v[48:55], v128\n\tv_exp_f32 v58, v58\n\tv_exp_f32 v59, v59\n\tv_exp_f32 v60, v60\n\tv_exp_f32 v61, v61\n\tv_exp_f32 v62, v62\n\t"
        "v_exp_f32 v63, v63\n\tv_exp_f32 v64, v64\n\tv_exp_f32 v65, v65\n\tv_smfmac_f32_32x32x32_bf16 %[o0], v[116:119], v[56:63], v128\n\tv_exp_f32 v66, v66\n\tv_exp_f32 v67, v67\n\t"
        "v_exp_f32 v68, v68\n\tv_mfma_f32_32x32x16_bf16 v[48:63], v[88:91], %[q0], 0\n\tv_exp_f32 v69, v69\n\tv_exp_f32 v70, v70\n\tv_exp_f32 v71, v71\n\tv_exp_f32 v72, v72\n\t"
        "v_exp_f32 v73, v73\n\tv_smfmac_f32_32x32x32_bf16 %[o1], v[100:103], v[64:71], v128\n\tv_exp_f32 v74, v74\n\tv_exp_f32 v75, v75\n\tv_exp_f32 v76, v76\n\tv_exp_f32 v77, v77\n\t"
        "v_exp_f32 v78, v78\n\tv_exp_f32 v79, v79\n\tv_exp_f32 v48, v48\n\tv_exp_f32 v49, v49\n\tv_smfmac_f32_32x32x32_bf16 %[o1], v[116:119], v[72:79], v128\n\tv_exp_f32 v50, v50\n\t"
        "v_exp_f32 v51, v51\n\tv_exp_f32 v52, v52\n\tv_mfma_f32_32x32x16_bf16 v[64:79], v[88:91], %[q1], 0\n\tglobal_load_dwordx4 v[88:91], %[koff], s[40:41] offset:2048 sc1\n\t"
        "s_mov_b64 exec, %[vm]\n\tglobal_load_dwordx4 v[100:103], %[voff], s[42:43] offset:1024 sc1\n\tglobal_load_dwordx4 v[116:119], %[voff], s[42:43] offset:1536 sc1\n\t"
        "s_mov_b64 exec, -1\n\tv_exp_f32 v53, v53\n\tv_exp_f32 v54, v54\n\tv_exp_f32 v55, v55\n\tv_exp_f32 v56, v56\n\tv_exp_f32 v57, v57\n\ts_waitcnt vmcnt(9)\n\t"
        "v_smfmac_f32_32x32x32_bf16 %[o0], v[104:107], v[48:55], v128\n\tv_exp_f32 v58, v58\n\tv_exp_f32 v59, v59\n\tv_exp_f32 v60, v60\n\tv_exp_f32 v61, v61\n\tv_exp_f32 v62, v62\n\t"
        "v_exp_f32 v63, v63\n\tv_exp_f32 v64, v64\n\tv_exp_f32 v65, v65\n\tv_smfmac_f32_32x32x32_bf16 %[o0], v[120:123], v[56:63], v128\n\tv_exp_f32 v66, v66\n\tv_exp_f32 v67, v67\n\t"
        "v_exp_f32 v68, v68\n\tv_mfma_f32_32x32x16_bf16 v[48:63], v[92:95], %[q0], 0\n\tv_exp_f32 v69, v69\n\tv_exp_f32 v70, v70\n\tv_exp_f32 v71, v71\n\tv_exp_f32 v72, v72\n\t"
        "v_exp_f32 v73, v73\n\tv_smfmac_f32_32x32x32_bf16 %[o1], v[104:107], v[64:71], v128\n\tv_exp_f32 v74, v74\n\tv_exp_f32 v75, v75\n\tv_exp_f32 v76, v76\n\tv_exp_f32 v77, v77\n\t"
        "v_exp_f32 v78, v78\n\tv_exp_f32 v79, v79\n\tv_exp_f32 v48, v48\n\tv_exp_f32 v49, v49\n\tv_smfmac_f32_32x32x32_bf16 %[o1], v[120:123], v[72:79], v128\n\tv_exp_f32 v50, v50\n\t"
        "v_exp_f32 v51, v51\n\tv_exp_f32 v52, v52\n\tv_mfma_f32_32x32x16_bf16 v[64:79], v[92:95], %[q1], 0\n\tglobal_load_dwordx4 v[92:95], %[koff], s[40:41] offset:3072 sc1\n\t"
        "s_mov_b64 exec, %[vm]\n\tglobal_load_dwordx4 v[104:107], %[voff], s[42:43] offset:2048 sc1\n\tglobal_load_dwordx4 v[120:123], %[voff], s[42:43] offset:2560 sc1\n\t"
        "s_mov_b64 exec, -1\n\tv_exp_f32 v53, v53\n\tv_exp_f32 v54, v54\n\tv_exp_f32 v55, v55\n\tv_exp_f32 v56, v56\n\tv_exp_f32 v57, v57\n\ts_waitcnt vmcnt(9)\n\t"
        "v_smfmac_f32_32x32x32_bf16 %[o0], v[108:111], v[48:55], v128\n\tv_exp_f32 v58, v58\n\tv_exp_f32 v59, v59\n\tv_exp_f32 v60, v60\n\tv_exp_f32 v61, v61\n\tv_exp_f32 v62, v62\n\t"
        "v_exp_f32 v63, v63\n\tv_exp_f32 v64, v64\n\tv_exp_f32 v65, v65\n\tv_smfmac_f32_32x32x32_bf16 %[o0], v[124:127], v[56:63], v128\n\tv_exp_f32 v66, v66\n\tv_exp_f32 v67, v67\n\t"
        "v_exp_f32 v68, v68\n\tv_mfma_f32_32x32x16_bf16 v[48:63], v[80:83], %[q0], 0\n\tglobal_load_dword v253, v[254:255], off\n\tv_exp_f32 v69, v69\n\tv_exp_f32 v70, v70\n\t"
        "v_exp_f32 v71, v71\n\tv_exp_f32 v72, v72\n\tv_exp_f32 v73, v73\n\tv_smfmac_f32_32x32x32_bf16 %[o1], v[108:111], v[64:71], v128\n\tv_exp_f32 v74, v74\n\tv_exp_f32 v75, v75\n\t"
        "v_exp_f32 v76, v76\n\tv_exp_f32 v77, v77\n\tv_exp_f32 v78, v78\n\tv_exp_f32 v79, v79\n\tv_exp_f32 v48, v48\n\tv_exp_f32 v49, v49\n\t"
        "v_smfmac_f32_32x32x32_bf16 %[o1], v[124:127], v[72:79], v128\n\tv_exp_f32 v50, v50\n\tv_exp_f32 v51, v51\n\tv_exp_f32 v52, v52\n\t"
        "v_mfma_f32_32x32x16_bf16 v[64:79], v[80:83], %[q1], 0\n\ts_add_u32 s40, s40, 4096\n\ts_addc_u32 s41, s41, 0\n\tglobal_load_dwordx4 v[80:83], %[koff], s[40:41] offset:0 sc1\n\t"
        "s_mov_b64 exec, %[vm]\n\tglobal_load_dwordx4 v[108:111], %[voff], s[42:43] offset:3072 sc1\n\tglobal_load_dwordx4 v[124:127], %[voff], s[42:43] offset:3584 sc1\n\t"
        "s_mov_b64 exec, -1\n\tv_exp_f32 v53, v53\n\tv_exp_f32 v54, v54\n\tv_exp_f32 v55, v55\n\tv_exp_f32 v56, v56\n\tv_exp_f32 v57, v57\n\ts_waitcnt vmcnt(10)\n\t"
        "v_smfmac_f32_32x32x32_bf16 %[o0], v[96:99], v[48:55], v128\n\tv_exp_f32 v58, v58\n\tv_exp_f32 v59, v59\n\tv_exp_f32 v60, v60\n\tv_exp_f32 v61, v61\n\tv_exp_f32 v62, v62\n\t"
        "v_exp_f32 v63, v63\n\tv_exp_f32 v64, v64\n\tv_exp_f32 v65, v65\n\tv_smfmac_f32_32x32x32_bf16 %[o0], v[112:115], v[56:63], v128\n\tv_exp_f32 v66, v66\n\tv_exp_f32 v67, v67\n\t"
        "v_exp_f32 v68, v68\n\tv_mfma_f32_32x32x16_bf16 v[48:63], v[84:87], %[q0], 0\n\tv_exp_f32 v69, v69\n\tv_exp_f32 v70, v70\n\tv_exp_f32 v71, v71\n\tv_exp_f32 v72, v72\n\t"
        "v_exp_f32 v73, v73\n\tv_smfmac_f32_32x32x32_bf16 %[o1], v[96:99], v[64:71], v128\n\tv_exp_f32 v74, v74\n\tv_exp_f32 v75, v75\n\tv_exp_f32 v76, v76\n\tv_exp_f32 v77, v77\n\t"
        "v_exp_f32 v78, v78\n\tv_exp_f32 v79, v79\n\tv_exp_f32 v48, v48\n\tv_exp_f32 v49, v49\n\tv_smfmac_f32_32x32x32_bf16 %[o1], v[112:115], v[72:79], v128\n\tv_exp_f32 v50, v50\n\t"
        "v_exp_f32 v51, v51\n\tv_exp_f32 v52, v52\n\tv_mfma_f32_32x32x16_bf16 v[64:79], v[84:87], %[q1], 0\n\tglobal_load_dwordx4 v[84:87], %[koff], s[40:41] offset:1024 sc1\n\t"
        "s_add_u32 s42, s42, 4096\n\ts_addc_u32 s43, s43, 0\n\ts_mov_b64 exec, %[vm]\n\tglobal_load_dwordx4 v[96:99], %[voff], s[42:43] offset:0 sc1\n\t"
        "global_load_dwordx4 v[112:115], %[voff], s[42:43] offset:512 sc1\n\ts_mov_b64 exec, -1\n\tv_exp_f32 v53, v53\n\tv_exp_f32 v54, v54\n\tv_exp_f32 v55, v55\n\tv_exp_f32 v56, v56\n\t"
        "v_exp_f32 v57, v57\n\ts_waitcnt vmcnt(10)\n\tv_smfmac_f32_32x32x32_bf16 %[o0], v[100:103], v[48:55], v128\n\tv_exp_f32 v58, v58\n\tv_exp_f32 v59, v59\n\tv_exp_f32 v60, v60\n\t"
        "v_exp_f32 v61, v61\n\tv_exp_f32 v62, v62\n\tv_exp_f32 v63, v63\n\tv_exp_f32 v64, v64\n\tv_exp_f32 v65, v65\n\tv_smfmac_f32_32x32x32_bf16 %[o0], v[116:119], v[56:63], v128\n\t"
        "v_exp_f32 v66, v66\n\tv_exp_f32 v67, v67\n\tv_exp_f32 v68, v68\n\tv_mfma_f32_32x32x16_bf16 v[48:63], v[88:91], %[q0], 0\n\tv_exp_f32 v69, v69\n\tv_exp_f32 v70, v70\n\t"
        "v_exp_f32 v71, v71\n\tv_exp_f32 v72, v72\n\tv_exp_f32 v73, v73\n\tv_smfmac_f32_32x32x32_bf16 %[o1], v[100:103], v[64:71], v128\n\tv_exp_f32 v74, v74\n\tv_exp_f32 v75, v75\n\t"
        "v_exp_f32 v76, v76\n\tv_exp_f32 v77, v77\n\tv_exp_f32 v78, v78\n\tv_exp_f32 v79, v79\n\tv_exp_f32 v48, v48\n\tv_exp_f32 v49, v49\n\t"
        "v_smfmac_f32_32x32x32_bf16 %[o1], v[116:119], v[72:79], v128\n\tv_exp_f32 v50, v50\n\tv_exp_f32 v51, v51\n\tv_exp_f32 v52, v52\n\t"
        "v_mfma_f32_32x32x16_bf16 v[64:79], v[88:91], %[q1], 0\n\tglobal_load_dwordx4 v[88:91], %[koff], s[40:41] offset:2048 sc1\n\ts_mov_b64 exec, %[vm]\n\t"
        "global_load_dwordx4 v[100:103], %[voff], s[42:43] offset:1024 sc1\n\tglobal_load_dwordx4 v[116:119], %[voff], s[42:43] offset:1536 sc1\n\ts_mov_b64 exec, -1\n\t"
        "v_exp_f32 v53, v53\n\tv_exp_f32 v54, v54\n\tv_exp_f32 v55, v55\n\tv_exp_f32 v56, v56\n\tv_exp_f32 v57, v57\n\ts_waitcnt vmcnt(10)\n\t"
        "v_smfmac_f32_32x32x32_bf16 %[o0], v[104:107], v[48:55], v128\n\tv_exp_f32 v58, v58\n\tv_exp_f32 v59, v59\n\tv_exp_f32 v60, v60\n\tv_exp_f32 v61, v61\n\tv_exp_f32 v62, v62\n\t"
        "v_exp_f32 v63, v63\n\tv_exp_f32 v64, v64\n\tv_exp_f32 v65, v65\n\tv_smfmac_f32_32x32x32_bf16 %[o0], v[120:123], v[56:63], v128\n\tv_exp_f32 v66, v66\n\tv_exp_f32 v67, v67\n\t"
        "v_exp_f32 v68, v68\n\tv_mfma_f32_32x32x16_bf16 v[48:63], v[92:95], %[q0], 0\n\tv_exp_f32 v69, v69\n\tv_exp_f32 v70, v70\n\tv_exp_f32 v71, v71\n\tv_exp_f32 v72, v72\n\t"
        "v_exp_f32 v73, v73\n\tv_smfmac_f32_32x32x32_bf16 %[o1], v[104:107], v[64:71], v128\n\tv_exp_f32 v74, v74\n\tv_exp_f32 v75, v75\n\tv_exp_f32 v76, v76\n\tv_exp_f32 v77, v77\n\t"
        "v_exp_f32 v78, v78\n\tv_exp_f32 v79, v79\n\tv_exp_f32 v48, v48\n\tv_exp_f32 v49, v49\n\tv_smfmac_f32_32x32x32_bf16 %[o1], v[120:123], v[72:79], v128\n\tv_exp_f32 v50, v50\n\t"
        "v_exp_f32 v51, v51\n\tv_exp_f32 v52, v52\n\tv_mfma_f32_32x32x16_bf16 v[64:79], v[92:95], %[q1], 0\n\tglobal_load_dwordx4 v[92:95], %[koff], s[40:41] offset:3072 sc1\n\t"
        "s_mov_b64 exec, %[vm]\n\tglobal_load_dwordx4 v[104:107], %[voff], s[42:43] offset:2048 sc1\n\tglobal_load_dwordx4 v[120:123], %[voff], s[42:43] offset:2560 sc1\n\t"
        "s_mov_b64 exec, -1\n\tv_exp_f32 v53, v53\n\tv_exp_f32 v54, v54\n\tv_exp_f32 v55, v55\n\tv_exp_f32 v56, v56\n\tv_exp_f32 v57, v57\n\ts_waitcnt vmcnt(9)\n\t"
        "v_smfmac_f32_32x32x32_bf16 %[o0], v[108:111], v[48:55], v128\n\tv_exp_f32 v58, v58\n\tv_exp_f32 v59, v59\n\tv_exp_f32 v60, v60\n\tv_exp_f32 v61, v61\n\tv_exp_f32 v62, v62\n\t"
        "v_exp_f32 v63, v63\n\tv_exp_f32 v64, v64\n\tv_exp_f32 v65, v65\n\tv_smfmac_f32_32x32x32_bf16 %[o0], v[124:127], v[56:63], v128\n\tv_exp_f32 v66, v66\n\tv_exp_f32 v67, v67\n\t"
        "v_exp_f32 v68, v68\n\tv_mfma_f32_32x32x16_bf16 v[48:63], v[80:83], %[q0], 0\n\tv_exp_f32 v69, v69\n\tv_exp_f32 v70, v70\n\tv_exp_f32 v71, v71\n\tv_exp_f32 v72, v72\n\t"
        "v_exp_f32 v73, v73\n\tv_smfmac_f32_32x32x32_bf16 %[o1], v[108:111], v[64:71], v128\n\tv_exp_f32 v74, v74\n\tv_exp_f32 v75, v75\n\tv_exp_f32 v76, v76\n\tv_exp_f32 v77, v77\n\t"
        "v_exp_f32 v78, v78\n\tv_exp_f32 v79, v79\n\tv_exp_f32 v48, v48\n\tv_exp_f32 v49, v49\n\tv_smfmac_f32_32x32x32_bf16 %[o1], v[124:127], v[72:79], v128\n\tv_exp_f32 v50, v50\n\t"
        "v_exp_f32 v51, v51\n\tv_exp_f32 v52, v52\n\tv_mfma_f32_32x32x16_bf16 v[64:79], v[80:83], %[q1], 0\n\ts_add_u32 s40, s40, 4096\n\ts_addc_u32 s41, s41, 0\n\t"
        "global_load_dwordx4 v[80:83], %[koff], s[40:41] offset:0 sc1\n\ts_mov_b64 exec, %[vm]\n\tglobal_load_dwordx4 v[108:111], %[voff], s[42:43] offset:3072 sc1\n\t"
        "global_load_dwordx4 v[124:127], %[voff], s[42:43] offset:3584 sc1\n\ts_mov_b64 exec, -1\n\tv_exp_f32 v53, v53\n\tv_exp_f32 v54, v54\n\tv_exp_f32 v55, v55\n\tv_exp_f32 v56, v56\n\t"
        "v_exp_f32 v57, v57\n\ts_waitcnt vmcnt(9)\n\tv_smfmac_f32_32x32x32_bf16 %[o0], v[96:99], v[48:55], v128\n\tv_exp_f32 v58, v58\n\tv_exp_f32 v59, v59\n\tv_exp_f32 v60, v60\n\t"
        "v_exp_f32 v61, v61\n\tv_exp_f32 v62, v62\n\tv_exp_f32 v63, v63\n\tv_exp_f32 v64, v64\n\tv_exp_f32 v65, v65\n\tv_smfmac_f32_32x32x32_bf16 %[o0], v[112:115], v[56:63], v128\n\t"
        "v_exp_f32 v66, v66\n\tv_exp_f32 v67, v67\n\tv_exp_f32 v68, v68\n\tv_mfma_f32_32x32x16_bf16 v[48:63], v[84:87], %[q0], 0\n\tglobal_load_dwordx4 v[144:147], %[a0], s[46:47]\n\t"
        "v_exp_f32 v69, v69\n\tv_exp_f32 v70, v70\n\tv_exp_f32 v71, v71\n\tv_exp_f32 v72, v72\n\tv_exp_f32 v73, v73\n\tv_smfmac_f32_32x32x32_bf16 %[o1], v[96:99], v[64:71], v128\n\t"
        "v_exp_f32 v74, v74\n\tv_exp_f32 v75, v75\n\tv_exp_f32 v76, v76\n\tv_exp_f32 v77, v77\n\tv_exp_f32 v78, v78\n\tv_exp_f32 v79, v79\n\tv_exp_f32 v48, v48\n\tv_exp_f32 v49, v49\n\t"
        "v_smfmac_f32_32x32x32_bf16 %[o1], v[112:115], v[72:79], v128\n\tv_exp_f32 v50, v50\n\tv_exp_f32 v51, v51\n\tv_exp_f32 v52, v52\n\t"
        "v_mfma_f32_32x32x16_bf16 v[64:79], v[84:87], %[q1], 0\n\tglobal_load_dwordx4 v[84:87], %[koff], s[40:41] offset:1024 sc1\n\ts_add_u32 s42, s42, 4096\n\ts_addc_u32 s43, s43, 0\n\t"
        "s_mov_b64 exec, %[vm]\n\tglobal_load_dwordx4 v[96:99], %[voff], s[42:43] offset:0 sc1\n\tglobal_load_dwordx4 v[112:115], %[voff], s[42:43] offset:512 sc1\n\ts_mov_b64 exec, -1\n\t"
        "v_exp_f32 v53, v53\n\tv_exp_f32 v54, v54\n\tv_exp_f32 v55, v55\n\tv_exp_f32 v56, v56\n\tv_exp_f32 v57, v57\n\ts_waitcnt vmcnt(10)\n\t"
        "v_smfmac_f32_32x32x32_bf16 %[o0], v[100:103], v[48:55], v128\n\tv_exp_f32 v58, v58\n\tv_exp_f32 v59, v59\n\tv_exp_f32 v60, v60\n\tv_exp_f32 v61, v61\n\tv_exp_f32 v62, v62\n\t"
        "v_exp_f32 v63, v63\n\tv_exp_f32 v64, v64\n\tv_exp_f32 v65, v65\n\tv_smfmac_f32_32x32x32_bf16 %[o0], v[116:119], v[56:63], v128\n\tv_exp_f32 v66, v66\n\tv_exp_f32 v67, v67\n\t"
        "v_exp_f32 v68, v68\n\tv_mfma_f32_32x32x16_bf16 v[48:63], v[88:91], %[q0], 0\n\ts_add_u32 m0, %[ol], 0\n\ts_add_u32 s50, s50, 8192\n\ts_addc_u32 s51, s51, 0\n\t"
        "global_load_lds_dwordx4 %[vw], s[50:51]\n\tglobal_load_dwordx4 v[148:151], %[a1], s[46:47]\n\tv_exp_f32 v69, v69\n\tv_exp_f32 v70, v70\n\tv_exp_f32 v71, v71\n\t"
        "v_exp_f32 v72, v72\n\tv_exp_f32 v73, v73\n\tv_smfmac_f32_32x32x32_bf16 %[o1], v[100:103], v[64:71], v128\n\tv_exp_f32 v74, v74\n\tv_exp_f32 v75, v75\n\tv_exp_f32 v76, v76\n\t"
        "v_exp_f32 v77, v77\n\tv_exp_f32 v78, v78\n\tv_exp_f32 v79, v79\n\tv_exp_f32 v48, v48\n\tv_exp_f32 v49, v49\n\tv_smfmac_f32_32x32x32_bf16 %[o1], v[116:119], v[72:79], v128\n\t"
        "v_exp_f32 v50, v50\n\tv_exp_f32 v51, v51\n\tv_exp_f32 v52, v52\n\tv_mfma_f32_32x32x16_bf16 v[64:79], v[88:91], %[q1], 0\n\t"
        "global_load_dwordx4 v[88:91], %[koff], s[40:41] offset:2048 sc1\n\ts_mov_b64 exec, %[vm]\n\tglobal_load_dwordx4 v[100:103], %[voff], s[42:43] offset:1024 sc1\n\t"
        "global_load_dwordx4 v[116:119], %[voff], s[42:43] offset:1536 sc1\n\ts_mov_b64 exec, -1\n\tv_exp_f32 v53, v53\n\tv_exp_f32 v54, v54\n\tv_exp_f32 v55, v55\n\tv_exp_f32 v56, v56\n\t"
        "v_exp_f32 v57, v57\n\ts_waitcnt vmcnt(12)\n\tv_smfmac_f32_32x32x32_bf16 %[o0], v[104:107], v[48:55], v128\n\tv_exp_f32 v58, v58\n\tv_exp_f32 v59, v59\n\tv_exp_f32 v60, v60\n\t"
        "v_exp_f32 v61, v61\n\tv_exp_f32 v62, v62\n\tv_exp_f32 v63, v63\n\tv_exp_f32 v64, v64\n\tv_exp_f32 v65, v65\n\tv_smfmac_f32_32x32x32_bf16 %[o0], v[120:123], v[56:63], v128\n\t"
        "v_exp_f32 v66, v66\n\tv_exp_f32 v67, v67\n\tv_exp_f32 v68, v68\n\tv_mfma_f32_32x32x16_bf16 v[48:63], v[92:95], %[q0], 0\n\tglobal_load_dwordx4 v[152:155], %[a2], s[46:47]\n\t"
        "s_add_u32 s46, s46, %[ga]\n\ts_addc_u32 s47, s47, 0\n\ts_add_u32 m0, %[ol], 1024\n\ts_add_u32 s50, s50, 8192\n\ts_addc_u32 s51, s51, 0\n\t"
        "global_load_lds_dwordx4 %[vw], s[50:51]\n\tv_exp_f32 v69, v69\n\tv_exp_f32 v70, v70\n\tv_exp_f32 v71, v71\n\tv_exp_f32 v72, v72\n\tv_exp_f32 v73, v73\n\t"
        "v_smfmac_f32_32x32x32_bf16 %[o1], v[104:107], v[64:71], v128\n\tv_exp_f32 v74, v74\n\tv_exp_f32 v75, v75\n\tv_exp_f32 v76, v76\n\tv_exp_f32 v77, v77\n\tv_exp_f32 v78, v78\n\t"
        "v_exp_f32 v79, v79\n\tv_exp_f32 v48, v48\n\tv_exp_f32 v49, v49\n\tv_smfmac_f32_32x32x32_bf16 %[o1], v[120:123], v[72:79], v128\n\tv_exp_f32 v50, v50\n\tv_exp_f32 v51, v51\n\t"
        "v_exp_f32 v52, v52\n\tv_mfma_f32_32x32x16_bf16 v[64:79], v[92:95], %[q1], 0\n\tglobal_load_dwordx4 v[92:95], %[koff], s[40:41] offset:3072 sc1\n\ts_mov_b64 exec, %[vm]\n\t"
        "global_load_dwordx4 v[104:107], %[voff], s[42:43] offset:2048 sc1\n\tglobal_load_dwordx4 v[120:123], %[voff], s[42:43] offset:2560 sc1\n\ts_mov_b64 exec, -1\n\t"
        "v_exp_f32 v53, v53\n\tv_exp_f32 v54, v54\n\tv_exp_f32 v55, v55\n\tv_exp_f32 v56, v56\n\tv_exp_f32 v57, v57\n\ts_waitcnt vmcnt(14)\n\t"
        "v_smfmac_f32_32x32x32_bf16 %[o0], v[108:111], v[48:55], v128\n\tv_exp_f32 v58, v58\n\tv_exp_f32 v59, v59\n\tv_exp_f32 v60, v60\n\tv_exp_f32 v61, v61\n\tv_exp_f32 v62, v62\n\t"
        "v_exp_f32 v63, v63\n\tv_exp_f32 v64, v64\n\tv_exp_f32 v65, v65\n\tv_smfmac_f32_32x32x32_bf16 %[o0], v[124:127], v[56:63], v128\n\tv_exp_f32 v66, v66\n\tv_exp_f32 v67, v67\n\t"
        "v_exp_f32 v68, v68\n\tv_mfma_f32_32x32x16_bf16 v[48:63], v[80:83], %[q0], 0\n\tglobal_load_dwordx4 v[156:159], %[a0], s[46:47]\n\t"
        "global_load_dwordx4 v[160:163], %[a1], s[46:47]\n\tv_exp_f32 v69, v69\n\tv_exp_f32 v70, v70\n\tv_exp_f32 v71, v71\n\tv_exp_f32 v72, v72\n\tv_exp_f32 v73, v73\n\t"
        "v_smfmac_f32_32x32x32_bf16 %[o1], v[108:111], v[64:71], v128\n\tv_exp_f32 v74, v74\n\tv_exp_f32 v75, v75\n\tv_exp_f32 v76, v76\n\tv_exp_f32 v77, v77\n\tv_exp_f32 v78, v78\n\t"
        "v_exp_f32 v79, v79\n\tv_exp_f32 v48, v48\n\tv_exp_f32 v49, v49\n\tv_smfmac_f32_32x32x32_bf16 %[o1], v[124:127], v[72:79], v128\n\tv_exp_f32 v50, v50\n\tv_exp_f32 v51, v51\n\t"
        "v_exp_f32 v52, v52\n\tv_mfma_f32_32x32x16_bf16 v[64:79], v[80:83], %[q1], 0\n\ts_add_u32 s40, s40, 4096\n\ts_addc_u32 s41, s41, 0\n\t"
        "global_load_dwordx4 v[80:83], %[koff], s[40:41] offset:0 sc1\n\ts_mov_b64 exec, %[vm]\n\tglobal_load_dwordx4 v[108:111], %[voff], s[42:43] offset:3072 sc1\n\t"
        "global_load_dwordx4 v[124:127], %[voff], s[42:43] offset:3584 sc1\n\ts_mov_b64 exec, -1\n\tv_exp_f32 v53, v53\n\tv_exp_f32 v54, v54\n\tv_exp_f32 v55, v55\n\tv_exp_f32 v56, v56\n\t"
        "v_exp_f32 v57, v57\n\ts_waitcnt vmcnt(15)\n\tv_smfmac_f32_32x32x32_bf16 %[o0], v[96:99], v[48:55], v128\n\tv_exp_f32 v58, v58\n\tv_exp_f32 v59, v59\n\tv_exp_f32 v60, v60\n\t"
        "v_exp_f32 v61, v61\n\tv_exp_f32 v62, v62\n\tv_exp_f32 v63, v63\n\tv_exp_f32 v64, v64\n\tv_exp_f32 v65, v65\n\tv_smfmac_f32_32x32x32_bf16 %[o0], v[112:115], v[56:63], v128\n\t"
        "v_exp_f32 v66, v66\n\tv_exp_f32 v67, v67\n\tv_exp_f32 v68, v68\n\tv_mfma_f32_32x32x16_bf16 v[48:63], v[84:87], %[q0], 0\n\ts_add_u32 m0, %[ol], 2048\n\ts_add_u32 s50, s50, 8192\n\t"
        "s_addc_u32 s51, s51, 0\n\tglobal_load_lds_dwordx4 %[vw], s[50:51]\n\tglobal_load_dwordx4 v[164:167], %[a2], s[46:47]\n\ts_add_u32 s46, s46, %[ga]\n\ts_addc_u32 s47, s47, 0\n\t"
        "v_exp_f32 v69, v69\n\tv_exp_f32 v70, v70\n\tv_exp_f32 v71, v71\n\tv_exp_f32 v72, v72\n\tv_exp_f32 v73, v73\n\tv_smfmac_f32_32x32x32_bf16 %[o1], v[96:99], v[64:71], v128\n\t"
        "v_exp_f32 v74, v74\n\tv_exp_f32 v75, v75\n\tv_exp_f32 v76, v76\n\tv_exp_f32 v77, v77\n\tv_exp_f32 v78, v78\n\tv_exp_f32 v79, v79\n\tv_exp_f32 v48, v48\n\tv_exp_f32 v49, v49\n\t"
        "v_smfmac_f32_32x32x32_bf16 %[o1], v[112:115], v[72:79], v128\n\tv_exp_f32 v50, v50\n\tv_exp_f32 v51, v51\n\tv_exp_f32 v52, v52\n\t"
        "v_mfma_f32_32x32x16_bf16 v[64:79], v[84:87], %[q1], 0\n\tglobal_load_dwordx4 v[84:87], %[koff], s[40:41] offset:1024 sc1\n\ts_add_u32 s42, s42, 4096\n\ts_addc_u32 s43, s43, 0\n\t"
        "s_mov_b64 exec, %[vm]\n\tglobal_load_dwordx4 v[96:99], %[voff], s[42:43] offset:0 sc1\n\tglobal_load_dwordx4 v[112:115], %[voff], s[42:43] offset:512 sc1\n\ts_mov_b64 exec, -1\n\t"
        "v_exp_f32 v53, v53\n\tv_exp_f32 v54, v54\n\tv_exp_f32 v55, v55\n\tv_exp_f32 v56, v56\n\tv_exp_f32 v57, v57\n\ts_waitcnt vmcnt(15)\n\t"
        "v_smfmac_f32_32x32x32_bf16 %[o0], v[100:103], v[48:55], v128\n\tv_exp_f32 v58, v58\n\tv_exp_f32 v59, v59\n\tv_exp_f32 v60, v60\n\tv_exp_f32 v61, v61\n\tv_exp_f32 v62, v62\n\t"
        "v_exp_f32 v63, v63\n\tv_exp_f32 v64, v64\n\tv_exp_f32 v65, v65\n\tv_smfmac_f32_32x32x32_bf16 %[o0], v[116:119], v[56:63], v128\n\tv_exp_f32 v66, v66\n\tv_exp_f32 v67, v67\n\t"
        "v_exp_f32 v68, v68\n\tv_mfma_f32_32x32x16_bf16 v[48:63], v[88:91], %[q0], 0\n\tglobal_load_dwordx4 v[168:171], %[a0], s[46:47]\n\ts_add_u32 m0, %[ol], 3072\n\t"
        "s_add_u32 s50, s50, 8192\n\ts_addc_u32 s51, s51, 0\n\tglobal_load_lds_dwordx4 %[vw], s[50:51]\n\tv_exp_f32 v69, v69\n\tv_exp_f32 v70, v70\n\tv_exp_f32 v71, v71\n\t"
        "v_exp_f32 v72, v72\n\tv_exp_f32 v73, v73\n\tv_smfmac_f32_32x32x32_bf16 %[o1], v[100:103], v[64:71], v128\n\tv_exp_f32 v74, v74\n\tv_exp_f32 v75, v75\n\tv_exp_f32 v76, v76\n\t"
        "v_exp_f32 v77, v77\n\tv_exp_f32 v78, v78\n\tv_exp_f32 v79, v79\n\tv_exp_f32 v48, v48\n\tv_exp_f32 v49, v49\n\tv_smfmac_f32_32x32x32_bf16 %[o1], v[116:119], v[72:79], v128\n\t"
        "v_exp_f32 v50, v50\n\tv_exp_f32 v51, v51\n\tv_exp_f32 v52, v52\n\tv_mfma_f32_32x32x16_bf16 v[64:79], v[88:91], %[q1], 0\n\t"
        "global_load_dwordx4 v[88:91], %[koff], s[40:41] offset:2048 sc1\n\ts_mov_b64 exec, %[vm]\n\tglobal_load_dwordx4 v[100:103], %[voff], s[42:43] offset:1024 sc1\n\t"
        "global_load_dwordx4 v[116:119], %[voff], s[42:43] offset:1536 sc1\n\ts_mov_b64 exec, -1\n\tv_exp_f32 v53, v53\n\tv_exp_f32 v54, v54\n\tv_exp_f32 v55, v55\n\tv_exp_f32 v56, v56\n\t"
        "v_exp_f32 v57, v57\n\ts_waitcnt vmcnt(15)\n\tv_smfmac_f32_32x32x32_bf16 %[o0], v[104:107], v[48:55], v128\n\tv_exp_f32 v58, v58\n\tv_exp_f32 v59, v59\n\tv_exp_f32 v60, v60\n\t"
        "v_exp_f32 v61, v61\n\tv_exp_f32 v62, v62\n\tv_exp_f32 v63, v63\n\tv_exp_f32 v64, v64\n\tv_exp_f32 v65, v65\n\tv_smfmac_f32_32x32x32_bf16 %[o0], v[120:123], v[56:63], v128\n\t"
        "v_exp_f32 v66, v66\n\tv_exp_f32 v67, v67\n\tv_exp_f32 v68, v68\n\tv_mfma_f32_32x32x16_bf16 v[48:63], v[92:95], %[q0], 0\n\tglobal_load_dwordx4 v[172:175], %[a1], s[46:47]\n\t"
        "global_load_dwordx4 v[176:179], %[a2], s[46:47]\n\ts_add_u32 s46, s46, %[ga]\n\ts_addc_u32 s47, s47, 0\n\tv_exp_f32 v69, v69\n\tv_exp_f32 v70, v70\n\tv_exp_f32 v71, v71\n\t"
        "v_exp_f32 v72, v72\n\tv_exp_f32 v73, v73\n\tv_smfmac_f32_32x32x32_bf16 %[o1], v[104:107], v[64:71], v128\n\tv_exp_f32 v74, v74\n\tv_exp_f32 v75, v75\n\tv_exp_f32 v76, v76\n\t"
        "v_exp_f32 v77, v77\n\tv_exp_f32 v78, v78\n\tv_exp_f32 v79, v79\n\tv_exp_f32 v48, v48\n\tv_exp_f32 v49, v49\n\tv_smfmac_f32_32x32x32_bf16 %[o1], v[120:123], v[72:79], v128\n\t"
        "v_exp_f32 v50, v50\n\tv_exp_f32 v51, v51\n\tv_exp_f32 v52, v52\n\tv_mfma_f32_32x32x16_bf16 v[64:79], v[92:95], %[q1], 0\n\t"
        "global_load_dwordx4 v[92:95], %[koff], s[40:41] offset:3072 sc1\n\ts_mov_b64 exec, %[vm]\n\tglobal_load_dwordx4 v[104:107], %[voff], s[42:43] offset:2048 sc1\n\t"
        "global_load_dwordx4 v[120:123], %[voff], s[42:43] offset:2560 sc1\n\ts_mov_b64 exec, -1\n\tv_exp_f32 v53, v53\n\tv_exp_f32 v54, v54\n\tv_exp_f32 v55, v55\n\tv_exp_f32 v56, v56\n\t"
        "v_exp_f32 v57, v57\n\ts_waitcnt vmcnt(15)\n\tv_smfmac_f32_32x32x32_bf16 %[o0], v[108:111], v[48:55], v128\n\tv_exp_f32 v58, v58\n\tv_exp_f32 v59, v59\n\tv_exp_f32 v60, v60\n\t"
        "v_exp_f32 v61, v61\n\tv_exp_f32 v62, v62\n\tv_exp_f32 v63, v63\n\tv_exp_f32 v64, v64\n\tv_exp_f32 v65, v65\n\tv_smfmac_f32_32x32x32_bf16 %[o0], v[124:127], v[56:63], v128\n\t"
        "v_exp_f32 v66, v66\n\tv_exp_f32 v67, v67\n\tv_exp_f32 v68, v68\n\tv_mfma_f32_32x32x16_bf16 v[48:63], v[80:83], %[q0], 0\n\ts_add_u32 m0, %[ol], 4096\n\ts_add_u32 s50, s50, 8192\n\t"
        "s_addc_u32 s51, s51, 0\n\tglobal_load_lds_dwordx4 %[vw], s[50:51]\n\tglobal_load_dwordx4 v[180:183], %[a0], s[46:47]\n\tv_exp_f32 v69, v69\n\tv_exp_f32 v70, v70\n\t"
        "v_exp_f32 v71, v71\n\tv_exp_f32 v72, v72\n\tv_exp_f32 v73, v73\n\tv_smfmac_f32_32x32x32_bf16 %[o1], v[108:111], v[64:71], v128\n\tv_exp_f32 v74, v74\n\tv_exp_f32 v75, v75\n\t"
        "v_exp_f32 v76, v76\n\tv_exp_f32 v77, v77\n\tv_exp_f32 v78, v78\n\tv_exp_f32 v79, v79\n\tv_exp_f32 v48, v48\n\tv_exp_f32 v49, v49\n\t"
        "v_smfmac_f32_32x32x32_bf16 %[o1], v[124:127], v[72:79], v128\n\tv_exp_f32 v50, v50\n\tv_exp_f32 v51, v51\n\tv_exp_f32 v52, v52\n\t"
        "v_mfma_f32_32x32x16_bf16 v[64:79], v[80:83], %[q1], 0\n\ts_add_u32 s40, s40, 4096\n\ts_addc_u32 s41, s41, 0\n\tglobal_load_dwordx4 v[80:83], %[koff], s[40:41] offset:0 sc1\n\t"
        "s_mov_b64 exec, %[vm]\n\tglobal_load_dwordx4 v[108:111], %[voff], s[42:43] offset:3072 sc1\n\tglobal_load_dwordx4 v[124:127], %[voff], s[42:43] offset:3584 sc1\n\t"
        "s_mov_b64 exec, -1\n\tv_exp_f32 v53, v53\n\tv_exp_f32 v54, v54\n\tv_exp_f32 v55, v55\n\tv_exp_f32 v56, v56\n\tv_exp_f32 v57, v57\n\ts_waitcnt vmcnt(15)\n\t"
        "v_smfmac_f32_32x32x32_bf16 %[o0], v[96:99], v[48:55], v128\n\tv_exp_f32 v58, v58\n\tv_exp_f32 v59, v59\n\tv_exp_f32 v60, v60\n\tv_exp_f32 v61, v61\n\tv_exp_f32 v62, v62\n\t"
        "v_exp_f32 v63, v63\n\tv_exp_f32 v64, v64\n\tv_exp_f32 v65, v65\n\tv_smfmac_f32_32x32x32_bf16 %[o0], v[112:115], v[56:63], v128\n\tv_exp_f32 v66, v66\n\tv_exp_f32 v67, v67\n\t"
        "v_exp_f32 v68, v68\n\tv_mfma_f32_32x32x16_bf16 v[48:63], v[84:87], %[q0], 0\n\tglobal_load_dwordx4 v[184:187], %[a1], s[46:47]\n\ts_add_u32 m0, %[ol], 5120\n\t"
        "s_add_u32 s50, s50, 8192\n\ts_addc_u32 s51, s51, 0\n\tglobal_load_lds_dwordx4 %[vw], s[50:51]\n\tv_exp_f32 v69, v69\n\tv_exp_f32 v70, v70\n\tv_exp_f32 v71, v71\n\t"
        "v_exp_f32 v72, v72\n\tv_exp_f32 v73, v73\n\tv_smfmac_f32_32x32x32_bf16 %[o1], v[96:99], v[64:71], v128\n\tv_exp_f32 v74, v74\n\tv_exp_f32 v75, v75\n\tv_exp_f32 v76, v76\n\t"
        "v_exp_f32 v77, v77\n\tv_exp_f32 v78, v78\n\tv_exp_f32 v79, v79\n\tv_exp_f32 v48, v48\n\tv_exp_f32 v49, v49\n\tv_smfmac_f32_32x32x32_bf16 %[o1], v[112:115], v[72:79], v128\n\t"
        "v_exp_f32 v50, v50\n\tv_exp_f32 v51, v51\n\tv_exp_f32 v52, v52\n\tv_mfma_f32_32x32x16_bf16 v[64:79], v[84:87], %[q1], 0\n\t"
        "global_load_dwordx4 v[84:87], %[koff], s[40:41] offset:1024 sc1\n\ts_add_u32 s42, s42, 4096\n\ts_addc_u32 s43, s43, 0\n\ts_mov_b64 exec, %[vm]\n\t"
        "global_load_dwordx4 v[96:99], %[voff], s[42:43] offset:0 sc1\n\tglobal_load_dwordx4 v[112:115], %[voff], s[42:43] offset:512 sc1\n\ts_mov_b64 exec, -1\n\tv_exp_f32 v53, v53\n\t"
        "v_exp_f32 v54, v54\n\tv_exp_f32 v55, v55\n\tv_exp_f32 v56, v56\n\tv_exp_f32 v57, v57\n\ts_waitcnt vmcnt(15)\n\tv_smfmac_f32_32x32x32_bf16 %[o0], v[100:103], v[48:55], v128\n\t"
        "v_exp_f32 v58, v58\n\tv_exp_f32 v59, v59\n\tv_exp_f32 v60, v60\n\tv_exp_f32 v61, v61\n\tv_exp_f32 v62, v62\n\tv_exp_f32 v63, v63\n\tv_exp_f32 v64, v64\n\tv_exp_f32 v65, v65\n\t"
        "v_smfmac_f32_32x32x32_bf16 %[o0], v[116:119], v[56:63], v128\n\tv_exp_f32 v66, v66\n\tv_exp_f32 v67, v67\n\tv_exp_f32 v68, v68\n\t"
        "v_mfma_f32_32x32x16_bf16 v[48:63], v[88:91], %[q0], 0\n\tglobal_load_dwordx4 v[188:191], %[a2], s[46:47]\n\ts_add_u32 s46, s46, %[ga]\n\ts_addc_u32 s47, s47, 0\n\t"
        "global_load_dwordx4 v[192:195], %[vw], s[48:49]\n\ts_add_u32 s48, s48, 8192\n\ts_addc_u32 s49, s49, 0\n\tv_exp_f32 v69, v69\n\tv_exp_f32 v70, v70\n\tv_exp_f32 v71, v71\n\t"
        "v_exp_f32 v72, v72\n\tv_exp_f32 v73, v73\n\tv_smfmac_f32_32x32x32_bf16 %[o1], v[100:103], v[64:71], v128\n\tv_exp_f32 v74, v74\n\tv_exp_f32 v75, v75\n\tv_exp_f32 v76, v76\n\t"
        "v_exp_f32 v77, v77\n\tv_exp_f32 v78, v78\n\tv_exp_f32 v79, v79\n\tv_exp_f32 v48, v48\n\tv_exp_f32 v49, v49\n\tv_smfmac_f32_32x32x32_bf16 %[o1], v[116:119], v[72:79], v128\n\t"
        "v_exp_f32 v50, v50\n\tv_exp_f32 v51, v51\n\tv_exp_f32 v52, v52\n\tv_mfma_f32_32x32x16_bf16 v[64:79], v[88:91], %[q1], 0\n\t"
        "global_load_dwordx4 v[88:91], %[koff], s[40:41] offset:2048 sc1\n\ts_mov_b64 exec, %[vm]\n\tglobal_load_dwordx4 v[100:103], %[voff], s[42:43] offset:1024 sc1\n\t"
        "global_load_dwordx4 v[116:119], %[voff], s[42:43] offset:1536 sc1\n\ts_mov_b64 exec, -1\n\tv_exp_f32 v53, v53\n\tv_exp_f32 v54, v54\n\tv_exp_f32 v55, v55\n\tv_exp_f32 v56, v56\n\t"
        "v_exp_f32 v57, v57\n\ts_waitcnt vmcnt(15)\n\tv_smfmac_f32_32x32x32_bf16 %[o0], v[104:107], v[48:55], v128\n\tv_exp_f32 v58, v58\n\tv_exp_f32 v59, v59\n\tv_exp_f32 v60, v60\n\t"
        "v_exp_f32 v61, v61\n\tv_exp_f32 v62, v62\n\tv_exp_f32 v63, v63\n\tv_exp_f32 v64, v64\n\tv_exp_f32 v65, v65\n\tv_smfmac_f32_32x32x32_bf16 %[o0], v[120:123], v[56:63], v128\n\t"
        "v_exp_f32 v66, v66\n\tv_exp_f32 v67, v67\n\tv_exp_f32 v68, v68\n\tv_mfma_f32_32x32x16_bf16 v[48:63], v[92:95], %[q0], 0\n\ts_add_u32 m0, %[ol], 6144\n\ts_add_u32 s50, s50, 8192\n\t"
        "s_addc_u32 s51, s51, 0\n\tglobal_load_lds_dwordx4 %[vw], s[50:51]\n\tglobal_load_dwordx4 v[196:199], %[vw], s[48:49]\n\ts_add_u32 s48, s48, 8192\n\ts_addc_u32 s49, s49, 0\n\t"
        "v_exp_f32 v69, v69\n\tv_exp_f32 v70, v70\n\tv_exp_f32 v71, v71\n\tv_exp_f32 v72, v72\n\tv_exp_f32 v73, v73\n\tv_smfmac_f32_32x32x32_bf16 %[o1], v[104:107], v[64:71], v128\n\t"
        "v_exp_f32 v74, v74\n\tv_exp_f32 v75, v75\n\tv_exp_f32 v76, v76\n\tv_exp_f32 v77, v77\n\tv_exp_f32 v78, v78\n\tv_exp_f32 v79, v79\n\tv_exp_f32 v48, v48\n\tv_exp_f32 v49, v49\n\t"
        "v_smfmac_f32_32x32x32_bf16 %[o1], v[120:123], v[72:79], v128\n\tv_exp_f32 v50, v50\n\tv_exp_f32 v51, v51\n\tv_exp_f32 v52, v52\n\t"
        "v_mfma_f32_32x32x16_bf16 v[64:79], v[92:95], %[q1], 0\n\tglobal_load_dwordx4 v[92:95], %[koff], s[40:41] offset:3072 sc1\n\ts_mov_b64 exec, %[vm]\n\t"
        "global_load_dwordx4 v[104:107], %[voff], s[42:43] offset:2048 sc1\n\tglobal_load_dwordx4 v[120:123], %[voff], s[42:43] offset:2560 sc1\n\ts_mov_b64 exec, -1\n\t"
        "v_exp_f32 v53, v53\n\tv_exp_f32 v54, v54\n\tv_exp_f32 v55, v55\n\tv_exp_f32 v56, v56\n\tv_exp_f32 v57, v57\n\ts_waitcnt vmcnt(15)\n\t"
        "v_smfmac_f32_32x32x32_bf16 %[o0], v[108:111], v[48:55], v128\n\tv_exp_f32 v58, v58\n\tv_exp_f32 v59, v59\n\tv_exp_f32 v60, v60\n\tv_exp_f32 v61, v61\n\tv_exp_f32 v62, v62\n\t"
        "v_exp_f32 v63, v63\n\tv_exp_f32 v64, v64\n\tv_exp_f32 v65, v65\n\tv_smfmac_f32_32x32x32_bf16 %[o0], v[124:127], v[56:63], v128\n\tv_exp_f32 v66, v66\n\tv_exp_f32 v67, v67\n\t"
        "v_exp_f32 v68, v68\n\tv_mfma_f32_32x32x16_bf16 v[48:63], v[80:83], %[q0], 0\n\tglobal_load_dwordx4 v[200:203], %[vw], s[48:49]\n\ts_add_u32 s48, s48, 8192\n\t"
        "s_addc_u32 s49, s49, 0\n\tv_exp_f32 v69, v69\n\tv_exp_f32 v70, v70\n\tv_exp_f32 v71, v71\n\tv_exp_f32 v72, v72\n\tv_exp_f32 v73, v73\n\t"
        "v_smfmac_f32_32x32x32_bf16 %[o1], v[108:111], v[64:71], v128\n\tv_exp_f32 v74, v74\n\tv_exp_f32 v75, v75\n\tv_exp_f32 v76, v76\n\tv_exp_f32 v77, v77\n\tv_exp_f32 v78, v78\n\t"
        "v_exp_f32 v79, v79\n\tv_exp_f32 v48, v48\n\tv_exp_f32 v49, v49\n\tv_smfmac_f32_32x32x32_bf16 %[o1], v[124:127], v[72:79], v128\n\tv_exp_f32 v50, v50\n\tv_exp_f32 v51, v51\n\t"
        "v_exp_f32 v52, v52\n\tv_mfma_f32_32x32x16_bf16 v[64:79], v[80:83], %[q1], 0\n\ts_add_u32 s40, s40, 4096\n\ts_addc_u32 s41, s41, 0\n\t"
        "global_load_dwordx4 v[80:83], %[koff], s[40:41] offset:0 sc1\n\ts_mov_b64 exec, %[vm]\n\tglobal_load_dwordx4 v[108:111], %[voff], s[42:43] offset:3072 sc1\n\t"
        "global_load_dwordx4 v[124:127], %[voff], s[42:43] offset:3584 sc1\n\ts_mov_b64 exec, -1\n\tv_exp_f32 v53, v53\n\tv_exp_f32 v54, v54\n\tv_exp_f32 v55, v55\n\tv_exp_f32 v56, v56\n\t"
        "v_exp_f32 v57, v57\n\ts_waitcnt vmcnt(14)\n\tv_smfmac_f32_32x32x32_bf16 %[o0], v[96:99], v[48:55], v128\n\tv_exp_f32 v58, v58\n\tv_exp_f32 v59, v59\n\tv_exp_f32 v60, v60\n\t"
        "v_exp_f32 v61, v61\n\tv_exp_f32 v62, v62\n\tv_exp_f32 v63, v63\n\tv_exp_f32 v64, v64\n\tv_exp_f32 v65, v65\n\tv_smfmac_f32_32x32x32_bf16 %[o0], v[112:115], v[56:63], v128\n\t"
        "v_exp_f32 v66, v66\n\tv_exp_f32 v67, v67\n\tv_exp_f32 v68, v68\n\tv_mfma_f32_32x32x16_bf16 v[48:63], v[84:87], %[q0], 0\n\ts_add_u32 m0, %[ol], 7168\n\ts_add_u32 s50, s50, 8192\n\t"
        "s_addc_u32 s51, s51, 0\n\tglobal_load_lds_dwordx4 %[vw], s[50:51]\n\tglobal_load_dwordx4 v[204:207], %[vw], s[48:49]\n\ts_add_u32 s48, s48, 8192\n\ts_addc_u32 s49, s49, 0\n\t"
        "v_exp_f32 v69, v69\n\tv_exp_f32 v70, v70\n\tv_exp_f32 v71, v71\n\tv_exp_f32 v72, v72\n\tv_exp_f32 v73, v73\n\tv_smfmac_f32_32x32x32_bf16 %[o1], v[96:99], v[64:71], v128\n\t"
        "v_exp_f32 v74, v74\n\tv_exp_f32 v75, v75\n\tv_exp_f32 v76, v76\n\tv_exp_f32 v77, v77\n\tv_exp_f32 v78, v78\n\tv_exp_f32 v79, v79\n\tv_exp_f32 v48, v48\n\tv_exp_f32 v49, v49\n\t"
        "v_smfmac_f32_32x32x32_bf16 %[o1], v[112:115], v[72:79], v128\n\tv_exp_f32 v50, v50\n\tv_exp_f32 v51, v51\n\tv_exp_f32 v52, v52\n\t"
        "v_mfma_f32_32x32x16_bf16 v[64:79], v[84:87], %[q1], 0\n\tglobal_load_dwordx4 v[84:87], %[koff], s[40:41] offset:1024 sc1\n\ts_add_u32 s42, s42, 4096\n\ts_addc_u32 s43, s43, 0\n\t"
        "s_mov_b64 exec, %[vm]\n\tglobal_load_dwordx4 v[96:99], %[voff], s[42:43] offset:0 sc1\n\tglobal_load_dwordx4 v[112:115], %[voff], s[42:43] offset:512 sc1\n\ts_mov_b64 exec, -1\n\t"
        "v_exp_f32 v53, v53\n\tv_exp_f32 v54, v54\n\tv_exp_f32 v55, v55\n\tv_exp_f32 v56, v56\n\tv_exp_f32 v57, v57\n\ts_waitcnt vmcnt(14)\n\t"
        "v_smfmac_f32_32x32x32_bf16 %[o0], v[100:103], v[48:55], v128\n\tv_exp_f32 v58, v58\n\tv_exp_f32 v59, v59\n\tv_exp_f32 v60, v60\n\tv_exp_f32 v61, v61\n\tv_exp_f32 v62, v62\n\t"
        "v_exp_f32 v63, v63\n\tv_exp_f32 v64, v64\n\tv_exp_f32 v65, v65\n\tv_smfmac_f32_32x32x32_bf16 %[o0], v[116:119], v[56:63], v128\n\tv_exp_f32 v66, v66\n\tv_exp_f32 v67, v67\n\t"
        "v_exp_f32 v68, v68\n\tv_mfma_f32_32x32x16_bf16 v[48:63], v[88:91], %[q0], 0\n\tglobal_load_dwordx4 v[208:211], %[vw], s[48:49]\n\ts_add_u32 s48, s48, 8192\n\t"
        "s_addc_u32 s49, s49, 0\n\ts_add_u32 m0, %[hl], 0\n\ts_nop 0\n\tglobal_load_lds_dwordx4 %[b0], s[52:53]\n\tv_exp_f32 v69, v69\n\tv_exp_f32 v70, v70\n\tv_exp_f32 v71, v71\n\t"
        "v_exp_f32 v72, v72\n\tv_exp_f32 v73, v73\n\tv_smfmac_f32_32x32x32_bf16 %[o1], v[100:103], v[64:71], v128\n\tv_exp_f32 v74, v74\n\tv_exp_f32 v75, v75\n\tv_exp_f32 v76, v76\n\t"
        "v_exp_f32 v77, v77\n\tv_exp_f32 v78, v78\n\tv_exp_f32 v79, v79\n\tv_exp_f32 v48, v48\n\tv_exp_f32 v49, v49\n\tv_smfmac_f32_32x32x32_bf16 %[o1], v[116:119], v[72:79], v128\n\t"
        "v_exp_f32 v50, v50\n\tv_exp_f32 v51, v51\n\tv_exp_f32 v52, v52\n\tv_mfma_f32_32x32x16_bf16 v[64:79], v[88:91], %[q1], 0\n\t"
        "global_load_dwordx4 v[88:91], %[koff], s[40:41] offset:2048 sc1\n\ts_mov_b64 exec, %[vm]\n\tglobal_load_dwordx4 v[100:103], %[voff], s[42:43] offset:1024 sc1\n\t"
        "global_load_dwordx4 v[116:119], %[voff], s[42:43] offset:1536 sc1\n\ts_mov_b64 exec, -1\n\tv_exp_f32 v53, v53\n\tv_exp_f32 v54, v54\n\tv_exp_f32 v55, v55\n\tv_exp_f32 v56, v56\n\t"
        "v_exp_f32 v57, v57\n\ts_waitcnt vmcnt(14)\n\tv_smfmac_f32_32x32x32_bf16 %[o0], v[104:107], v[48:55], v128\n\tv_exp_f32 v58, v58\n\tv_exp_f32 v59, v59\n\tv_exp_f32 v60, v60\n\t"
        "v_exp_f32 v61, v61\n\tv_exp_f32 v62, v62\n\tv_exp_f32 v63, v63\n\tv_exp_f32 v64, v64\n\tv_exp_f32 v65, v65\n\tv_smfmac_f32_32x32x32_bf16 %[o0], v[120:123], v[56:63], v128\n\t"
        "v_exp_f32 v66, v66\n\tv_exp_f32 v67, v67\n\tv_exp_f32 v68, v68\n\tv_mfma_f32_32x32x16_bf16 v[48:63], v[92:95], %[q0], 0\n\tglobal_load_dwordx4 v[212:215], %[vw], s[48:49]\n\t"
        "s_add_u32 s48, s48, 8192\n\ts_addc_u32 s49, s49, 0\n\tglobal_load_dwordx4 v[216:219], %[vw], s[48:49]\n\ts_add_u32 s48, s48, 8192\n\ts_addc_u32 s49, s49, 0\n\t"
        "v_exp_f32 v69, v69\n\tv_exp_f32 v70, v70\n\tv_exp_f32 v71, v71\n\tv_exp_f32 v72, v72\n\tv_exp_f32 v73, v73\n\tv_smfmac_f32_32x32x32_bf16 %[o1], v[104:107], v[64:71], v128\n\t"
        "v_exp_f32 v74, v74\n\tv_exp_f32 v75, v75\n\tv_exp_f32 v76, v76\n\tv_exp_f32 v77, v77\n\tv_exp_f32 v78, v78\n\tv_exp_f32 v79, v79\n\tv_exp_f32 v48, v48\n\tv_exp_f32 v49, v49\n\t"
        "v_smfmac_f32_32x32x32_bf16 %[o1], v[120:123], v[72:79], v128\n\tv_exp_f32 v50, v50\n\tv_exp_f32 v51, v51\n\tv_exp_f32 v52, v52\n\t"
        "v_mfma_f32_32x32x16_bf16 v[64:79], v[92:95], %[q1], 0\n\tglobal_load_dwordx4 v[92:95], %[koff], s[40:41] offset:3072 sc1\n\ts_mov_b64 exec, %[vm]\n\t"
        "global_load_dwordx4 v[104:107], %[voff], s[42:43] offset:2048 sc1\n\tglobal_load_dwordx4 v[120:123], %[voff], s[42:43] offset:2560 sc1\n\ts_mov_b64 exec, -1\n\t"
        "v_exp_f32 v53, v53\n\tv_exp_f32 v54, v54\n\tv_exp_f32 v55, v55\n\tv_exp_f32 v56, v56\n\tv_exp_f32 v57, v57\n\ts_waitcnt vmcnt(15)\n\t"
        "v_smfmac_f32_32x32x32_bf16 %[o0], v[108:111], v[48:55], v128\n\tv_exp_f32 v58, v58\n\tv_exp_f32 v59, v59\n\tv_exp_f32 v60, v60\n\tv_exp_f32 v61, v61\n\tv_exp_f32 v62, v62\n\t"
        "v_exp_f32 v63, v63\n\tv_exp_f32 v64, v64\n\tv_exp_f32 v65, v65\n\tv_smfmac_f32_32x32x32_bf16 %[o0], v[124:127], v[56:63], v128\n\tv_exp_f32 v66, v66\n\tv_exp_f32 v67, v67\n\t"
        "v_exp_f32 v68, v68\n\tv_mfma_f32_32x32x16_bf16 v[48:63], v[80:83], %[q0], 0\n\ts_add_u32 m0, %[hl], 1024\n\ts_nop 0\n\tglobal_load_lds_dwordx4 %[b1], s[52:53]\n\t"
        "global_load_dwordx4 v[220:223], %[vw], s[48:49]\n\ts_add_u32 s48, s48, 8192\n\ts_addc_u32 s49, s49, 0\n\tv_exp_f32 v69, v69\n\tv_exp_f32 v70, v70\n\tv_exp_f32 v71, v71\n\t"
        "v_exp_f32 v72, v72\n\tv_exp_f32 v73, v73\n\tv_smfmac_f32_32x32x32_bf16 %[o1], v[108:111], v[64:71], v128\n\tv_exp_f32 v74, v74\n\tv_exp_f32 v75, v75\n\tv_exp_f32 v76, v76\n\t"
        "v_exp_f32 v77, v77\n\tv_exp_f32 v78, v78\n\tv_exp_f32 v79, v79\n\tv_exp_f32 v48, v48\n\tv_exp_f32 v49, v49\n\tv_smfmac_f32_32x32x32_bf16 %[o1], v[124:127], v[72:79], v128\n\t"
        "v_exp_f32 v50, v50\n\tv_exp_f32 v51, v51\n\tv_exp_f32 v52, v52\n\tv_mfma_f32_32x32x16_bf16 v[64:79], v[80:83], %[q1], 0\n\ts_add_u32 s40, s40, 4096\n\ts_addc_u32 s41, s41, 0\n\t"
        "global_load_dwordx4 v[80:83], %[koff], s[40:41] offset:0 sc1\n\ts_mov_b64 exec, %[vm]\n\tglobal_load_dwordx4 v[108:111], %[voff], s[42:43] offset:3072 sc1\n\t"
        "global_load_dwordx4 v[124:127], %[voff], s[42:43] offset:3584 sc1\n\ts_mov_b64 exec, -1\n\tv_exp_f32 v53, v53\n\tv_exp_f32 v54, v54\n\tv_exp_f32 v55, v55\n\tv_exp_f32 v56, v56\n\t"
        "v_exp_f32 v57, v57\n\ts_waitcnt vmcnt(15)\n\tv_smfmac_f32_32x32x32_bf16 %[o0], v[96:99], v[48:55], v128\n\tv_exp_f32 v58, v58\n\tv_exp_f32 v59, v59\n\tv_exp_f32 v60, v60\n\t"
        "v_exp_f32 v61, v61\n\tv_exp_f32 v62, v62\n\tv_exp_f32 v63, v63\n\tv_exp_f32 v64, v64\n\tv_exp_f32 v65, v65\n\tv_smfmac_f32_32x32x32_bf16 %[o0], v[112:115], v[56:63], v128\n\t"
        "v_exp_f32 v66, v66\n\tv_exp_f32 v67, v67\n\tv_exp_f32 v68, v68\n\tv_mfma_f32_32x32x16_bf16 v[48:63], v[84:87], %[q0], 0\n\tglobal_load_dwordx4 v[224:227], %[vw], s[48:49]\n\t"
        "s_add_u32 s48, s48, 8192\n\ts_addc_u32 s49, s49, 0\n\ts_add_u32 m0, %[hl], 2048\n\ts_nop 0\n\tglobal_load_lds_dwordx4 %[b2], s[52:53]\n\tv_exp_f32 v69, v69\n\t"
        "v_exp_f32 v70, v70\n\tv_exp_f32 v71, v71\n\tv_exp_f32 v72, v72\n\tv_exp_f32 v73, v73\n\tv_smfmac_f32_32x32x32_bf16 %[o1], v[96:99], v[64:71], v128\n\tv_exp_f32 v74, v74\n\t"
        "v_exp_f32 v75, v75\n\tv_exp_f32 v76, v76\n\tv_exp_f32 v77, v77\n\tv_exp_f32 v78, v78\n\tv_exp_f32 v79, v79\n\tv_exp_f32 v48, v48\n\tv_exp_f32 v49, v49\n\t"
        "v_smfmac_f32_32x32x32_bf16 %[o1], v[112:115], v[72:79], v128\n\tv_exp_f32 v50, v50\n\tv_exp_f32 v51, v51\n\tv_exp_f32 v52, v52\n\t"
        "v_mfma_f32_32x32x16_bf16 v[64:79], v[84:87], %[q1], 0\n\tglobal_load_dwordx4 v[84:87], %[koff], s[40:41] offset:1024 sc1\n\ts_add_u32 s42, s42, 4096\n\ts_addc_u32 s43, s43, 0\n\t"
        "s_mov_b64 exec, %[vm]\n\tglobal_load_dwordx4 v[96:99], %[voff], s[42:43] offset:0 sc1\n\tglobal_load_dwordx4 v[112:115], %[voff], s[42:43] offset:512 sc1\n\ts_mov_b64 exec, -1\n\t"
        "v_exp_f32 v53, v53\n\tv_exp_f32 v54, v54\n\tv_exp_f32 v55, v55\n\tv_exp_f32 v56, v56\n\tv_exp_f32 v57, v57\n\ts_waitcnt vmcnt(15)\n\t"
        "v_smfmac_f32_32x32x32_bf16 %[o0], v[100:103], v[48:55], v128\n\tv_exp_f32 v58, v58\n\tv_exp_f32 v59, v59\n\tv_exp_f32 v60, v60\n\tv_exp_f32 v61, v61\n\tv_exp_f32 v62, v62\n\t"
        "v_exp_f32 v63, v63\n\tv_exp_f32 v64, v64\n\tv_exp_f32 v65, v65\n\tv_smfmac_f32_32x32x32_bf16 %[o0], v[116:119], v[56:63], v128\n\tv_exp_f32 v66, v66\n\tv_exp_f32 v67, v67\n\t"
        "v_exp_f32 v68, v68\n\tv_mfma_f32_32x32x16_bf16 v[48:63], v[88:91], %[q0], 0\n\tglobal_load_dwordx4 v[228:231], %[vw], s[48:49]\n\ts_add_u32 s48, s48, 8192\n\t"
        "s_addc_u32 s49, s49, 0\n\tglobal_load_dwordx4 v[232:235], %[vw], s[48:49]\n\ts_add_u32 s48, s48, 8192\n\ts_addc_u32 s49, s49, 0\n\tv_exp_f32 v69, v69\n\tv_exp_f32 v70, v70\n\t"
        "v_exp_f32 v71, v71\n\tv_exp_f32 v72, v72\n\tv_exp_f32 v73, v73\n\tv_smfmac_f32_32x32x32_bf16 %[o1], v[100:103], v[64:71], v128\n\tv_exp_f32 v74, v74\n\tv_exp_f32 v75, v75\n\t"
        "v_exp_f32 v76, v76\n\tv_exp_f32 v77, v77\n\tv_exp_f32 v78, v78\n\tv_exp_f32 v79, v79\n\tv_exp_f32 v48, v48\n\tv_exp_f32 v49, v49\n\t"
        "v_smfmac_f32_32x32x32_bf16 %[o1], v[116:119], v[72:79], v128\n\tv_exp_f32 v50, v50\n\tv_exp_f32 v51, v51\n\tv_exp_f32 v52, v52\n\t"
        "v_mfma_f32_32x32x16_bf16 v[64:79], v[88:91], %[q1], 0\n\tglobal_load_dwordx4 v[88:91], %[koff], s[40:41] offset:2048 sc1\n\ts_mov_b64 exec, %[vm]\n\t"
        "global_load_dwordx4 v[100:103], %[voff], s[42:43] offset:1024 sc1\n\tglobal_load_dwordx4 v[116:119], %[voff], s[42:43] offset:1536 sc1\n\ts_mov_b64 exec, -1\n\t"
        "v_exp_f32 v53, v53\n\tv_exp_f32 v54, v54\n\tv_exp_f32 v55, v55\n\tv_exp_f32 v56, v56\n\tv_exp_f32 v57, v57\n\ts_waitcnt vmcnt(15)\n\t"
        "v_smfmac_f32_32x32x32_bf16 %[o0], v[104:107], v[48:55], v128\n\tv_exp_f32 v58, v58\n\tv_exp_f32 v59, v59\n\tv_exp_f32 v60, v60\n\tv_exp_f32 v61, v61\n\tv_exp_f32 v62, v62\n\t"
        "v_exp_f32 v63, v63\n\tv_exp_f32 v64, v64\n\tv_exp_f32 v65, v65\n\tv_smfmac_f32_32x32x32_bf16 %[o0], v[120:123], v[56:63], v128\n\tv_exp_f32 v66, v66\n\tv_exp_f32 v67, v67\n\t"
        "v_exp_f32 v68, v68\n\tv_mfma_f32_32x32x16_bf16 v[48:63], v[92:95], %[q0], 0\n\ts_add_u32 m0, %[hl], 3072\n\ts_add_u32 s52, s52, 44032\n\ts_addc_u32 s53, s53, 0\n\t"
        "global_load_lds_dwordx4 %[b0], s[52:53]\n\tglobal_load_dwordx4 v[236:239], %[vw], s[48:49]\n\ts_add_u32 s48, s48, 8192\n\ts_addc_u32 s49, s49, 0\n\ts_cmp_eq_u32 %[drop], 0\n\t"
        "s_cbranch_scc1 2f\n\ts_setprio 0\n\t2:\n\tv_exp_f32 v69, v69\n\tv_exp_f32 v70, v70\n\tv_exp_f32 v71, v71\n\tv_exp_f32 v72, v72\n\tv_exp_f32 v73, v73\n\t"
        "v_smfmac_f32_32x32x32_bf16 %[o1], v[104:107], v[64:71], v128\n\tv_exp_f32 v74, v74\n\tv_exp_f32 v75, v75\n\tv_exp_f32 v76, v76\n\tv_exp_f32 v77, v77\n\tv_exp_f32 v78, v78\n\t"
        "v_exp_f32 v79, v79\n\tv_exp_f32 v48, v48\n\tv_exp_f32 v49, v49\n\tv_smfmac_f32_32x32x32_bf16 %[o1], v[120:123], v[72:79], v128\n\tv_exp_f32 v50, v50\n\tv_exp_f32 v51, v51\n\t"
        "v_exp_f32 v52, v52\n\tv_mfma_f32_32x32x16_bf16 v[64:79], v[92:95], %[q1], 0\n\tglobal_load_dwordx4 v[92:95], %[koff], s[40:41] offset:3072 sc1\n\ts_mov_b64 exec, %[vm]\n\t"
        "global_load_dwordx4 v[104:107], %[voff], s[42:43] offset:2048 sc1\n\tglobal_load_dwordx4 v[120:123], %[voff], s[42:43] offset:2560 sc1\n\ts_mov_b64 exec, -1\n\t"
        "v_exp_f32 v53, v53\n\tv_exp_f32 v54, v54\n\tv_exp_f32 v55, v55\n\tv_exp_f32 v56, v56\n\tv_exp_f32 v57, v57\n\ts_waitcnt vmcnt(15)\n\t"
        "v_smfmac_f32_32x32x32_bf16 %[o0], v[108:111], v[48:55], v128\n\tv_exp_f32 v58, v58\n\tv_exp_f32 v59, v59\n\tv_exp_f32 v60, v60\n\tv_exp_f32 v61, v61\n\tv_exp_f32 v62, v62\n\t"
        "v_exp_f32 v63, v63\n\tv_exp_f32 v64, v64\n\tv_exp_f32 v65, v65\n\tv_smfmac_f32_32x32x32_bf16 %[o0], v[124:127], v[56:63], v128\n\tv_exp_f32 v66, v66\n\tv_exp_f32 v67, v67\n\t"
        "v_exp_f32 v68, v68\n\tv_mfma_f32_32x32x16_bf16 v[48:63], v[80:83], %[q0], 0\n\tglobal_load_dwordx4 v[240:243], %[vw], s[48:49]\n\ts_add_u32 s48, s48, 8192\n\t"
        "s_addc_u32 s49, s49, 0\n\ts_add_u32 m0, %[hl], 4096\n\ts_nop 0\n\tglobal_load_lds_dwordx4 %[b1], s[52:53]\n\tv_exp_f32 v69, v69\n\tv_exp_f32 v70, v70\n\tv_exp_f32 v71, v71\n\t"
        "v_exp_f32 v72, v72\n\tv_exp_f32 v73, v73\n\tv_smfmac_f32_32x32x32_bf16 %[o1], v[108:111], v[64:71], v128\n\tv_exp_f32 v74, v74\n\tv_exp_f32 v75, v75\n\tv_exp_f32 v76, v76\n\t"
        "v_exp_f32 v77, v77\n\tv_exp_f32 v78, v78\n\tv_exp_f32 v79, v79\n\tv_exp_f32 v48, v48\n\tv_exp_f32 v49, v49\n\tv_smfmac_f32_32x32x32_bf16 %[o1], v[124:127], v[72:79], v128\n\t"
        "v_exp_f32 v50, v50\n\tv_exp_f32 v51, v51\n\tv_exp_f32 v52, v52\n\tv_mfma_f32_32x32x16_bf16 v[64:79], v[80:83], %[q1], 0\n\ts_mov_b64 exec, %[vm]\n\t"
        "global_load_dwordx4 v[108:111], %[voff], s[42:43] offset:3072 sc1\n\tglobal_load_dwordx4 v[124:127], %[voff], s[42:43] offset:3584 sc1\n\ts_mov_b64 exec, -1\n\t"
        "v_exp_f32 v53, v53\n\tv_exp_f32 v54, v54\n\tv_exp_f32 v55, v55\n\tv_exp_f32 v56, v56\n\tv_exp_f32 v57, v57\n\ts_waitcnt vmcnt(14)\n\t"
        "v_smfmac_f32_32x32x32_bf16 %[o0], v[96:99], v[48:55], v128\n\tv_exp_f32 v58, v58\n\tv_exp_f32 v59, v59\n\tv_exp_f32 v60, v60\n\tv_exp_f32 v61, v61\n\tv_exp_f32 v62, v62\n\t"
        "v_exp_f32 v63, v63\n\tv_exp_f32 v64, v64\n\tv_exp_f32 v65, v65\n\tv_smfmac_f32_32x32x32_bf16 %[o0], v[112:115], v[56:63], v128\n\tv_exp_f32 v66, v66\n\tv_exp_f32 v67, v67\n\t"
        "v_exp_f32 v68, v68\n\tv_mfma_f32_32x32x16_bf16 v[48:63], v[84:87], %[q0], 0\n\tglobal_load_dwordx4 v[244:247], %[vw], s[48:49]\n\ts_add_u32 s48, s48, 8192\n\t"
        "s_addc_u32 s49, s49, 0\n\tglobal_load_dwordx4 v[248:251], %[vw], s[48:49]\n\ts_add_u32 s48, s48, 8192\n\ts_addc_u32 s49, s49, 0\n\tv_exp_f32 v69, v69\n\tv_exp_f32 v70, v70\n\t"
        "v_exp_f32 v71, v71\n\tv_exp_f32 v72, v72\n\tv_exp_f32 v73, v73\n\tv_smfmac_f32_32x32x32_bf16 %[o1], v[96:99], v[64:71], v128\n\tv_exp_f32 v74, v74\n\tv_exp_f32 v75, v75\n\t"
        "v_exp_f32 v76, v76\n\tv_exp_f32 v77, v77\n\tv_exp_f32 v78, v78\n\tv_exp_f32 v79, v79\n\tv_exp_f32 v48, v48\n\tv_exp_f32 v49, v49\n\t"
        "v_smfmac_f32_32x32x32_bf16 %[o1], v[112:115], v[72:79], v128\n\tv_exp_f32 v50, v50\n\tv_exp_f32 v51, v51\n\tv_exp_f32 v52, v52\n\t"
        "v_mfma_f32_32x32x16_bf16 v[64:79], v[84:87], %[q1], 0\n\tv_exp_f32 v53, v53\n\tv_exp_f32 v54, v54\n\tv_exp_f32 v55, v55\n\tv_exp_f32 v56, v56\n\tv_exp_f32 v57, v57\n\t"
        "s_waitcnt vmcnt(11)\n\tv_smfmac_f32_32x32x32_bf16 %[o0], v[100:103], v[48:55], v128\n\tv_exp_f32 v58, v58\n\tv_exp_f32 v59, v59\n\tv_exp_f32 v60, v60\n\tv_exp_f32 v61, v61\n\t"
        "v_exp_f32 v62, v62\n\tv_exp_f32 v63, v63\n\tv_exp_f32 v64, v64\n\tv_exp_f32 v65, v65\n\tv_smfmac_f32_32x32x32_bf16 %[o0], v[116:119], v[56:63], v128\n\tv_exp_f32 v66, v66\n\t"
        "v_exp_f32 v67, v67\n\tv_exp_f32 v68, v68\n\tv_mfma_f32_32x32x16_bf16 v[48:63], v[88:91], %[q0], 0\n\ts_add_u32 m0, %[hl], 5120\n\ts_nop 0\n\t"
        "global_load_lds_dwordx4 %[b2], s[52:53]\n\tglobal_load_dwordx4 v[252:255], %[vw], s[48:49]\n\ts_add_u32 s48, s48, 8192\n\ts_addc_u32 s49, s49, 0\n\tv_exp_f32 v69, v69\n\t"
        "v_exp_f32 v70, v70\n\tv_exp_f32 v71, v71\n\tv_exp_f32 v72, v72\n\tv_exp_f32 v73, v73\n\tv_smfmac_f32_32x32x32_bf16 %[o1], v[100:103], v[64:71], v128\n\tv_exp_f32 v74, v74\n\t"
        "v_exp_f32 v75, v75\n\tv_exp_f32 v76, v76\n\tv_exp_f32 v77, v77\n\tv_exp_f32 v78, v78\n\tv_exp_f32 v79, v79\n\tv_exp_f32 v48, v48\n\tv_exp_f32 v49, v49\n\t"
        "v_smfmac_f32_32x32x32_bf16 %[o1], v[116:119], v[72:79], v128\n\tv_exp_f32 v50, v50\n\tv_exp_f32 v51, v51\n\tv_exp_f32 v52, v52\n\t"
        "v_mfma_f32_32x32x16_bf16 v[64:79], v[88:91], %[q1], 0\n\tv_exp_f32 v53, v53\n\tv_exp_f32 v54, v54\n\tv_exp_f32 v55, v55\n\tv_exp_f32 v56, v56\n\tv_exp_f32 v57, v57\n\t"
        "s_waitcnt vmcnt(8)\n\tv_smfmac_f32_32x32x32_bf16 %[o0], v[104:107], v[48:55], v128\n\tv_exp_f32 v58, v58\n\tv_exp_f32 v59, v59\n\tv_exp_f32 v60, v60\n\tv_exp_f32 v61, v61\n\t"
        "v_exp_f32 v62, v62\n\tv_exp_f32 v63, v63\n\tv_exp_f32 v64, v64\n\tv_exp_f32 v65, v65\n\tv_smfmac_f32_32x32x32_bf16 %[o0], v[120:123], v[56:63], v128\n\tv_exp_f32 v66, v66\n\t"
        "v_exp_f32 v67, v67\n\tv_exp_f32 v68, v68\n\tv_mfma_f32_32x32x16_bf16 v[48:63], v[92:95], %[q0], 0\n\tv_exp_f32 v69, v69\n\tv_exp_f32 v70, v70\n\tv_exp_f32 v71, v71\n\t"
        "v_exp_f32 v72, v72\n\tv_exp_f32 v73, v73\n\tv_smfmac_f32_32x32x32_bf16 %[o1], v[104:107], v[64:71], v128\n\tv_exp_f32 v74, v74\n\tv_exp_f32 v75, v75\n\tv_exp_f32 v76, v76\n\t"
        "v_exp_f32 v77, v77\n\tv_exp_f32 v78, v78\n\tv_exp_f32 v79, v79\n\tv_exp_f32 v48, v48\n\tv_exp_f32 v49, v49\n\tv_smfmac_f32_32x32x32_bf16 %[o1], v[120:123], v[72:79], v128\n\t"
        "v_exp_f32 v50, v50\n\tv_exp_f32 v51, v51\n\tv_exp_f32 v52, v52\n\tv_mfma_f32_32x32x16_bf16 v[64:79], v[92:95], %[q1], 0\n\tv_exp_f32 v53, v53\n\tv_exp_f32 v54, v54\n\t"
        "v_exp_f32 v55, v55\n\tv_exp_f32 v56, v56\n\tv_exp_f32 v57, v57\n\ts_waitcnt vmcnt(4)\n\tv_smfmac_f32_32x32x32_bf16 %[o0], v[108:111], v[48:55], v128\n\tv_exp_f32 v58, v58\n\t"
        "v_exp_f32 v59, v59\n\tv_exp_f32 v60, v60\n\tv_exp_f32 v61, v61\n\tv_exp_f32 v62, v62\n\tv_exp_f32 v63, v63\n\tv_exp_f32 v64, v64\n\tv_exp_f32 v65, v65\n\t"
        "v_smfmac_f32_32x32x32_bf16 %[o0], v[124:127], v[56:63], v128\n\tv_exp_f32 v66, v66\n\tv_exp_f32 v67, v67\n\tv_exp_f32 v68, v68\n\tv_exp_f32 v69, v69\n\tv_exp_f32 v70, v70\n\t"
        "v_exp_f32 v71, v71\n\tv_exp_f32 v72, v72\n\tv_exp_f32 v73, v73\n\tv_smfmac_f32_32x32x32_bf16 %[o1], v[108:111], v[64:71], v128\n\tv_exp_f32 v74, v74\n\tv_exp_f32 v75, v75\n\t"
        "v_exp_f32 v76, v76\n\tv_exp_f32 v77, v77\n\tv_exp_f32 v78, v78\n\tv_exp_f32 v79, v79\n\ts_setprio 0\n\ts_nop 0\n\tv_smfmac_f32_32x32x32_bf16 %[o1], v[124:127], v[72:79], v128\n\t"
        "s_waitcnt vmcnt(0)\n\ts_nop 15\n\t"
        : [o0] "+v"(o0), [o1] "+v"(o1), [q0] "=&v"(q0), [q1] "=&v"(q1), "=&{v[144:159]}"(sg[0]), "=&{v[160:175]}"(sg[1]), "=&{v[176:191]}"(sg[2]), "=&{v[192:207]}"(sg[3]), "=&{v[208:223]}"(sg[4]), "=&{v[224:239]}"(sg[5]), "+{v[240:255]}"(sg[6])
        : [qa] "v"(qa), [qb] "v"(qb), [koff] "v"(koff), [voff] "v"(voff), [vinit] "v"(vinit), [vm] "s"(0x0000ffff0000ffffull), [kin] "s"(kb), [vin] "s"(vb), [drop] "s"(drop),
          [a0] "v"(a0), [a1] "v"(a1), [a2] "v"(a2), [b0] "v"(v30), [b1] "v"(v31), [b2] "v"(v32), [vw] "v"(vw), [sa] "s"(sa), [sb] "s"(sb), [so] "s"(so), [sh] "s"(sh), [ga] "s"(ga), [ol] "s"(ol), [hl] "s"(hl)
        : "memory", "scc", "s40", "s41", "s42", "s43", "s46", "s47", "s48", "s49", "s50", "s51", "s52", "s53",
          "v48","v49","v50","v51","v52","v53","v54","v55","v56","v57","v58","v59","v60","v61","v62","v63","v64","v65","v66","v67","v68","v69","v70","v71","v72","v73","v74","v75","v76","v77","v78","v79",
          "v80","v81","v82","v83","v84","v85","v86","v87","v88","v89","v90","v91","v92","v93","v94","v95","v96","v97","v98","v99","v100","v101","v102","v103","v104","v105","v106","v107","v108","v109","v110","v111",
          "v112","v113","v114","v115","v116","v117","v118","v119","v120","v121","v122","v123","v124","v125","v126","v127","v128");
    }
    STAMP(7); STAMPC(27);
    lane = __builtin_amdgcn_mbcnt_hi(~0u, __builtin_amdgcn_mbcnt_lo(~0u, 0u)); asm volatile("" : "+v"(lane));
    r32 = lane & 31; h = lane >> 5; q = (lane & 15) >> 2; p = lane & 3; blk = (lane >> 4) & 1;
    rowoff = ((rg * 2 + (lw >> 1)) * 32 + r32) * D_ + cs * 64 + (lw & 1) * 32 + 8 * h;
    if (wave < 4) { res[0] = *(const u32x4*)(x + rowoff); res[1] = *(const u32x4*)(x + rowoff + 16); }
    {
      LAS char* d = smem + L_RED + wave * 2304 + lane * 16;
      *(LAS f32x4*)d = f32x4{o1[0], o1[1], o1[2], o1[3]}; *(LAS f32x4*)(d + 1024) = f32x4{o1[4], o1[5], o1[6], o1[7]};
      *(LAS float*)(smem + L_RED + wave * 2304 + 2048 + lane * 4) = o1[8];
    }
    BARRIER();
    float o[9];
    {
      LAS char* d = smem + L_RED + (wave ^ 1) * 2304 + lane * 16;
      const f32x4 e0 = *(LAS f32x4*)d, e1 = *(LAS f32x4*)(d + 1024);
      o[8] = o0[8] + *(LAS float*)(smem + L_RED + (wave ^ 1) * 2304 + 2048 + lane * 4);
#pragma unroll
      for (int i = 0; i < 4; i++) { o[i] = o0[i] + e0[i]; o[4 + i] = o0[4 + i] + e1[i]; }
    }
    const bool ok = o[8] >= 0x1p-40f && o[8] <= 0x1p40f;
    if (__builtin_expect(!__all(ok), 0)) {
      const u32x4 qq = q0;
      f32x16 z, oc;
#pragma unroll
      for (int i = 0; i < 16; i++) { z[i] = 0.f; oc[i] = 0.f; }
      float m = -__builtin_inff();
      const bool vl = (lane & 16) == 0;
      const u32x4 ones = {0x3f803f80u, 0x3f803f80u, 0x3f803f80u, 0x3f803f80u};
#pragma unroll 1
      for (int t = 0; t < 64; t++) {
        const u32x4 kk = ld16(rs, lane * 16, WS_K + head * 65536 + t * 1024);
        u32x4 wa = ld16(rs, h * 256 + (lane & 15) * 16, WS_V + head * 65536 + t * 1024), wb = ld16(rs, h * 256 + (lane & 15) * 16, WS_V + head * 65536 + t * 1024 + 512);
        wa = vl ? wa : ones; wb = vl ? wb : ones;
        f32x16 s = mfma(kk, qq, z);
        float tm = s[0];
#pragma unroll
        for (int i = 1; i < 16; i++) tm = fmaxf(tm, s[i]);
        tm = fmaxf(tm, __shfl_xor(tm, 32));
        const float mn = fmaxf(m, tm), al = __builtin_amdgcn_exp2f(m - mn);
        m = mn;
#pragma unroll
        for (int i = 0; i < 16; i++) { oc[i] *= al; s[i] -= mn; }
        oc = pv(wa, exp8(s, 0), oc); oc = pv(wb, exp8(s, 8), oc);
      }
#pragma unroll
      for (int i = 0; i < 9; i++) o[i] = oc[i];
    }
    const float rl = __builtin_amdgcn_rcpf(o[8]);
    u32x4 r;
#pragma unroll
    for (int j = 0; j < 4; j++) r[j] = pack2(o[2 * j] * rl, o[2 * j + 1] * rl);
    st16(rs, lane * 16, WS_O + ((rg1 * 2 + kh) * 32 + head) * 1024, r);
    STAMP(8);
    DRAIN(2);
    BARRIER();
    if (wave == 4) publish(ws, WS_F2 + (rg1 * 8 + hg) * 128, tag, lane);
    STAMP(9);
  }

  auto opq = [](int v) { asm volatile("" : "+v"(v)); return v; };
  const int bb1 = L_B1 + (8 * h + q) * 192 + blk * 32 + p * 8;
  if (wave < 4) {
  const int rb = wave >> 1, kh = wave & 1;
  const int tile = rg * 2 + rb;
  LAS char* bsq[2];
#pragma unroll
  for (int u = 0; u < 2; u++) bsq[u] = smem + (4 * h + q) * 128 + (((4 * (u ^ kh) + 2 * blk + (p & 1)) ^ ((q >> 1) << 2)) << 4) + 8 * (p >> 1);
  auto nohook = [](auto) {};
  auto exchange = [&](f32x16 (&acc)[2], int lds) {
#pragma unroll
    for (int i = 0; i < 4; i++) *(LAS f32x4*)(smem + lds + (wave * 4 + i) * 1024 + lane * 16) = f32x4{acc[1][4 * i], acc[1][4 * i + 1], acc[1][4 * i + 2], acc[1][4 * i + 3]};
    BARRIER();
#pragma unroll
    for (int i = 0; i < 4; i++) {
      const f32x4 v = *(LAS f32x4*)(smem + lds + ((wave ^ 1) * 4 + i) * 1024 + lane * 16);
      for (int j = 0; j < 4; j++) acc[0][4 * i + j] += v[j];
    }
  };
  {
    f32x16 acc[2];
#pragma unroll
    for (int i = 0; i < 16; i++) { acc[0][i] = (i & 1) ? bfhi(res[i >> 3][(i >> 1) & 3]) : bflo(res[i >> 3][(i >> 1) & 3]); acc[1][i] = 0.f; }
    asm volatile("" : "+v"(acc[0]));
    gemm_direct<16, 2, 0, Run4>(rs, lane * 16, [&](int pos) { return WS_O + (tile * 32 + kh * 16 + ((pos + off3) & 15)) * 1024; },
                          [&](int pos, int u) { return tr_frag(bsq[u] + kh * 32768 + pos * 2048, 1024); }, (const u32x4(*)[2]) nullptr, acc,
                                [&]() { BARRIER(); STAMP(10); },
                                nohook);
    STAMP(11);
    exchange(acc, L_Q);
    f32x2 t[8];
#pragma unroll
    for (int i = 0; i < 8; i++) t[i] = f32x2{acc[0][2 * i], acc[0][2 * i + 1]};
    FENCE();
#pragma unroll
    for (int i = 0; i < 8; i++) res[i >> 2][i & 3] = pack2v(t[i]);
    FENCE();
    st16(rs, lane * 16, WS_H + (tile * 32 + cs * 4 + kh * 2) * 1024, res[0]);
    st16(rs, lane * 16, WS_H + (tile * 32 + cs * 4 + kh * 2 + 1) * 1024, res[1]);
    f32x2 s2[4], q2[4];
#pragma unroll
    for (int i = 0; i < 4; i++) { s2[i] = t[i] + t[i + 4]; q2[i] = pfma(t[i], t[i], t[i + 4] * t[i + 4]); }
    const f32x2 sa = (s2[0] + s2[1]) + (s2[2] + s2[3]), qa = (q2[0] + q2[1]) + (q2[2] + q2[3]);
    const float sx = xhalf_sum(sa[0] + sa[1]), sxx = xhalf_sum(qa[0] + qa[1]);
    if (h == 0) __builtin_amdgcn_raw_buffer_store_b64(u32x2{asu(sx), asu(sxx)}, rs, r32 * 8, WS_ST + (tile * 16 + cs * 2 + kh) * 256, 16);
    STAMP(12);
    BARRIER();
#pragma unroll
    for (int i = 0; i < 12; i++) *(LAS u32x4*)(smem + L_B1 + lw * 12288 + i * 1024 + lane * 16) = stg(i);
    DRAIN(4);
    BARRIER();
#pragma unroll
    for (int i = 0; i < 8; i++) {
      LAS char* dst = smem + (2 * ((lw >> 1) * 7 + i) + (lw & 1)) * 2048 + lane * 16;
      *(LAS u32x4*)dst = stg(12 + 2 * i); *(LAS u32x4*)(dst + 1024) = stg(13 + 2 * i);
    }
    STAMP(13);
  }
  u32x4 pre[4][2];
  {
    const int t4 = rp * 4 + wave;
    f32x16 acc[3];
#pragma unroll
    for (int u = 0; u < 3; u++) for (int i = 0; i < 16; i++) acc[u][i] = 0.f;
    u32x4 wx[3];
    const int b4[2] = {opq(bb1), opq(bb1 + 49152)};
    unsigned sto = WS_ST + t4 * 4096; asm volatile("" : "+s"(sto));
    u32x2 st[8];
    float rs2 = 0.f, sx = 0.f, sxx = 0.f, nm = 0.f, sd = 0.f;
    u32x4 ax = {0u, 0u, 0u, 0u};
    gemm_direct<32, 3, 0, Run4>(rs, lane * 16, [&](int pos) { return WS_H + (t4 * 32 + ((pos + off4) & 31)) * 1024; },
        [&](int pos, int u) { return tr_frag(smem + b4[pos >> 4] + (pos & 15) * 3072 + u * 64, 768); }, (const u32x4(*)[3]) nullptr, acc,
        [&]() { BARRIER(); STAMP(16); },
        [&](auto pp) {
          constexpr int pos = pp.value;
          if constexpr (pos < 8) st[pos] = __builtin_amdgcn_raw_buffer_load_b64(rs, h * 2048 + r32 * 8 + pos * 256, sto, 16);
          if constexpr (pos >= 12 && pos < 20) { sx = add1(asf(st[pos - 12][0]), sx); sxx = add1(asf(st[pos - 12][1]), sxx); }
          if constexpr (pos == 20) sx = xhalf_sum(sx);
          if constexpr (pos == 21) sxx = xhalf_sum(sxx);
          if constexpr (pos == 22) { nm = sx * (-1.f / D_); sd = fmaxf(fma1(sxx, 1.f / D_, -nm * nm), 0.f) + 1e-5f; }
          if constexpr (pos == 23) { rs2 = __builtin_amdgcn_rsqf(sd); asm("" : "+v"(rs2)); }
          if constexpr (pos == 24) { sd *= rs2; rs2 *= LOG2E; nm = h ? 0.f : nm; sd = h ? 0.f : sd; asm("" : "+v"(nm), "+v"(sd), "+v"(rs2)); }
          if constexpr (pos == 25) { ax[0] = pack2(nm, nm); ax[3] = pack2(sd, sd); }
          if constexpr (pos == 26) { nm = add1(nm, -bflo(ax[0])); sd = add1(sd, -bflo(ax[3])); }
          if constexpr (pos >= 28 && pos < 31) wx[pos - 28] = *(LAS u32x4*)(smem + L_RES + 256 + (pos - 28) * 512 + r32 * 16);
          if constexpr (pos == 27) { ax[1] = (pack2(nm, 0.f) & 0xffffu) | (ax[3] & 0xffff0000u); ax[2] = pack2(sd, 0.f); ax[3] = 0u; }
        });
#pragma unroll
    for (int u = 0; u < 3; u++) acc[u] = mfma(wx[u], ax, acc[u]);
    STAMP(17);
#pragma unroll
    for (int i = 0; i < 4; i++) for (int u = 0; u < 2; u++) pre[i][u] = tr_frag(bsq[u] + (2 * i + kh) * 2048, 1024);
    FENCE();
    prog[wave] = 34;
    FENCE();
#pragma unroll
    for (int u = 0; u < 3; u++) {
      if (u < nunit) {
        f32x2 w[8], t[8];
#pragma unroll
        for (int i = 0; i < 8; i++) w[i] = f32x2{acc[u][2 * i], acc[u][2 * i + 1]} * pk(rs2);
        FENCE();
#pragma unroll
        for (int i = 0; i < 8; i++) t[i] = f32x2{__builtin_amdgcn_exp2f(w[i][0]), __builtin_amdgcn_exp2f(w[i][1])};
        FENCE();
        f32x2 a2[8];
#pragma unroll
        for (int i = 0; i < 8; i++) a2[i] = t[i] + pk(2.f);
        FENCE();
#pragma unroll
        for (int i = 0; i < 8; i++) t[i] = pfma(t[i], a2[i], pk(2.f));
        FENCE();
#pragma unroll
        for (int i = 0; i < 8; i++) t[i] = f32x2{__builtin_amdgcn_rcpf(t[i][0]), __builtin_amdgcn_rcpf(t[i][1])};
        FENCE();
#pragma unroll
        for (int i = 0; i < 8; i++) t[i] = pfma(t[i], pk(-2.f / LOG2E), pk(1.f / LOG2E));
        FENCE();
#pragma unroll
        for (int i = 0; i < 8; i++) t[i] = t[i] * w[i];
        FENCE();
        unsigned o[8];
#pragma unroll
        for (int i = 0; i < 8; i++) o[i] = pack2v(t[i]);
        FENCE();
        const unsigned so = WS_ACT + (t4 * 86 + (unit0 + u) * 2) * 1024;
        st16(rs, lane * 16, so, u32x4{o[0], o[1], o[2], o[3]});
        st16(rs, lane * 16, so + 1024, u32x4{o[4], o[5], o[6], o[7]});
      }
    }
    STAMP(18);
    DRAIN(8);
    STAMP(19);
  }
  {
    f32x16 acc[2];
#pragma unroll
    for (int i = 0; i < 16; i++) { acc[0][i] = (i & 1) ? bfhi(res[i >> 3][(i >> 1) & 3]) : bflo(res[i >> 3][(i >> 1) & 3]); acc[1][i] = 0.f; }
    asm volatile("" : "+v"(acc[0]));
    int b5[2][3];
    unsigned fl;
#pragma unroll
    for (int i = 0; i < 6; i++) b5[i / 3][i % 3] = opq((int)(bsq[i / 3] - smem) + kh * 2048 + (i % 3) * 13 * 4096);
    BARRIER();
    gemm_direct<43, 2, 4, Run5>(rs, lane * 16, [&](int pos) { int ks = pos + off5; ks = ks >= 43 ? ks - 43 : ks; return WS_ACT + (tile * 86 + kh * 43 + ks) * 1024; },
                          [&](int pos, int u) { const int sp = pos >= 39 ? pos - 39 : pos; return tr_frag(smem + b5[u][sp / 13] + (sp % 13) * 4096, 1024); }, pre, acc,
                                [&]() { BARRIER(); STAMP(20); },
                                [&](auto pp) {
                                  if constexpr (pp.value == 8) prog[wave] = 35;
                                  if constexpr (pp.value == 40) fl = *(volatile LAS unsigned*)(smem + L_RES + p * 4);
                                });
    STAMP(21);
    if (__builtin_expect(__any(fl < 35), 0)) lds_spin<0>(L_RES + p * 4, 35);
    exchange(acc, 16384);
#pragma unroll
    for (int s = 0; s < 2; s++) {
      u32x4 o;
#pragma unroll
      for (int j = 0; j < 4; j++) o[j] = pack2(acc[0][8 * s + 2 * j], acc[0][8 * s + 2 * j + 1]);
      __builtin_amdgcn_raw_buffer_store_b128(o, __builtin_amdgcn_make_buffer_rsrc(out, 0, 2048 * D_ * 2, 0x00020000), (unsigned)rowoff * 2 + 32 * s, 0, OUTAUX);
    }
    STAMP(22);
    STAMP_FLUSH();
  }
  } else {
    const unsigned bv = b1[(unit0 + max(lw - 1, 0)) * 32 + r32];
    const unsigned selv = ((lane & 15) == 4 * blk) ? 0x3f803f80u : 0u;
    const u32x4 sel = {selv, selv, selv, selv};
    f32x4 c4 = {0.f, 0.f, 0.f, 0.f};
    auto colsum = [&](int k0) {
      u32x4 f[16];
      const int bc = opq(bb1 + k0 * 3072 + max(lw - 1, 0) * 64);
#pragma unroll
      for (int i = 0; i < 16; i++) f[i] = tr_frag(smem + bc + i * 3072, 768);
#pragma unroll
      for (int i = 0; i < 16; i++) c4 = mfma16(sel, f[i], c4);
    };
    if (wave == 4) wait_flags<PD2>(ws, WS_F2 + (rg * 8 + (lane & 7)) * 128, tag); else colsum(16);
    BARRIER();
    BARRIER();
    BARRIER();
    BARRIER();
    STAMP(14);
    if (wave == 4) { publish(ws, WS_F3 + (rg * 8 + cs) * 128, tag, lane); wait_flags<PD3>(ws, WS_F3 + (rp * 16 + (lane & 15)) * 128, tag); }
    else colsum(0);
    if (lw > 0 && h == 0) {
      const unsigned ch = pack2(c4[0], 0.f), cl = pack2(c4[0] - bflo(ch), 0.f);
      *(LAS u32x4*)(smem + L_RES + 256 + (lw - 1) * 512 + r32 * 16) = u32x4{ch | (cl << 16), ch | (bv << 16), bv, 0u};
    }
    STAMP(15);
    BARRIER();
    lds_spin<SL4>(L_RES + p * 4, 34);
    LAS char* dst = smem + (2 * (15 + (lw >> 1) * 14) + (lw & 1)) * 2048 + lane * 16;
    LAS char* dst2 = (lw >> 1) ? dst - 78 * 2048 : dst;
#pragma unroll
    for (int i = 0; i < 14; i++) { LAS char* d = (i >= 10 ? dst2 : dst) + i * 4096; *(LAS u32x4*)d = stg(2 * i); *(LAS u32x4*)(d + 1024) = stg(2 * i + 1); }
    STAMP(19);
    BARRIER();
    if (wave == 4) {
      publish(ws, WS_F4 + (rp * 16 + sl) * 128, tag, lane); wait_flags<PD4>(ws, WS_F4 + ((rg >> 1) * 16 + (lane & 15)) * 128, tag);
      if (b == 0 && lane == 0) *(u64*)(ws + WS_EP) = tag;
    }
    BARRIER();
    STAMP_FLUSH();
  }
}

extern "C" void kernel_launch(void* const* d_in, const int* in_sizes, int n_in, void* d_out, int out_size, void* d_ws, size_t ws_size, hipStream_t stream) {
  fused<<<256, 512, 0, stream>>>((const u16*)d_in[0], (const u16*)d_in[1], (const u16*)d_in[2], (const u16*)d_in[4], (const u16*)d_in[5], (const u16*)d_in[6], (char*)d_ws, (u16*)d_out);
  hipStreamCaptureStatus cs; (void)hipStreamIsCapturing(stream, &cs);
  if (cs == hipStreamCaptureStatusNone) {
    (void)hipStreamSynchronize(stream);
    fprintf(stderr, "ptrs x %p wqkv %p wo %p w1 %p b1 %p w2 %p ws %p out %p\n", d_in[0], d_in[1], d_in[2], d_in[4], d_in[5], d_in[6], d_ws, d_out);
    hipPointerAttribute_t at; (void)hipPointerGetAttributes(&at, d_ws); fprintf(stderr, "ws type %d flags %u managed %d\n", (int)at.type, at.allocationFlags, at.isManaged);
    for (int i = 0; i < 200; i++) fused<<<256, 512, 0, stream>>>((const u16*)d_in[0], (const u16*)d_in[1], (const u16*)d_in[2], (const u16*)d_in[4], (const u16*)d_in[5], (const u16*)d_in[6], (char*)d_ws, (u16*)d_out);
    (void)hipStreamSynchronize(stream);
    dbg_dump(d_ws);
  }
}
```

```cpp
#include <hip/hip_runtime.h>
#include <hip/hip_bf16.h>
#include <stdint.h>

typedef __attribute__((ext_vector_type(16))) float f32x16;
typedef __attribute__((ext_vector_type(8))) float f32x8;
typedef __attribute__((ext_vector_type(4))) float f32x4;
typedef __attribute__((ext_vector_type(2))) float f32x2;
typedef __attribute__((ext_vector_type(8))) __bf16 bf16x8;
typedef __attribute__((ext_vector_type(16))) __bf16 bf16x16;
typedef __attribute__((ext_vector_type(2))) __bf16 bf16x2;
typedef __attribute__((ext_vector_type(4))) short s16x4;
typedef __attribute__((ext_vector_type(4))) unsigned u32x4;
typedef __attribute__((ext_vector_type(2))) unsigned u32x2;
typedef __attribute__((ext_vector_type(16))) unsigned u32x16;
typedef unsigned short u16;
typedef unsigned long long u64;
#define LAS __attribute__((address_space(3)))
#define GAS __attribute__((address_space(1)))
#define DEV __device__ __forceinline__

constexpr int D_ = 512, F_ = 1376;
constexpr float LOG2E = 1.4426950408889634f;

constexpr unsigned WS_K = 0, WS_V = 2u << 20, WS_O = 4u << 20, WS_H = 6u << 20, WS_ACT = 8u << 20, WS_ST = 14u << 20;
constexpr unsigned WS_EP = 15u << 20, WS_F1 = WS_EP + 4096, WS_F2 = WS_F1 + 32768, WS_F3 = WS_F2 + 32768, WS_F4 = WS_F3 + 32768, WS_F5 = WS_F4 + 32768, WS_END = 16u << 20;

DEV unsigned pack2(float a, float b) { f32x2 p = {a, b}; return __builtin_bit_cast(unsigned, __builtin_convertvector(p, bf16x2)); }
DEV float asf(unsigned u) { return __builtin_bit_cast(float, u); }
DEV unsigned asu(float f) { return __builtin_bit_cast(unsigned, f); }
DEV float bflo(unsigned u) { return __builtin_bit_cast(float, u << 16); }
DEV float bfhi(unsigned u) { return __builtin_bit_cast(float, u & 0xffff0000u); }
DEV f32x16 mfma(u32x4 a, u32x4 b, f32x16 c) { return __builtin_amdgcn_mfma_f32_32x32x16_bf16(__builtin_bit_cast(bf16x8, a), __builtin_bit_cast(bf16x8, b), c, 0, 0, 0); }
DEV f32x4 mfma16(u32x4 a, u32x4 b, f32x4 c) { return __builtin_amdgcn_mfma_f32_16x16x32_bf16(__builtin_bit_cast(bf16x8, a), __builtin_bit_cast(bf16x8, b), c, 0, 0, 0); }
DEV u32x2 tr_read(LAS char* p) { return __builtin_bit_cast(u32x2, __builtin_amdgcn_ds_read_tr16_b64_v4i16((LAS s16x4*)p)); }
DEV u32x4 tr_frag(LAS char* p, int step) { const u32x2 w0 = tr_read(p), w1 = tr_read(p + step); return u32x4{w0[0], w0[1], w1[0], w1[1]}; }
DEV float xhalf_sum(float v) {
  const unsigned u = __builtin_bit_cast(unsigned, v);
  const auto r = __builtin_amdgcn_permlane32_swap(u, u, false, false);
  return asf(r[0]) + asf(r[1]);
}
#define BARRIER() do { __builtin_amdgcn_sched_barrier(0); __builtin_amdgcn_s_waitcnt(0xC07F); asm volatile("s_barrier" ::: "memory"); __builtin_amdgcn_sched_barrier(0); } while (0)
template <int N> DEV void wait_vm() { asm volatile("s_waitcnt vmcnt(%0)" ::"n"(N) : "memory"); }
#define SGB(m, n) __builtin_amdgcn_sched_group_barrier(m, n, 0)
template <int I, int N, class Fn> DEV void static_for(Fn&& f) {
  if constexpr (I < N) { f(std::integral_constant<int, I>{}); static_for<I + 1, N>(f); }
}

#define STAMP(i)
#define STAMPC(i)
#define STAMP_DECL
#define STAMP_FLUSH()

DEV void dma_kv(u64 sw, unsigned v, int lds) {
  asm volatile("s_mov_b32 m0, %[m]\n\ts_nop 0\n\tglobal_load_lds_dwordx4 %[v], %[sw]\n\t"
               "s_add_u32 m0, %[m], 0x1c00\n\ts_nop 0\n\tglobal_load_lds_dwordx4 %[v], %[sw] offset:1024"
               :: [m] "s"(lds), [sw] "s"(sw), [v] "v"(v) : "memory", "scc");
}
DEV void dma_slot(u64 sw, unsigned v, int lds) {
#define P1(dm) "s_add_u32 m0, %[m], " #dm "\n\ts_add_u32 s44, s44, 0x6000\n\ts_addc_u32 s45, s45, 0\n\tglobal_load_lds_dwordx4 %[v], s[44:45]\n\t"
  asm volatile("s_mov_b64 s[44:45], %[sw]\n\t" P1(0) P1(0x400) P1(0x800) P1(0xc00) P1(0x1000) P1(0x1400) P1(0x1800) P1(0x1c00)
               :: [m] "s"(lds), [sw] "s"(sw - 0x6000), [v] "v"(v) : "memory", "scc", "s44", "s45");
}
#define RLX_AGENT __ATOMIC_RELAXED, __HIP_MEMORY_SCOPE_AGENT
DEV void publish(char* ws, unsigned off, u64 tag, int lane) {
  if (lane == 0) __hip_atomic_store((GAS u64*)(ws + off), tag, RLX_AGENT);
}
#define PD1 14
#define PD2 14
#define PD3 14
#define PD4 14
#define PSL 2
template <int PDLY> DEV void wait_flags(char* ws, unsigned off, u64 tag) {
  u64 v; int n = 1 << 18;
  asm volatile("s_sleep %[d]\n\t1:\n\tglobal_load_dwordx2 %[v], %[off], %[ws] sc1\n\ts_waitcnt vmcnt(0)\n\tv_cmp_ne_u64 vcc, %[tag], %[v]\n\ts_cbranch_vccz 2f\n\t"
               "s_mov_b64 exec, vcc\n\ts_sleep %[s]\n\ts_sub_u32 %[n], %[n], 1\n\ts_cbranch_scc0 1b\n\t2:\n\ts_mov_b64 exec, -1"
               : [v] "=&v"(v), [n] "+s"(n) : [off] "v"(off), [ws] "s"(ws), [tag] "s"(tag), [s] "n"(PSL), [d] "n"(PDLY) : "memory", "vcc", "scc");
}
typedef __amdgpu_buffer_rsrc_t rsrc_t;
DEV void st16(rsrc_t r, unsigned voff, unsigned soff, u32x4 v) { __builtin_amdgcn_raw_buffer_store_b128(v, r, voff, soff, 16); }
DEV u32x4 ld16(rsrc_t r, unsigned voff, unsigned soff) { return __builtin_amdgcn_raw_buffer_load_b128(r, voff, soff, 16); }
#define LSLEEP 1
#define THR 6
#define W2POS 12
#define OUTAUX 0
#define FIRST 4
#define FSLEEP 6
#define FLPOS 1
#define EARLY 0
#define DRAIN(m) do { if (!(EARLY & m)) wait_vm<0>(); } while (0)
#define SL1 1
#define SL4 1
#define QREL 0
#define XRX 0
#define QT 0
#define BX 0
#define MIX 1
#define NINIT 3
#define WDELAY 4
#define DROPI 4
template <int SL> DEV void lds_spin(unsigned addr, unsigned need) {
  unsigned a;
  asm volatile("1:\n\tds_read_b32 %[a], %[p]\n\ts_waitcnt lgkmcnt(0)\n\tv_cmp_gt_u32 vcc, %[n], %[a]\n\ts_cbranch_vccz 2f\n\ts_sleep %[s]\n\ts_branch 1b\n\t2:"
               : [a] "=&v"(a) : [p] "v"(addr), [n] "s"(need), [s] "n"(SL) : "memory", "vcc");
}
DEV f32x16 pv(u32x4 v, f32x8 pr, f32x16 o) {
  return __builtin_amdgcn_smfmac_f32_32x32x32_bf16(__builtin_bit_cast(bf16x8, v), __builtin_bit_cast(bf16x16, pr), o, 0xDDDD, 0, 0);
}
DEV f32x8 exp8(f32x16 s, int o) { f32x8 r; for (int i = 0; i < 8; i++) r[i] = __builtin_amdgcn_exp2f(s[o + i]); return r; }
DEV float fma1(float a, float b, float c) { float r = __builtin_fmaf(a, b, c); asm("" : "+v"(r)); return r; }
DEV float add1(float a, float b) { float r = a + b; asm("" : "+v"(r)); return r; }
DEV float dot2(unsigned a, unsigned b, float c) { return __builtin_amdgcn_fdot2_f32_bf16(__builtin_bit_cast(bf16x2, a), __builtin_bit_cast(bf16x2, b), c, false); }
DEV float rbf(float a) { return bflo(pack2(a, 0.f)); }
DEV f32x2 pk(float a) { return f32x2{a, a}; }
DEV f32x2 pfma(f32x2 a, f32x2 b, f32x2 c) { return __builtin_elementwise_fma(a, b, c); }
DEV unsigned pack2v(f32x2 p) { return __builtin_bit_cast(unsigned, __builtin_convertvector(p, bf16x2)); }
#define FENCE() __builtin_amdgcn_sched_barrier(0)

struct WFrag { u32x4 a[8]; u32x4 w[8]; };
constexpr int L_RES = 159744;
constexpr int L_Q = 67584, L_RED = 75776, L_DUM = 94208;
constexpr int L_W1 = 65536, L_Q1 = 98304;
constexpr int L_B1 = 61440;
constexpr int CODE_BYTES = 30720;
#define RD_ 10
constexpr int RD = RD_;

struct Run4 { static constexpr int start(int pos) { return pos & ~3; } };
struct Run5 { static constexpr int start(int pos) { const int s0 = pos >= 35 ? 35 : pos >= 29 ? 29 : pos >= 21 ? 21 : pos >= 15 ? 15 : 0; return s0 + ((pos - s0) & ~3); } };
template <int NK, int NU, int NPRE, class GS, class ASO, class BRD, class RDY, class H1>
DEV void gemm_direct(rsrc_t rs, unsigned voff, ASO&& aso, BRD&& brd, const u32x4 (*pre)[NU], f32x16 (&acc)[NU], RDY&& ready, H1&& hook) {
  u32x4 a[RD], w[3][NU];
  unsigned so[NK];
  static_for<0, NK>([&](auto i) { if constexpr (GS::start(i.value) == i.value) { so[i.value] = aso(i.value); asm volatile("" : "+s"(so[i.value])); } });
  auto lda = [&](auto i) { return ld16(rs, voff + (i.value - GS::start(i.value)) * 1024, so[GS::start(i.value)]); };
  static_for<0, 2>([&](auto i) {
    if constexpr (i.value >= NPRE) { for (int u = 0; u < NU; u++) w[i.value][u] = brd(i.value, u); }
  });
  ready();
  static_for<0, RD>([&](auto i) { a[i.value] = lda(i); FENCE(); });
  FENCE();
  static_for<0, NK>([&](auto pp) {
    constexpr int pos = pp.value;
    static_for<0, NU>([&](auto uu) {
      constexpr int u = uu.value;
      if constexpr (pos < NPRE) acc[u] = mfma(pre[pos][u], a[pos % RD], acc[u]); else acc[u] = mfma(w[pos % 3][u], a[pos % RD], acc[u]);
      FENCE();
      if constexpr (u == 0) hook(pp);
      if constexpr (pos + 2 < NK && pos + 2 >= NPRE) w[(pos + 2) % 3][u] = brd(pos + 2, u);
      if constexpr (u == NU - 1) {
        if constexpr (pos + RD < NK) a[pos % RD] = lda(std::integral_constant<int, pos + RD>{});
      }
      FENCE();
    });
  });
}

__global__ __launch_bounds__(512, 2) void fused(const u16* __restrict__ x, const u16* __restrict__ wqkv, const u16* __restrict__ wo, const u16* __restrict__ w1,
                                                const u16* __restrict__ b1, const u16* __restrict__ w2, char* __restrict__ ws, u16* __restrict__ out) {
  __shared__ __attribute__((aligned(16))) char smem_[163840];
  LAS char* smem = (LAS char*)smem_;
  const int wave = __builtin_amdgcn_readfirstlane(threadIdx.x >> 6);
  int lane = threadIdx.x & 63;
  const int b = blockIdx.x ^ BX, xcd = b & 7, bi = b >> 3;
  STAMP_DECL; STAMP(0);
  u64 pc0; asm volatile("s_getpc_b64 %0" : "=s"(pc0));
  asm volatile("" ::"s"(x), "s"(wqkv), "s"(wo), "s"(w1), "s"(b1), "s"(w2), "s"(ws), "s"(out));
  u64 tag = 0;
  auto load_tag = [&]() { asm volatile("s_load_dwordx2 %0, %1, 0x0\n\ts_waitcnt lgkmcnt(0)" : "=s"(tag) : "s"(ws + WS_EP) : "memory"); tag += 1; };
  const rsrc_t rs = __builtin_amdgcn_make_buffer_rsrc(ws, 0, WS_END, 0x00020000);
  int r32 = lane & 31, h = lane >> 5, q = (lane & 15) >> 2, p = lane & 3, blk = (lane >> 4) & 1;
  const int lw = wave & 3;
  const unsigned vrow128 = 16 * ((lane & 7) ^ (((lane >> 4) & 1) << 2));
  volatile LAS unsigned* prog = (volatile LAS unsigned*)(smem + L_RES);

#define MAPB 1
  constexpr int HS = 3 - MAPB;
  const int hg = ((xcd & ((1 << MAPB) - 1)) << HS) + (bi & ((1 << HS) - 1)), rg1 = (xcd >> MAPB) * (32 >> HS) + (bi >> HS);
  const int c0 = bi >> HS;
  const u64 sw0 = (u64)(wqkv + hg * 64);
  const unsigned vw0 = (lane >> 3) * (3 * D_ * 2) + vrow128;
  const int seg = lane & 7, xr = (8 >> HS) * (bi & ((1 << HS) - 1)) + XRX * (xcd & 1);
  u32x4 xv[8];
  auto kv8 = [&](int c) { dma_kv(sw0 + 1024 + (unsigned)((((c + c0) & 7) * 8 + wave) * (8 * 3 * D_ * 2)), vw0, L_W1 + c * 16384 + wave * 1024); };
  {
    const char* xb = (const char*)x + (size_t)(rg1 * 64 + wave * 8) * 1024;
    const unsigned xo = (lane >> 3) * 1024 + seg * 16;
#pragma unroll
    for (int i = 0; i < 8; i++) asm volatile("global_load_dwordx4 %0, %1, %2" : "=v"(xv[i]) : "v"(xo), "s"(xb + ((i + xr) & 7) * 128) : "memory");
    asm volatile("s_barrier" ::: "memory");
    kv8(0);
    const char* tp = lane == 2 ? ws + WS_F1 + lw * 32768 + b * 128 : (const char*)(pc0 & ~127ull) + min(((bi * 8 + wave) * 2 + (lane & 1)) * 128, CODE_BYTES - 128);
    asm volatile("s_mov_b32 m0, %1\n\ts_nop 0\n\tglobal_load_lds_dword %0, off" ::"v"(tp), "s"(L_RES + 2048) : "memory");
  }

  const int rg = xcd * 4 + (bi >> 3), cs = bi & 7;
  const int rp = xcd * 2 + (bi >> 4), sl = bi & 15;
  const int unit0 = sl < 5 ? 2 * sl : 3 * sl - 5, nunit = sl < 5 ? 2 : 3;
  const int mx = MIX * (bi >> 3);
  const int off3 = ((cs + mx) & 3) * 4, off4 = ((sl + (mx & 2)) & 3) * 8, off5 = (0x080e161c00ull >> (8 * ((cs + mx) % 5))) & 0xff;
  u32x16 sg[7];
  u32x4 res[2];
  auto stg = [&](int i) { return u32x4{sg[i >> 2][(i & 3) * 4], sg[i >> 2][(i & 3) * 4 + 1], sg[i >> 2][(i & 3) * 4 + 2], sg[i >> 2][(i & 3) * 4 + 3]}; };
  unsigned rowoff;
  const int head = hg * 4 + (wave >> 1), kh = wave & 1;
  unsigned koff, voff, vinit, a0, a1, a2, v30, v31, v32, vw, tpl, tph;
  const char *kb, *vb, *sa, *sb, *so, *sh;
  auto setup = [&](auto first) {
    auto v192 = [&](int j) { const int idx = j * 64 + lane, row = idx / 12; return (unsigned)(row * (F_ * 2) + (idx - row * 12) * 16); };
    auto w2at = [&](int pos) {
      int ks = pos + off5; ks = ks >= 43 ? ks - 43 : ks;
      return (const char*)w2 + (unsigned)(((lw & 1) * 43 + ks) * 16 * D_ * 2 + cs * 128);
    };
    auto w1at = [&](int pos) { return (const char*)w1 + (unsigned)(((pos + off4) & 31) * 16 * F_ * 2 + unit0 * 64); };
    vw = (lane >> 3) * (D_ * 2) + vrow128;
    v30 = v192(0); v31 = v192(1); v32 = v192(2);
    koff = lane * 16; voff = h * 256 + (lane & 15) * 16;
    kb = ws + WS_K + (unsigned)(head * 65536 + kh * 32768); vb = kb + (WS_V - WS_K);
    vinit = (r32 == 16 || r32 == 20) ? 0x3f803f80u : 0u;
    so = (const char*)wo + (unsigned)(((wave >> 2) * 256 + ((lw * 4 + off3) & 15) * 16) * D_ * 2 + cs * 128) - 8192;
    sh = w1at(16 + wave * 2);
    const char* tp;
    if constexpr (first.value) {
      a0 = v30; a1 = v31; a2 = v32; sa = w1at(lw * 4); sb = w2at((lw >> 1) * 7);
      const char* t0 = (const char*)((lw & 1) ? w2 : w1) + (unsigned)(b * 5504 + (lw >> 1) * 21 * 128);
      const char* t1 = (const char*)wo + (unsigned)(b * 2048 + lw * 512) - 22 * 128;
      const char* t2 = (const char*)b1 + (unsigned)(((b * 4 + lw) & 15) * 168) - 26 * 128;
      const int l = min(lane, 26);
      tp = (l < 22 ? t0 : l < 26 ? t1 : t2) + l * 128;
    } else {
      a0 = vw; a1 = vw + 8 * D_ * 2; a2 = vw + 16 * D_ * 2; sa = w2at(15 + (lw >> 1) * 14); sb = w2at(21 + (lw >> 1) * 14);
      tp = kb;
    }
    tpl = (unsigned)(u64)tp; tph = (unsigned)((u64)tp >> 32);
    asm volatile("" : "+v"(koff), "+v"(voff), "+v"(vinit), "+v"(a0), "+v"(a1), "+v"(a2), "+v"(v30), "+v"(v31), "+v"(v32), "+v"(vw), "+v"(tpl), "+v"(tph));
    asm volatile("" : "+s"(kb), "+s"(vb), "+s"(sa), "+s"(sb), "+s"(so), "+s"(sh));
  };
  {
    prog[wave] = wave < 4 ? 0 : 2 * NINIT + lw; prog[8 + lw] = 0;
    STAMP(1);
    float s4[4] = {0.f, 0.f, 0.f, 0.f}, q4[4] = {0.f, 0.f, 0.f, 0.f};
    static_for<0, 8>([&](auto ii) {
      constexpr int i = ii.value;
      asm volatile("s_waitcnt vmcnt(%1)" : "+v"(xv[i]) : "n"(10 - i));
#pragma unroll
      for (int j = 0; j < 4; j++) { s4[j] = dot2(xv[i][j], 0x3f803f80u, s4[j]); q4[j] = dot2(xv[i][j], xv[i][j], q4[j]); }
    });
    FENCE();
#pragma unroll
    for (int c = 1; c < NINIT; c++) kv8(c);
    asm volatile("s_mov_b32 m0, %2\n\ts_nop 0\n\tglobal_load_lds_dword %0, %1" ::"v"((lane & 7) * (3 * D_ * 2)), "s"(sw0 + (unsigned)(((c0 & 7) * 8 + wave) * (8 * 3 * D_ * 2))), "s"(L_RES + 2048) : "memory");
    STAMP(25);
    float sx = (s4[0] + s4[1]) + (s4[2] + s4[3]), sxx = (q4[0] + q4[1]) + (q4[2] + q4[3]);
    sx += asf(__builtin_amdgcn_mov_dpp(asu(sx), 0xB1, 0xf, 0xf, true)); sxx += asf(__builtin_amdgcn_mov_dpp(asu(sxx), 0xB1, 0xf, 0xf, true));
    sx += asf(__builtin_amdgcn_mov_dpp(asu(sx), 0x4E, 0xf, 0xf, true)); sxx += asf(__builtin_amdgcn_mov_dpp(asu(sxx), 0x4E, 0xf, 0xf, true));
    sx += asf(__builtin_amdgcn_mov_dpp(asu(sx), 0x141, 0xf, 0xf, true)); sxx += asf(__builtin_amdgcn_mov_dpp(asu(sxx), 0x141, 0xf, 0xf, true));
    const float mean = sx * (1.f / D_);
    const float var = rbf(fmaxf(sxx * (1.f / D_) - mean * mean, 0.f));
    const float rstd = __builtin_amdgcn_rcpf(rbf(__builtin_amdgcn_sqrtf(rbf(var + 1e-5f)))), nmr = -rbf(mean) * rstd;
    const int r = lw * 8 + (lane >> 3);
    LAS char* dst = smem + (wave >> 2) * 32768 + seg * 512 + ((r ^ seg) << 4);
#pragma unroll
    for (int i0 = 0; i0 < 8; i0 += 2) {
      f32x2 y[8];
#pragma unroll
      for (int j = 0; j < 8; j++) { const unsigned u = xv[i0 + (j >> 2)][j & 3]; y[j] = f32x2{bflo(u), bfhi(u)}; }
      FENCE();
#pragma unroll
      for (int j = 0; j < 8; j++) y[j] = pfma(y[j], pk(rstd), pk(nmr));
      FENCE();
      const u32x4 o0 = {pack2v(y[0]), pack2v(y[1]), pack2v(y[2]), pack2v(y[3])}, o1 = {pack2v(y[4]), pack2v(y[5]), pack2v(y[6]), pack2v(y[7])};
      *(LAS u32x4*)(dst + ((i0 + xr) & 7) * 4096) = o0;
      *(LAS u32x4*)(dst + ((i0 + 1 + xr) & 7) * 4096) = o1;
      FENCE();
    }
    STAMP(23);
    if (wave < 4) setup(std::true_type{});
    wait_vm<(QT < 2)>();
    BARRIER();
  }
  if (wave >= 4) {
    load_tag();
#pragma unroll 1
    for (int n = lw + 2 * NINIT; n < 24; n += 4) {
      if (n >= 10) lds_spin<SL1>(L_RES + p * 4, n == 23 ? max(15, QREL) : n >= 16 ? max(n - 9, QREL) : n - 9);
      const int c = n < 16 ? n >> 1 : n - 16;
      dma_slot(sw0 + (unsigned)(((c + c0) & 7) * (64 * 3 * D_ * 2) + (n < 16 ? 1024 + (n & 1) * 1024 : 0)), vw0, L_W1 + (n % 10) * 8192);
      wait_vm<0>(); prog[wave] = n + 4;
    }
    STAMP(2);
    setup(std::false_type{});
    if (wave == 4 + ((20 - 2 * NINIT) & 3)) {
      lds_spin<SL1>(L_RES + 32 + p * 4, 1);
      publish(ws, WS_F1 + (hg * 32 + rg1) * 128, tag, lane);
      STAMP(5);
      wait_flags<PD1>(ws, WS_F1 + (hg * 32 + r32) * 128, tag);
    }
  } else {
    const int rb = wave >> 1, g = wave & 1;
    int aoff[4];
#pragma unroll
    for (int i = 0; i < 4; i++) aoff[i] = rb * 32768 + i * 1024 + h * 512 + ((r32 ^ (2 * i + h)) << 4);
    int boff[2];
    boff[0] = L_W1 + (8 * h + q) * 128 + (((4 * g + 2 * blk + (p >> 1)) ^ ((q >> 1) << 2)) << 4) + 8 * (p & 1); boff[1] = boff[0] + 40960;
    asm volatile("" : "+v"(boff[0]), "+v"(boff[1]));
    f32x16 acc[3];
#pragma unroll
    for (int u = 0; u < 3; u++) for (int i = 0; i < 16; i++) acc[u][i] = 0.f;
    unsigned fl;
    auto rd = [&](WFrag& f, int st) {
#pragma unroll
      for (int i = 0; i < 8; i++) {
        if (st >= 8) f.a[i] = *(LAS u32x4*)(smem + aoff[i & 3] + ((2 * st + (i >> 2) + c0) & 7) * 4096);
        else if (i < 4) f.a[i] = *(LAS u32x4*)(smem + aoff[i] + ((st + c0) & 7) * 4096);
        f.w[i] = tr_frag(smem + boff[((2 * st + (i >> 2)) % 10) / 5] + ((2 * st + (i >> 2)) % 5) * 8192 + (i & 3) * 2048, 512);
        if ((i & 3) == 3) prog[wave] = 2 * st + (i >> 2) + 1;
        if (i == 3) fl = *(volatile LAS unsigned*)(smem + L_RES + 16 + p * 4);
      }
    };
    auto mm = [&](const WFrag& f, int st) {
#pragma unroll
      for (int i = 0; i < 8; i++) {
        if (st >= 8) acc[0] = mfma(f.w[i], f.a[i], acc[0]);
        else if (i < 4) acc[1] = mfma(f.w[i], f.a[i], acc[1]);
        else acc[2] = mfma(f.a[i & 3], f.w[i], acc[2]);
      }
    };
    auto sched = [&]() {
      SGB(0x8, 1); SGB(0x2, 6);
#pragma unroll
      for (int i = 0; i < 7; i++) { SGB(0x80, 3); SGB(0x8, 1); }
    };
    auto step = [&](int st) { FENCE(); if (st >= NINIT && __builtin_expect(__any(fl < 2 * st + 2), 0)) lds_spin<0>(L_RES + 16 + p * 4, 2 * st + 2); FENCE(); };
    WFrag fa, fb;
    STAMP(24);
    rd(fa, 0);
    static_for<0, 3>([&](auto cc) {
      constexpr int c = 2 * cc.value + 1;
      step(c);
      rd(fb, c); mm(fa, c - 1); sched();
      step(c + 1);
      rd(fa, c + 1); mm(fb, c); sched();
    });
    step(7);
    STAMP(2);
    rd(fb, 7); mm(fa, 6); sched();
    FENCE();
    mm(fb, 7);
    STAMP(3);
    const int tile = rg1 * 2 + rb;
    const unsigned so1 = ((hg * 4 + 2 * g) * 64 + tile) * 1024;
    {
      unsigned o[8];
#pragma unroll
      for (int i = 0; i < 8; i++) o[i] = pack2(acc[1][2 * i], acc[1][2 * i + 1]);
      const unsigned pos = h * 32 + ((r32 & 19) | ((r32 & 4) << 1) | ((r32 & 8) >> 1));
      st16(rs, pos * 16, WS_K + so1, u32x4{o[0], o[1], o[2], o[3]});
      st16(rs, pos * 16, WS_K + so1 + 65536, u32x4{o[4], o[5], o[6], o[7]});
#pragma unroll
      for (int i = 0; i < 8; i++) o[i] = pack2(acc[2][2 * i], acc[2][2 * i + 1]);
      const unsigned vo = (r32 >> 4) * 65536 + h * 256 + (r32 & 15) * 16;
      st16(rs, vo, WS_V + so1, u32x4{o[0], o[1], o[2], o[3]});
      st16(rs, vo, WS_V + so1 + 512, u32x4{o[4], o[5], o[6], o[7]});
      STAMP(4);
      step(8); rd(fa, 8);
      step(9); rd(fb, 9); mm(fa, 8); sched();
      FENCE(); wait_vm<0>(); prog[8 + wave] = 1;
      STAMP(5);
      step(10); rd(fa, 10); mm(fb, 9); sched();
      step(11); rd(fb, 11); mm(fa, 10); sched();
      FENCE();
      mm(fb, 11);
#pragma unroll
      for (int i = 0; i < 8; i++) o[i] = pack2v(f32x2{acc[0][2 * i], acc[0][2 * i + 1]} * pk(0.25f * LOG2E));
      LAS char* d = smem + L_Q1 + (4 * g + rb) * 1024 + lane * 16;
      *(LAS u32x4*)d = u32x4{o[0], o[1], o[2], o[3]};
      *(LAS u32x4*)(d + 2048) = u32x4{o[4], o[5], o[6], o[7]};
    }
  }

  {
    u32x4 q0, q1;
    const unsigned qa = L_Q1 + wave * 1024 + lane * 16, qb = qa ^ 1024;
    const int drop = (wave >> 2) ^ 1;
    if (wave < 4) __builtin_amdgcn_s_setprio(2); else __builtin_amdgcn_s_setprio(1);
    f32x16 o0, o1;
#pragma unroll
    for (int i = 0; i < 16; i++) { o0[i] = 0.f; o1[i] = 0.f; }
    {
      const int ol = wave * 8192, hl = L_B1 + (16 + wave * 2) * 3072, ga = 16 * F_ * 2 - (wave >> 2) * (16 * F_ * 2 - 24 * D_ * 2);
      sg[6][14] = tpl; sg[6][15] = tph;
      STAMP(6); STAMPC(26);
      asm volatile(
        "s_mov_b64 s[40:41], %[kin]\n\ts_mov_b64 s[42:43], %[vin]\n\ts_mov_b64 s[46:47], %[sa]\n\ts_mov_b64 s[48:49], %[sb]\n\ts_mov_b64 s[50:51], %[so]\n\ts_mov_b64 s[52:53], %[sh]\n\t"
        "v_mov_b32 v96, %[vinit]\n\tv_mov_b32 v112, %[vinit]\n\tv_mov_b32 v97, %[vinit]\n\tv_mov_b32 v113, %[vinit]\n\tv_mov_b32 v98, %[vinit]\n\tv_mov_b32 v114, %[vinit]\n\t"
        "v_mov_b32 v99, %[vinit]\n\tv_mov_b32 v115, %[vinit]\n\tv_mov_b32 v100, %[vinit]\n\tv_mov_b32 v116, %[vinit]\n\tv_mov_b32 v101, %[vinit]\n\tv_mov_b32 v117, %[vinit]\n\t"
        "v_mov_b32 v102, %[vinit]\n\tv_mov_b32 v118, %[vinit]\n\tv_mov_b32 v103, %[vinit]\n\tv_mov_b32 v119, %[vinit]\n\tv_mov_b32 v104, %[vinit]\n\tv_mov_b32 v120, %[vinit]\n\t"
        "v_mov_b32 v105, %[vinit]\n\tv_mov_b32 v121, %[vinit]\n\tv_mov_b32 v106, %[vinit]\n\tv_mov_b32 v122, %[vinit]\n\tv_mov_b32 v107, %[vinit]\n\tv_mov_b32 v123, %[vinit]\n\t"
        "v_mov_b32 v108, %[vinit]\n\tv_mov_b32 v124, %[vinit]\n\tv_mov_b32 v109, %[vinit]\n\tv_mov_b32 v125, %[vinit]\n\tv_mov_b32 v110, %[vinit]\n\tv_mov_b32 v126, %[vinit]\n\t"
        "v_mov_b32 v111, %[vinit]\n\tv_mov_b32 v127, %[vinit]\n\tv_mov_b32 v128, 0xdddd\n\ts_waitcnt lgkmcnt(0)\n\ts_barrier\n\t"
        "global_load_dwordx4 v[80:83], %[koff], s[40:41] offset:0 sc1\n\tds_read_b128 %[q0], %[qa]\n\tds_read_b128 %[q1], %[qb]\n\t"
        "global_load_dwordx4 v[84:87], %[koff], s[40:41] offset:1024 sc1\n\ts_mov_b64 exec, %[vm]\n\tglobal_load_dwordx4 v[96:99], %[voff], s[42:43] offset:0 sc1\n\t"
        "global_load_dwordx4 v[112:115], %[voff], s[42:43] offset:512 sc1\n\ts_mov_b64 exec, -1\n\tglobal_load_dwordx4 v[88:91], %[koff], s[40:41] offset:2048 sc1\n\t"
        "s_mov_b64 exec, %[vm]\n\tglobal_load_dwordx4 v[100:103], %[voff], s[42:43] offset:1024 sc1\n\tglobal_load_dwordx4 v[116:119], %[voff], s[42:43] offset:1536 sc1\n\t"
        "s_mov_b64 exec, -1\n\tglobal_load_dwordx4 v[92:95], %[koff], s[40:41] offset:3072 sc1\n\ts_mov_b64 exec, %[vm]\n\t"
        "global_load_dwordx4 v[104:107], %[voff], s[42:43] offset:2048 sc1\n\tglobal_load_dwordx4 v[120:123], %[voff], s[42:43] offset:2560 sc1\n\ts_mov_b64 exec, -1\n\t"
        "s_waitcnt lgkmcnt(0)\n\ts_waitcnt vmcnt(9)\n\tv_mfma_f32_32x32x16_bf16 v[48:63], v[80:83], %[q0], 0\n\ts_nop 15\n\tv_exp_f32 v48, v48\n\tv_exp_f32 v49, v49\n\t"
        "v_exp_f32 v50, v50\n\tv_exp_f32 v51, v51\n\tv_exp_f32 v52, v52\n\tv_mfma_f32_32x32x16_bf16 v[64:79], v[80:83], %[q1], 0\n\ts_add_u32 s40, s40, 4096\n\ts_addc_u32 s41, s41, 0\n\t"
        "global_load_dwordx4 v[80:83], %[koff], s[40:41] offset:0 sc1\n\ts_mov_b64 exec, %[vm]\n\tglobal_load_dwordx4 v[108:111], %[voff], s[42:43] offset:3072 sc1\n\t"
        "global_load_dwordx4 v[124:127], %[voff], s[42:43] offset:3584 sc1\n\ts_mov_b64 exec, -1\n\tv_exp_f32 v53, v53\n\tv_exp_f32 v54, v54\n\tv_exp_f32 v55, v55\n\tv_exp_f32 v56, v56\n\t"
        "v_exp_f32 v57, v57\n\ts_waitcnt vmcnt(9)\n\tv_smfmac_f32_32x32x32_bf16 %[o0], v[96:99], v[48:55], v128\n\tv_exp_f32 v58, v58\n\tv_exp_f32 v59, v59\n\tv_exp_f32 v60, v60\n\t"
        "v_exp_f32 v61, v61\n\tv_exp_f32 v62, v62\n\tv_exp_f32 v63, v63\n\tv_exp_f32 v64, v64\n\tv_exp_f32 v65, v65\n\tv_smfmac_f32_32x32x32_bf16 %[o0], v[112:115], v[56:63], v128\n\t"
        "v_exp_f32 v66, v66\n\tv_exp_f32 v67, v67\n\tv_exp_f32 v68, v68\n\tv_mfma_f32_32x32x16_bf16 v[48:63], v[84:87], %[q0], 0\n\tv_exp_f32 v69, v69\n\tv_exp_f32 v70, v70\n\t"
        "v_exp_f32 v71, v71\n\tv_exp_f32 v72, v72\n\tv_exp_f32 v73, v73\n\tv_smfmac_f32_32x32x32_bf16 %[o1], v[96:99], v[64:71], v128\n\tv_exp_f32 v74, v74\n\tv_exp_f32 v75, v75\n\t"
        "v_exp_f32 v76, v76\n\tv_exp_f32 v77, v77\n\tv_exp_f32 v78, v78\n\tv_exp_f32 v79, v79\n\tv_exp_f32 v48, v48\n\tv_exp_f32 v49, v49\n\t"
        "v_smfmac_f32_32x32x32_bf16 %[o1], v[112:115], v[72:79], v128\n\tv_exp_f32 v50, v50\n\tv_exp_f32 v51, v51\n\tv_exp_f32 v52, v52\n\t"
        "v_mfma_f32_32x32x16_bf16 v[64:79], v[84:87], %[q1], 0\n\tglobal_load_dwordx4 v[84:87], %[koff], s[40:41] offset:1024 sc1\n\ts_add_u32 s42, s42, 4096\n\ts_addc_u32 s43, s43, 0\n\t"
        "s_mov_b64 exec, %[vm]\n\tglobal_load_dwordx4 v[96:99], %[voff], s[42:43] offset:0 sc1\n\tglobal_load_dwordx4 v[112:115], %[voff], s[42:43] offset:512 sc1\n\ts_mov_b64 exec, -1\n\t"
        "v_exp_f32 v53, v53\n\tv_exp_f32 v54, v54\n\tv_exp_f32 v55, v55\n\tv_exp_f32 v56, v56\n\tv_exp_f32 v57, v57\n\ts_waitcnt vmcnt(9)\n\t"
        "v_smfmac_f32_32x32x32_bf16 %[o0], v[100:103], v[48:55], v128\n\tv_exp_f32 v58, v58\n\tv_exp_f32 v59, v59\n\tv_exp_f32 v60, v60\n\tv_exp_f32 v61, v61\n\tv_exp_f32 v62, v62\n\t"
        "v_exp_f32 v63, v63\n\tv_exp_f32 v64, v64\n\tv_exp_f32 v65, v65\n\tv_smfmac_f32_32x32x32_bf16 %[o0], v[116:119], v[56:63], v128\n\tv_exp_f32 v66, v66\n\tv_exp_f32 v67, v67\n\t"
        "v_exp_f32 v68, v68\n\tv_mfma_f32_32x32x16_bf16 v[48:63], v[88:91], %[q0], 0\n\tv_exp_f32 v69, v69\n\tv_exp_f32 v70, v70\n\tv_exp_f32 v71, v71\n\tv_exp_f32 v72, v72\n\t"
        "v_exp_f32 v73, v73\n\tv_smfmac_f32_32x32x32_bf16 %[o1], v[100:103], v[64:71], v128\n\tv_exp_f32 v74, v74\n\tv_exp_f32 v75, v75\n\tv_exp_f32 v76, v76\n\tv_exp_f32 v77, v77\n\t"
        "v_exp_f32 v78, v78\n\tv_exp_f32 v79, v79\n\tv_exp_f32 v48, v48\n\tv_exp_f32 v49, v49\n\tv_smfmac_f32_32x32x32_bf16 %[o1], v[116:119], v[72:79], v128\n\tv_exp_f32 v50, v50\n\t"
        "v_exp_f32 v51, v51\n\tv_exp_f32 v52, v52\n\tv_mfma_f32_32x32x16_bf16 v[64:79], v[88:91], %[q1], 0\n\tglobal_load_dwordx4 v[88:91], %[koff], s[40:41] offset:2048 sc1\n\t"
        "s_mov_b64 exec, %[vm]\n\tglobal_load_dwordx4 v[100:103], %[voff], s[42:43] offset:1024 sc1\n\tglobal_load_dwordx4 v[116:119], %[voff], s[42:43] offset:1536 sc1\n\t"
        "s_mov_b64 exec, -1\n\tv_exp_f32 v53, v53\n\tv_exp_f32 v54, v54\n\tv_exp_f32 v55, v55\n\tv_exp_f32 v56, v56\n\tv_exp_f32 v57, v57\n\ts_waitcnt vmcnt(9)\n\t"
        "v_smfmac_f32_32x32x32_bf16 %[o0], v[104:107], v[48:55], v128\n\tv_exp_f32 v58, v58\n\tv_exp_f32 v59, v59\n\tv_exp_f32 v60, v60\n\tv_exp_f32 v61, v61\n\tv_exp_f32 v62, v62\n\t"
        "v_exp_f32 v63, v63\n\tv_exp_f32 v64, v64\n\tv_exp_f32 v65, v65\n\tv_smfmac_f32_32x32x32_bf16 %[o0], v[120:123], v[56:63], v128\n\tv_exp_f32 v66, v66\n\tv_exp_f32 v67, v67\n\t"
        "v_exp_f32 v68, v68\n\tv_mfma_f32_32x32x16_bf16 v[48:63], v[92:95], %[q0], 0\n\tv_exp_f32 v69, v69\n\tv_exp_f32 v70, v70\n\tv_exp_f32 v71, v71\n\tv_exp_f32 v72, v72\n\t"
        "v_exp_f32 v73, v73\n\tv_smfmac_f32_32x32x32_bf16 %[o1], v[104:107], v[64:71], v128\n\tv_exp_f32 v74, v74\n\tv_exp_f32 v75, v75\n\tv_exp_f32 v76, v76\n\tv_exp_f32 v77, v77\n\t"
        "v_exp_f32 v78, v78\n\tv_exp_f32 v79, v79\n\tv_exp_f32 v48, v48\n\tv_exp_f32 v49, v49\n\tv_smfmac_f32_32x32x32_bf16 %[o1], v[120:123], v[72:79], v128\n\tv_exp_f32 v50, v50\n\t"
        "v_exp_f32 v51, v51\n\tv_exp_f32 v52, v52\n\tv_mfma_f32_32x32x16_bf16 v[64:79], v[92:95], %[q1], 0\n\tglobal_load_dwordx4 v[92:95], %[koff], s[40:41] offset:3072 sc1\n\t"
        "s_mov_b64 exec, %[vm]\n\tglobal_load_dwordx4 v[104:107], %[voff], s[42:43] offset:2048 sc1\n\tglobal_load_dwordx4 v[120:123], %[voff], s[42:43] offset:2560 sc1\n\t"
        "s_mov_b64 exec, -1\n\tv_exp_f32 v53, v53\n\tv_exp_f32 v54, v54\n\tv_exp_f32 v55, v55\n\tv_exp_f32 v56, v56\n\tv_exp_f32 v57, v57\n\ts_waitcnt vmcnt(9)\n\t"
        "v_smfmac_f32_32x32x32_bf16 %[o0], v[108:111], v[48:55], v128\n\tv_exp_f32 v58, v58\n\tv_exp_f32 v59, v59\n\tv_exp_f32 v60, v60\n\tv_exp_f32 v61, v61\n\tv_exp_f32 v62, v62\n\t"
        "v_exp_f32 v63, v63\n\tv_exp_f32 v64, v64\n\tv_exp_f32 v65, v65\n\tv_smfmac_f32_32x32x32_bf16 %[o0], v[124:127], v[56:63], v128\n\tv_exp_f32 v66, v66\n\tv_exp_f32 v67, v67\n\t"
        "v_exp_f32 v68, v68\n\tv_mfma_f32_32x32x16_bf16 v[48:63], v[80:83], %[q0], 0\n\tglobal_load_dword v253, v[254:255], off\n\tv_exp_f32 v69, v69\n\tv_exp_f32 v70, v70\n\t"
        "v_exp_f32 v71, v71\n\tv_exp_f32 v72, v72\n\tv_exp_f32 v73, v73\n\tv_smfmac_f32_32x32x32_bf16 %[o1], v[108:111], v[64:71], v128\n\tv_exp_f32 v74, v74\n\tv_exp_f32 v75, v75\n\t"
        "v_exp_f32 v76, v76\n\tv_exp_f32 v77, v77\n\tv_exp_f32 v78, v78\n\tv_exp_f32 v79, v79\n\tv_exp_f32 v48, v48\n\tv_exp_f32 v49, v49\n\t"
        "v_smfmac_f32_32x32x32_bf16 %[o1], v[124:127], v[72:79], v128\n\tv_exp_f32 v50, v50\n\tv_exp_f32 v51, v51\n\tv_exp_f32 v52, v52\n\t"
        "v_mfma_f32_32x32x16_bf16 v[64:79], v[80:83], %[q1], 0\n\ts_add_u32 s40, s40, 4096\n\ts_addc_u32 s41, s41, 0\n\tglobal_load_dwordx4 v[80:83], %[koff], s[40:41] offset:0 sc1\n\t"
        "s_mov_b64 exec, %[vm]\n\tglobal_load_dwordx4 v[108:111], %[voff], s[42:43] offset:3072 sc1\n\tglobal_load_dwordx4 v[124:127], %[voff], s[42:43] offset:3584 sc1\n\t"
        "s_mov_b64 exec, -1\n\tv_exp_f32 v53, v53\n\tv_exp_f32 v54, v54\n\tv_exp_f32 v55, v55\n\tv_exp_f32 v56, v56\n\tv_exp_f32 v57, v57\n\ts_waitcnt vmcnt(10)\n\t"
        "v_smfmac_f32_32x32x32_bf16 %[o0], v[96:99], v[48:55], v128\n\tv_exp_f32 v58, v58\n\tv_exp_f32 v59, v59\n\tv_exp_f32 v60, v60\n\tv_exp_f32 v61, v61\n\tv_exp_f32 v62, v62\n\t"
        "v_exp_f32 v63, v63\n\tv_exp_f32 v64, v64\n\tv_exp_f32 v65, v65\n\tv_smfmac_f32_32x32x32_bf16 %[o0], v[112:115], v[56:63], v128\n\tv_exp_f32 v66, v66\n\tv_exp_f32 v67, v67\n\t"
        "v_exp_f32 v68, v68\n\tv_mfma_f32_32x32x16_bf16 v[48:63], v[84:87], %[q0], 0\n\tv_exp_f32 v69, v69\n\tv_exp_f32 v70, v70\n\tv_exp_f32 v71, v71\n\tv_exp_f32 v72, v72\n\t"
        "v_exp_f32 v73, v73\n\tv_smfmac_f32_32x32x32_bf16 %[o1], v[96:99], v[64:71], v128\n\tv_exp_f32 v74, v74\n\tv_exp_f32 v75, v75\n\tv_exp_f32 v76, v76\n\tv_exp_f32 v77, v77\n\t"
        "v_exp_f32 v78, v78\n\tv_exp_f32 v79, v79\n\tv_exp_f32 v48, v48\n\tv_exp_f32 v49, v49\n\tv_smfmac_f32_32x32x32_bf16 %[o1], v[112:115], v[72:79], v128\n\tv_exp_f32 v50, v50\n\t"
        "v_exp_f32 v51, v51\n\tv_exp_f32 v52, v52\n\tv_mfma_f32_32x32x16_bf16 v[64:79], v[84:87], %[q1], 0\n\tglobal_load_dwordx4 v[84:87], %[koff], s[40:41] offset:1024 sc1\n\t"
        "s_add_u32 s42, s42, 4096\n\ts_addc_u32 s43, s43, 0\n\ts_mov_b64 exec, %[vm]\n\tglobal_load_dwordx4 v[96:99], %[voff], s[42:43] offset:0 sc1\n\t"
        "global_load_dwordx4 v[112:115], %[voff], s[42:43] offset:512 sc1\n\ts_mov_b64 exec, -1\n\tv_exp_f32 v53, v53\n\tv_exp_f32 v54, v54\n\tv_exp_f32 v55, v55\n\tv_exp_f32 v56, v56\n\t"
        "v_exp_f32 v57, v57\n\ts_waitcnt vmcnt(10)\n\tv_smfmac_f32_32x32x32_bf16 %[o0], v[100:103], v[48:55], v128\n\tv_exp_f32 v58, v58\n\tv_exp_f32 v59, v59\n\tv_exp_f32 v60, v60\n\t"
        "v_exp_f32 v61, v61\n\tv_exp_f32 v62, v62\n\tv_exp_f32 v63, v63\n\tv_exp_f32 v64, v64\n\tv_exp_f32 v65, v65\n\tv_smfmac_f32_32x32x32_bf16 %[o0], v[116:119], v[56:63], v128\n\t"
        "v_exp_f32 v66, v66\n\tv_exp_f32 v67, v67\n\tv_exp_f32 v68, v68\n\tv_mfma_f32_32x32x16_bf16 v[48:63], v[88:91], %[q0], 0\n\tv_exp_f32 v69, v69\n\tv_exp_f32 v70, v70\n\t"
        "v_exp_f32 v71, v71\n\tv_exp_f32 v72, v72\n\tv_exp_f32 v73, v73\n\tv_smfmac_f32_32x32x32_bf16 %[o1], v[100:103], v[64:71], v128\n\tv_exp_f32 v74, v74\n\tv_exp_f32 v75, v75\n\t"
        "v_exp_f32 v76, v76\n\tv_exp_f32 v77, v77\n\tv_exp_f32 v78, v78\n\tv_exp_f32 v79, v79\n\tv_exp_f32 v48, v48\n\tv_exp_f32 v49, v49\n\t"
        "v_smfmac_f32_32x32x32_bf16 %[o1], v[116:119], v[72:79], v128\n\tv_exp_f32 v50, v50\n\tv_exp_f32 v51, v51\n\tv_exp_f32 v52, v52\n\t"
        "v_mfma_f32_32x32x16_bf16 v[64:79], v[88:91], %[q1], 0\n\tglobal_load_dwordx4 v[88:91], %[koff], s[40:41] offset:2048 sc1\n\ts_mov_b64 exec, %[vm]\n\t"
        "global_load_dwordx4 v[100:103], %[voff], s[42:43] offset:1024 sc1\n\tglobal_load_dwordx4 v[116:119], %[voff], s[42:43] offset:1536 sc1\n\ts_mov_b64 exec, -1\n\t"
        "v_exp_f32 v53, v53\n\tv_exp_f32 v54, v54\n\tv_exp_f32 v55, v55\n\tv_exp_f32 v56, v56\n\tv_exp_f32 v57, v57\n\ts_waitcnt vmcnt(10)\n\t"
        "v_smfmac_f32_32x32x32_bf16 %[o0], v[104:107], v[48:55], v128\n\tv_exp_f32 v58, v58\n\tv_exp_f32 v59, v59\n\tv_exp_f32 v60, v60\n\tv_exp_f32 v61, v61\n\tv_exp_f32 v62, v62\n\t"
        "v_exp_f32 v63, v63\n\tv_exp_f32 v64, v64\n\tv_exp_f32 v65, v65\n\tv_smfmac_f32_32x32x32_bf16 %[o0], v[120:123], v[56:63], v128\n\tv_exp_f32 v66, v66\n\tv_exp_f32 v67, v67\n\t"
        "v_exp_f32 v68, v68\n\tv_mfma_f32_32x32x16_bf16 v[48:63], v[92:95], %[q0], 0\n\tv_exp_f32 v69, v69\n\tv_exp_f32 v70, v70\n\tv_exp_f32 v71, v71\n\tv_exp_f32 v72, v72\n\t"
        "v_exp_f32 v73, v73\n\tv_smfmac_f32_32x32x32_bf16 %[o1], v[104:107], v[64:71], v128\n\tv_exp_f32 v74, v74\n\tv_exp_f32 v75, v75\n\tv_exp_f32 v76, v76\n\tv_exp_f32 v77, v77\n\t"
        "v_exp_f32 v78, v78\n\tv_exp_f32 v79, v79\n\tv_exp_f32 v48, v48\n\tv_exp_f32 v49, v49\n\tv_smfmac_f32_32x32x32_bf16 %[o1], v[120:123], v[72:79], v128\n\tv_exp_f32 v50, v50\n\t"
        "v_exp_f32 v51, v51\n\tv_exp_f32 v52, v52\n\tv_mfma_f32_32x32x16_bf16 v[64:79], v[92:95], %[q1], 0\n\tglobal_load_dwordx4 v[92:95], %[koff], s[40:41] offset:3072 sc1\n\t"
        "s_mov_b64 exec, %[vm]\n\tglobal_load_dwordx4 v[104:107], %[voff], s[42:43] offset:2048 sc1\n\tglobal_load_dwordx4 v[120:123], %[voff], s[42:43] offset:2560 sc1\n\t"
        "s_mov_b64 exec, -1\n\tv_exp_f32 v53, v53\n\tv_exp_f32 v54, v54\n\tv_exp_f32 v55, v55\n\tv_exp_f32 v56, v56\n\tv_exp_f32 v57, v57\n\ts_waitcnt vmcnt(9)\n\t"
        "v_smfmac_f32_32x32x32_bf16 %[o0], v[108:111], v[48:55], v128\n\tv_exp_f32 v58, v58\n\tv_exp_f32 v59, v59\n\tv_exp_f32 v60, v60\n\tv_exp_f32 v61, v61\n\tv_exp_f32 v62, v62\n\t"
        "v_exp_f32 v63, v63\n\tv_exp_f32 v64, v64\n\tv_exp_f32 v65, v65\n\tv_smfmac_f32_32x32x32_bf16 %[o0], v[124:127], v[56:63], v128\n\tv_exp_f32 v66, v66\n\tv_exp_f32 v67, v67\n\t"
        "v_exp_f32 v68, v68\n\tv_mfma_f32_32x32x16_bf16 v[48:63], v[80:83], %[q0], 0\n\tv_exp_f32 v69, v69\n\tv_exp_f32 v70, v70\n\tv_exp_f32 v71, v71\n\tv_exp_f32 v72, v72\n\t"
        "v_exp_f32 v73, v73\n\tv_smfmac_f32_32x32x32_bf16 %[o1], v[108:111], v[64:71], v128\n\tv_exp_f32 v74, v74\n\tv_exp_f32 v75, v75\n\tv_exp_f32 v76, v76\n\tv_exp_f32 v77, v77\n\t"
        "v_exp_f32 v78, v78\n\tv_exp_f32 v79, v79\n\tv_exp_f32 v48, v48\n\tv_exp_f32 v49, v49\n\tv_smfmac_f32_32x32x32_bf16 %[o1], v[124:127], v[72:79], v128\n\tv_exp_f32 v50, v50\n\t"
        "v_exp_f32 v51, v51\n\tv_exp_f32 v52, v52\n\tv_mfma_f32_32x32x16_bf16 v[64:79], v[80:83], %[q1], 0\n\ts_add_u32 s40, s40, 4096\n\ts_addc_u32 s41, s41, 0\n\t"
        "global_load_dwordx4 v[80:83], %[koff], s[40:41] offset:0 sc1\n\ts_mov_b64 exec, %[vm]\n\tglobal_load_dwordx4 v[108:111], %[voff], s[42:43] offset:3072 sc1\n\t"
        "global_load_dwordx4 v[124:127], %[voff], s[42:43] offset:3584 sc1\n\ts_mov_b64 exec, -1\n\tv_exp_f32 v53, v53\n\tv_exp_f32 v54, v54\n\tv_exp_f32 v55, v55\n\tv_exp_f32 v56, v56\n\t"
        "v_exp_f32 v57, v57\n\ts_waitcnt vmcnt(9)\n\tv_smfmac_f32_32x32x32_bf16 %[o0], v[96:99], v[48:55], v128\n\tv_exp_f32 v58, v58\n\tv_exp_f32 v59, v59\n\tv_exp_f32 v60, v60\n\t"
        "v_exp_f32 v61, v61\n\tv_exp_f32 v62, v62\n\tv_exp_f32 v63, v63\n\tv_exp_f32 v64, v64\n\tv_exp_f32 v65, v65\n\tv_smfmac_f32_32x32x32_bf16 %[o0], v[112:115], v[56:63], v128\n\t"
        "v_exp_f32 v66, v66\n\tv_exp_f32 v67, v67\n\tv_exp_f32 v68, v68\n\tv_mfma_f32_32x32x16_bf16 v[48:63], v[84:87], %[q0], 0\n\tglobal_load_dwordx4 v[144:147], %[a0], s[46:47]\n\t"
        "v_exp_f32 v69, v69\n\tv_exp_f32 v70, v70\n\tv_exp_f32 v71, v71\n\tv_exp_f32 v72, v72\n\tv_exp_f32 v73, v73\n\tv_smfmac_f32_32x32x32_bf16 %[o1], v[96:99], v[64:71], v128\n\t"
        "v_exp_f32 v74, v74\n\tv_exp_f32 v75, v75\n\tv_exp_f32 v76, v76\n\tv_exp_f32 v77, v77\n\tv_exp_f32 v78, v78\n\tv_exp_f32 v79, v79\n\tv_exp_f32 v48, v48\n\tv_exp_f32 v49, v49\n\t"
        "v_smfmac_f32_32x32x32_bf16 %[o1], v[112:115], v[72:79], v128\n\tv_exp_f32 v50, v50\n\tv_exp_f32 v51, v51\n\tv_exp_f32 v52, v52\n\t"
        "v_mfma_f32_32x32x16_bf16 v[64:79], v[84:87], %[q1], 0\n\tglobal_load_dwordx4 v[84:87], %[koff], s[40:41] offset:1024 sc1\n\ts_add_u32 s42, s42, 4096\n\ts_addc_u32 s43, s43, 0\n\t"
        "s_mov_b64 exec, %[vm]\n\tglobal_load_dwordx4 v[96:99], %[voff], s[42:43] offset:0 sc1\n\tglobal_load_dwordx4 v[112:115], %[voff], s[42:43] offset:512 sc1\n\ts_mov_b64 exec, -1\n\t"
        "v_exp_f32 v53, v53\n\tv_exp_f32 v54, v54\n\tv_exp_f32 v55, v55\n\tv_exp_f32 v56, v56\n\tv_exp_f32 v57, v57\n\ts_waitcnt vmcnt(10)\n\t"
        "v_smfmac_f32_32x32x32_bf16 %[o0], v[100:103], v[48:55], v128\n\tv_exp_f32 v58, v58\n\tv_exp_f32 v59, v59\n\tv_exp_f32 v60, v60\n\tv_exp_f32 v61, v61\n\tv_exp_f32 v62, v62\n\t"
        "v_exp_f32 v63, v63\n\tv_exp_f32 v64, v64\n\tv_exp_f32 v65, v65\n\tv_smfmac_f32_32x32x32_bf16 %[o0], v[116:119], v[56:63], v128\n\tv_exp_f32 v66, v66\n\tv_exp_f32 v67, v67\n\t"
        "v_exp_f32 v68, v68\n\tv_mfma_f32_32x32x16_bf16 v[48:63], v[88:91], %[q0], 0\n\ts_add_u32 m0, %[ol], 0\n\ts_add_u32 s50, s50, 8192\n\ts_addc_u32 s51, s51, 0\n\t"
        "global_load_lds_dwordx4 %[vw], s[50:51]\n\tglobal_load_dwordx4 v[148:151], %[a1], s[46:47]\n\tv_exp_f32 v69, v69\n\tv_exp_f32 v70, v70\n\tv_exp_f32 v71, v71\n\t"
        "v_exp_f32 v72, v72\n\tv_exp_f32 v73, v73\n\tv_smfmac_f32_32x32x32_bf16 %[o1], v[100:103], v[64:71], v128\n\tv_exp_f32 v74, v74\n\tv_exp_f32 v75, v75\n\tv_exp_f32 v76, v76\n\t"
        "v_exp_f32 v77, v77\n\tv_exp_f32 v78, v78\n\tv_exp_f32 v79, v79\n\tv_exp_f32 v48, v48\n\tv_exp_f32 v49, v49\n\tv_smfmac_f32_32x32x32_bf16 %[o1], v[116:119], v[72:79], v128\n\t"
        "v_exp_f32 v50, v50\n\tv_exp_f32 v51, v51\n\tv_exp_f32 v52, v52\n\tv_mfma_f32_32x32x16_bf16 v[64:79], v[88:91], %[q1], 0\n\t"
        "global_load_dwordx4 v[88:91], %[koff], s[40:41] offset:2048 sc1\n\ts_mov_b64 exec, %[vm]\n\tglobal_load_dwordx4 v[100:103], %[voff], s[42:43] offset:1024 sc1\n\t"
        "global_load_dwordx4 v[116:119], %[voff], s[42:43] offset:1536 sc1\n\ts_mov_b64 exec, -1\n\tv_exp_f32 v53, v53\n\tv_exp_f32 v54, v54\n\tv_exp_f32 v55, v55\n\tv_exp_f32 v56, v56\n\t"
        "v_exp_f32 v57, v57\n\ts_waitcnt vmcnt(12)\n\tv_smfmac_f32_32x32x32_bf16 %[o0], v[104:107], v[48:55], v128\n\tv_exp_f32 v58, v58\n\tv_exp_f32 v59, v59\n\tv_exp_f32 v60, v60\n\t"
        "v_exp_f32 v61, v61\n\tv_exp_f32 v62, v62\n\tv_exp_f32 v63, v63\n\tv_exp_f32 v64, v64\n\tv_exp_f32 v65, v65\n\tv_smfmac_f32_32x32x32_bf16 %[o0], v[120:123], v[56:63], v128\n\t"
        "v_exp_f32 v66, v66\n\tv_exp_f32 v67, v67\n\tv_exp_f32 v68, v68\n\tv_mfma_f32_32x32x16_bf16 v[48:63], v[92:95], %[q0], 0\n\tglobal_load_dwordx4 v[152:155], %[a2], s[46:47]\n\t"
        "s_add_u32 s46, s46, %[ga]\n\ts_addc_u32 s47, s47, 0\n\ts_add_u32 m0, %[ol], 1024\n\ts_add_u32 s50, s50, 8192\n\ts_addc_u32 s51, s51, 0\n\t"
        "global_load_lds_dwordx4 %[vw], s[50:51]\n\tv_exp_f32 v69, v69\n\tv_exp_f32 v70, v70\n\tv_exp_f32 v71, v71\n\tv_exp_f32 v72, v72\n\tv_exp_f32 v73, v73\n\t"
        "v_smfmac_f32_32x32x32_bf16 %[o1], v[104:107], v[64:71], v128\n\tv_exp_f32 v74, v74\n\tv_exp_f32 v75, v75\n\tv_exp_f32 v76, v76\n\tv_exp_f32 v77, v77\n\tv_exp_f32 v78, v78\n\t"
        "v_exp_f32 v79, v79\n\tv_exp_f32 v48, v48\n\tv_exp_f32 v49, v49\n\tv_smfmac_f32_32x32x32_bf16 %[o1], v[120:123], v[72:79], v128\n\tv_exp_f32 v50, v50\n\tv_exp_f32 v51, v51\n\t"
        "v_exp_f32 v52, v52\n\tv_mfma_f32_32x32x16_bf16 v[64:79], v[92:95], %[q1], 0\n\tglobal_load_dwordx4 v[92:95], %[koff], s[40:41] offset:3072 sc1\n\ts_mov_b64 exec, %[vm]\n\t"
        "global_load_dwordx4 v[104:107], %[voff], s[42:43] offset:2048 sc1\n\tglobal_load_dwordx4 v[120:123], %[voff], s[42:43] offset:2560 sc1\n\ts_mov_b64 exec, -1\n\t"
        "v_exp_f32 v53, v53\n\tv_exp_f32 v54, v54\n\tv_exp_f32 v55, v55\n\tv_exp_f32 v56, v56\n\tv_exp_f32 v57, v57\n\ts_waitcnt vmcnt(14)\n\t"
        "v_smfmac_f32_32x32x32_bf16 %[o0], v[108:111], v[48:55], v128\n\tv_exp_f32 v58, v58\n\tv_exp_f32 v59, v59\n\tv_exp_f32 v60, v60\n\tv_exp_f32 v61, v61\n\tv_exp_f32 v62, v62\n\t"
        "v_exp_f32 v63, v63\n\tv_exp_f32 v64, v64\n\tv_exp_f32 v65, v65\n\tv_smfmac_f32_32x32x32_bf16 %[o0], v[124:127], v[56:63], v128\n\tv_exp_f32 v66, v66\n\tv_exp_f32 v67, v67\n\t"
        "v_exp_f32 v68, v68\n\tv_mfma_f32_32x32x16_bf16 v[48:63], v[80:83], %[q0], 0\n\tglobal_load_dwordx4 v[156:159], %[a0], s[46:47]\n\t"
        "global_load_dwordx4 v[160:163], %[a1], s[46:47]\n\tv_exp_f32 v69, v69\n\tv_exp_f32 v70, v70\n\tv_exp_f32 v71, v71\n\tv_exp_f32 v72, v72\n\tv_exp_f32 v73, v73\n\t"
        "v_smfmac_f32_32x32x32_bf16 %[o1], v[108:111], v[64:71], v128\n\tv_exp_f32 v74, v74\n\tv_exp_f32 v75, v75\n\tv_exp_f32 v76, v76\n\tv_exp_f32 v77, v77\n\tv_exp_f32 v78, v78\n\t"
        "v_exp_f32 v79, v79\n\tv_exp_f32 v48, v48\n\tv_exp_f32 v49, v49\n\tv_smfmac_f32_32x32x32_bf16 %[o1], v[124:127], v[72:79], v128\n\tv_exp_f32 v50, v50\n\tv_exp_f32 v51, v51\n\t"
        "v_exp_f32 v52, v52\n\tv_mfma_f32_32x32x16_bf16 v[64:79], v[80:83], %[q1], 0\n\ts_add_u32 s40, s40, 4096\n\ts_addc_u32 s41, s41, 0\n\t"
        "global_load_dwordx4 v[80:83], %[koff], s[40:41] offset:0 sc1\n\ts_mov_b64 exec, %[vm]\n\tglobal_load_dwordx4 v[108:111], %[voff], s[42:43] offset:3072 sc1\n\t"
        "global_load_dwordx4 v[124:127], %[voff], s[42:43] offset:3584 sc1\n\ts_mov_b64 exec, -1\n\tv_exp_f32 v53, v53\n\tv_exp_f32 v54, v54\n\tv_exp_f32 v55, v55\n\tv_exp_f32 v56, v56\n\t"
        "v_exp_f32 v57, v57\n\ts_waitcnt vmcnt(15)\n\tv_smfmac_f32_32x32x32_bf16 %[o0], v[96:99], v[48:55], v128\n\tv_exp_f32 v58, v58\n\tv_exp_f32 v59, v59\n\tv_exp_f32 v60, v60\n\t"
        "v_exp_f32 v61, v61\n\tv_exp_f32 v62, v62\n\tv_exp_f32 v63, v63\n\tv_exp_f32 v64, v64\n\tv_exp_f32 v65, v65\n\tv_smfmac_f32_32x32x32_bf16 %[o0], v[112:115], v[56:63], v128\n\t"
        "v_exp_f32 v66, v66\n\tv_exp_f32 v67, v67\n\tv_exp_f32 v68, v68\n\tv_mfma_f32_32x32x16_bf16 v[48:63], v[84:87], %[q0], 0\n\ts_add_u32 m0, %[ol], 2048\n\ts_add_u32 s50, s50, 8192\n\t"
        "s_addc_u32 s51, s51, 0\n\tglobal_load_lds_dwordx4 %[vw], s[50:51]\n\tglobal_load_dwordx4 v[164:167], %[a2], s[46:47]\n\ts_add_u32 s46, s46, %[ga]\n\ts_addc_u32 s47, s47, 0\n\t"
        "v_exp_f32 v69, v69\n\tv_exp_f32 v70, v70\n\tv_exp_f32 v71, v71\n\tv_exp_f32 v72, v72\n\tv_exp_f32 v73, v73\n\tv_smfmac_f32_32x32x32_bf16 %[o1], v[96:99], v[64:71], v128\n\t"
        "v_exp_f32 v74, v74\n\tv_exp_f32 v75, v75\n\tv_exp_f32 v76, v76\n\tv_exp_f32 v77, v77\n\tv_exp_f32 v78, v78\n\tv_exp_f32 v79, v79\n\tv_exp_f32 v48, v48\n\tv_exp_f32 v49, v49\n\t"
        "v_smfmac_f32_32x32x32_bf16 %[o1], v[112:115], v[72:79], v128\n\tv_exp_f32 v50, v50\n\tv_exp_f32 v51, v51\n\tv_exp_f32 v52, v52\n\t"
        "v_mfma_f32_32x32x16_bf16 v[64:79], v[84:87], %[q1], 0\n\tglobal_load_dwordx4 v[84:87], %[koff], s[40:41] offset:1024 sc1\n\ts_add_u32 s42, s42, 4096\n\ts_addc_u32 s43, s43, 0\n\t"
        "s_mov_b64 exec, %[vm]\n\tglobal_load_dwordx4 v[96:99], %[voff], s[42:43] offset:0 sc1\n\tglobal_load_dwordx4 v[112:115], %[voff], s[42:43] offset:512 sc1\n\ts_mov_b64 exec, -1\n\t"
        "v_exp_f32 v53, v53\n\tv_exp_f32 v54, v54\n\tv_exp_f32 v55, v55\n\tv_exp_f32 v56, v56\n\tv_exp_f32 v57, v57\n\ts_waitcnt vmcnt(15)\n\t"
        "v_smfmac_f32_32x32x32_bf16 %[o0], v[100:103], v[48:55], v128\n\tv_exp_f32 v58, v58\n\tv_exp_f32 v59, v59\n\tv_exp_f32 v60, v60\n\tv_exp_f32 v61, v61\n\tv_exp_f32 v62, v62\n\t"
        "v_exp_f32 v63, v63\n\tv_exp_f32 v64, v64\n\tv_exp_f32 v65, v65\n\tv_smfmac_f32_32x32x32_bf16 %[o0], v[116:119], v[56:63], v128\n\tv_exp_f32 v66, v66\n\tv_exp_f32 v67, v67\n\t"
        "v_exp_f32 v68, v68\n\tv_mfma_f32_32x32x16_bf16 v[48:63], v[88:91], %[q0], 0\n\tglobal_load_dwordx4 v[168:171], %[a0], s[46:47]\n\ts_add_u32 m0, %[ol], 3072\n\t"
        "s_add_u32 s50, s50, 8192\n\ts_addc_u32 s51, s51, 0\n\tglobal_load_lds_dwordx4 %[vw], s[50:51]\n\tv_exp_f32 v69, v69\n\tv_exp_f32 v70, v70\n\tv_exp_f32 v71, v71\n\t"
        "v_exp_f32 v72, v72\n\tv_exp_f32 v73, v73\n\tv_smfmac_f32_32x32x32_bf16 %[o1], v[100:103], v[64:71], v128\n\tv_exp_f32 v74, v74\n\tv_exp_f32 v75, v75\n\tv_exp_f32 v76, v76\n\t"
        "v_exp_f32 v77, v77\n\tv_exp_f32 v78, v78\n\tv_exp_f32 v79, v79\n\tv_exp_f32 v48, v48\n\tv_exp_f32 v49, v49\n\tv_smfmac_f32_32x32x32_bf16 %[o1], v[116:119], v[72:79], v128\n\t"
        "v_exp_f32 v50, v50\n\tv_exp_f32 v51, v51\n\tv_exp_f32 v52, v52\n\tv_mfma_f32_32x32x16_bf16 v[64:79], v[88:91], %[q1], 0\n\t"
        "global_load_dwordx4 v[88:91], %[koff], s[40:41] offset:2048 sc1\n\ts_mov_b64 exec, %[vm]\n\tglobal_load_dwordx4 v[100:103], %[voff], s[42:43] offset:1024 sc1\n\t"
        "global_load_dwordx4 v[116:119], %[voff], s[42:43] offset:1536 sc1\n\ts_mov_b64 exec, -1\n\tv_exp_f32 v53, v53\n\tv_exp_f32 v54, v54\n\tv_exp_f32 v55, v55\n\tv_exp_f32 v56, v56\n\t"
        "v_exp_f32 v57, v57\n\ts_waitcnt vmcnt(15)\n\tv_smfmac_f32_32x32x32_bf16 %[o0], v[104:107], v[48:55], v128\n\tv_exp_f32 v58, v58\n\tv_exp_f32 v59, v59\n\tv_exp_f32 v60, v60\n\t"
        "v_exp_f32 v61, v61\n\tv_exp_f32 v62, v62\n\tv_exp_f32 v63, v63\n\tv_exp_f32 v64, v64\n\tv_exp_f32 v65, v65\n\tv_smfmac_f32_32x32x32_bf16 %[o0], v[120:123], v[56:63], v128\n\t"
        "v_exp_f32 v66, v66\n\tv_exp_f32 v67, v67\n\tv_exp_f32 v68, v68\n\tv_mfma_f32_32x32x16_bf16 v[48:63], v[92:95], %[q0], 0\n\tglobal_load_dwordx4 v[172:175], %[a1], s[46:47]\n\t"
        "global_load_dwordx4 v[176:179], %[a2], s[46:47]\n\ts_add_u32 s46, s46, %[ga]\n\ts_addc_u32 s47, s47, 0\n\tv_exp_f32 v69, v69\n\tv_exp_f32 v70, v70\n\tv_exp_f32 v71, v71\n\t"
        "v_exp_f32 v72, v72\n\tv_exp_f32 v73, v73\n\tv_smfmac_f32_32x32x32_bf16 %[o1], v[104:107], v[64:71], v128\n\tv_exp_f32 v74, v74\n\tv_exp_f32 v75, v75\n\tv_exp_f32 v76, v76\n\t"
        "v_exp_f32 v77, v77\n\tv_exp_f32 v78, v78\n\tv_exp_f32 v79, v79\n\tv_exp_f32 v48, v48\n\tv_exp_f32 v49, v49\n\tv_smfmac_f32_32x32x32_bf16 %[o1], v[120:123], v[72:79], v128\n\t"
        "v_exp_f32 v50, v50\n\tv_exp_f32 v51, v51\n\tv_exp_f32 v52, v52\n\tv_mfma_f32_32x32x16_bf16 v[64:79], v[92:95], %[q1], 0\n\t"
        "global_load_dwordx4 v[92:95], %[koff], s[40:41] offset:3072 sc1\n\ts_mov_b64 exec, %[vm]\n\tglobal_load_dwordx4 v[104:107], %[voff], s[42:43] offset:2048 sc1\n\t"
        "global_load_dwordx4 v[120:123], %[voff], s[42:43] offset:2560 sc1\n\ts_mov_b64 exec, -1\n\tv_exp_f32 v53, v53\n\tv_exp_f32 v54, v54\n\tv_exp_f32 v55, v55\n\tv_exp_f32 v56, v56\n\t"
        "v_exp_f32 v57, v57\n\ts_waitcnt vmcnt(15)\n\tv_smfmac_f32_32x32x32_bf16 %[o0], v[108:111], v[48:55], v128\n\tv_exp_f32 v58, v58\n\tv_exp_f32 v59, v59\n\tv_exp_f32 v60, v60\n\t"
        "v_exp_f32 v61, v61\n\tv_exp_f32 v62, v62\n\tv_exp_f32 v63, v63\n\tv_exp_f32 v64, v64\n\tv_exp_f32 v65, v65\n\tv_smfmac_f32_32x32x32_bf16 %[o0], v[124:127], v[56:63], v128\n\t"
        "v_exp_f32 v66, v66\n\tv_exp_f32 v67, v67\n\tv_exp_f32 v68, v68\n\tv_mfma_f32_32x32x16_bf16 v[48:63], v[80:83], %[q0], 0\n\ts_add_u32 m0, %[ol], 4096\n\ts_add_u32 s50, s50, 8192\n\t"
        "s_addc_u32 s51, s51, 0\n\tglobal_load_lds_dwordx4 %[vw], s[50:51]\n\tglobal_load_dwordx4 v[180:183], %[a0], s[46:47]\n\tv_exp_f32 v69, v69\n\tv_exp_f32 v70, v70\n\t"
        "v_exp_f32 v71, v71\n\tv_exp_f32 v72, v72\n\tv_exp_f32 v73, v73\n\tv_smfmac_f32_32x32x32_bf16 %[o1], v[108:111], v[64:71], v128\n\tv_exp_f32 v74, v74\n\tv_exp_f32 v75, v75\n\t"
        "v_exp_f32 v76, v76\n\tv_exp_f32 v77, v77\n\tv_exp_f32 v78, v78\n\tv_exp_f32 v79, v79\n\tv_exp_f32 v48, v48\n\tv_exp_f32 v49, v49\n\t"
        "v_smfmac_f32_32x32x32_bf16 %[o1], v[124:127], v[72:79], v128\n\tv_exp_f32 v50, v50\n\tv_exp_f32 v51, v51\n\tv_exp_f32 v52, v52\n\t"
        "v_mfma_f32_32x32x16_bf16 v[64:79], v[80:83], %[q1], 0\n\ts_add_u32 s40, s40, 4096\n\ts_addc_u32 s41, s41, 0\n\tglobal_load_dwordx4 v[80:83], %[koff], s[40:41] offset:0 sc1\n\t"
        "s_mov_b64 exec, %[vm]\n\tglobal_load_dwordx4 v[108:111], %[voff], s[42:43] offset:3072 sc1\n\tglobal_load_dwordx4 v[124:127], %[voff], s[42:43] offset:3584 sc1\n\t"
        "s_mov_b64 exec, -1\n\tv_exp_f32 v53, v53\n\tv_exp_f32 v54, v54\n\tv_exp_f32 v55, v55\n\tv_exp_f32 v56, v56\n\tv_exp_f32 v57, v57\n\ts_waitcnt vmcnt(15)\n\t"
        "v_smfmac_f32_32x32x32_bf16 %[o0], v[96:99], v[48:55], v128\n\tv_exp_f32 v58, v58\n\tv_exp_f32 v59, v59\n\tv_exp_f32 v60, v60\n\tv_exp_f32 v61, v61\n\tv_exp_f32 v62, v62\n\t"
        "v_exp_f32 v63, v63\n\tv_exp_f32 v64, v64\n\tv_exp_f32 v65, v65\n\tv_smfmac_f32_32x32x32_bf16 %[o0], v[112:115], v[56:63], v128\n\tv_exp_f32 v66, v66\n\tv_exp_f32 v67, v67\n\t"
        "v_exp_f32 v68, v68\n\tv_mfma_f32_32x32x16_bf16 v[48:63], v[84:87], %[q0], 0\n\tglobal_load_dwordx4 v[184:187], %[a1], s[46:47]\n\ts_add_u32 m0, %[ol], 5120\n\t"
        "s_add_u32 s50, s50, 8192\n\ts_addc_u32 s51, s51, 0\n\tglobal_load_lds_dwordx4 %[vw], s[50:51]\n\tv_exp_f32 v69, v69\n\tv_exp_f32 v70, v70\n\tv_exp_f32 v71, v71\n\t"
        "v_exp_f32 v72, v72\n\tv_exp_f32 v73, v73\n\tv_smfmac_f32_32x32x32_bf16 %[o1], v[96:99], v[64:71], v128\n\tv_exp_f32 v74, v74\n\tv_exp_f32 v75, v75\n\tv_exp_f32 v76, v76\n\t"
        "v_exp_f32 v77, v77\n\tv_exp_f32 v78, v78\n\tv_exp_f32 v79, v79\n\tv_exp_f32 v48, v48\n\tv_exp_f32 v49, v49\n\tv_smfmac_f32_32x32x32_bf16 %[o1], v[112:115], v[72:79], v128\n\t"
        "v_exp_f32 v50, v50\n\tv_exp_f32 v51, v51\n\tv_exp_f32 v52, v52\n\tv_mfma_f32_32x32x16_bf16 v[64:79], v[84:87], %[q1], 0\n\t"
        "global_load_dwordx4 v[84:87], %[koff], s[40:41] offset:1024 sc1\n\ts_add_u32 s42, s42, 4096\n\ts_addc_u32 s43, s43, 0\n\ts_mov_b64 exec, %[vm]\n\t"
        "global_load_dwordx4 v[96:99], %[voff], s[42:43] offset:0 sc1\n\tglobal_load_dwordx4 v[112:115], %[voff], s[42:43] offset:512 sc1\n\ts_mov_b64 exec, -1\n\tv_exp_f32 v53, v53\n\t"
        "v_exp_f32 v54, v54\n\tv_exp_f32 v55, v55\n\tv_exp_f32 v56, v56\n\tv_exp_f32 v57, v57\n\ts_waitcnt vmcnt(15)\n\tv_smfmac_f32_32x32x32_bf16 %[o0], v[100:103], v[48:55], v128\n\t"
        "v_exp_f32 v58, v58\n\tv_exp_f32 v59, v59\n\tv_exp_f32 v60, v60\n\tv_exp_f32 v61, v61\n\tv_exp_f32 v62, v62\n\tv_exp_f32 v63, v63\n\tv_exp_f32 v64, v64\n\tv_exp_f32 v65, v65\n\t"
        "v_smfmac_f32_32x32x32_bf16 %[o0], v[116:119], v[56:63], v128\n\tv_exp_f32 v66, v66\n\tv_exp_f32 v67, v67\n\tv_exp_f32 v68, v68\n\t"
        "v_mfma_f32_32x32x16_bf16 v[48:63], v[88:91], %[q0], 0\n\tglobal_load_dwordx4 v[188:191], %[a2], s[46:47]\n\ts_add_u32 s46, s46, %[ga]\n\ts_addc_u32 s47, s47, 0\n\t"
        "global_load_dwordx4 v[192:195], %[vw], s[48:49]\n\ts_add_u32 s48, s48, 8192\n\ts_addc_u32 s49, s49, 0\n\tv_exp_f32 v69, v69\n\tv_exp_f32 v70, v70\n\tv_exp_f32 v71, v71\n\t"
        "v_exp_f32 v72, v72\n\tv_exp_f32 v73, v73\n\tv_smfmac_f32_32x32x32_bf16 %[o1], v[100:103], v[64:71], v128\n\tv_exp_f32 v74, v74\n\tv_exp_f32 v75, v75\n\tv_exp_f32 v76, v76\n\t"
        "v_exp_f32 v77, v77\n\tv_exp_f32 v78, v78\n\tv_exp_f32 v79, v79\n\tv_exp_f32 v48, v48\n\tv_exp_f32 v49, v49\n\tv_smfmac_f32_32x32x32_bf16 %[o1], v[116:119], v[72:79], v128\n\t"
        "v_exp_f32 v50, v50\n\tv_exp_f32 v51, v51\n\tv_exp_f32 v52, v52\n\tv_mfma_f32_32x32x16_bf16 v[64:79], v[88:91], %[q1], 0\n\t"
        "global_load_dwordx4 v[88:91], %[koff], s[40:41] offset:2048 sc1\n\ts_mov_b64 exec, %[vm]\n\tglobal_load_dwordx4 v[100:103], %[voff], s[42:43] offset:1024 sc1\n\t"
        "global_load_dwordx4 v[116:119], %[voff], s[42:43] offset:1536 sc1\n\ts_mov_b64 exec, -1\n\tv_exp_f32 v53, v53\n\tv_exp_f32 v54, v54\n\tv_exp_f32 v55, v55\n\tv_exp_f32 v56, v56\n\t"
        "v_exp_f32 v57, v57\n\ts_waitcnt vmcnt(15)\n\tv_smfmac_f32_32x32x32_bf16 %[o0], v[104:107], v[48:55], v128\n\tv_exp_f32 v58, v58\n\tv_exp_f32 v59, v59\n\tv_exp_f32 v60, v60\n\t"
        "v_exp_f32 v61, v61\n\tv_exp_f32 v62, v62\n\tv_exp_f32 v63, v63\n\tv_exp_f32 v64, v64\n\tv_exp_f32 v65, v65\n\tv_smfmac_f32_32x32x32_bf16 %[o0], v[120:123], v[56:63], v128\n\t"
        "v_exp_f32 v66, v66\n\tv_exp_f32 v67, v67\n\tv_exp_f32 v68, v68\n\tv_mfma_f32_32x32x16_bf16 v[48:63], v[92:95], %[q0], 0\n\ts_add_u32 m0, %[ol], 6144\n\ts_add_u32 s50, s50, 8192\n\t"
        "s_addc_u32 s51, s51, 0\n\tglobal_load_lds_dwordx4 %[vw], s[50:51]\n\tglobal_load_dwordx4 v[196:199], %[vw], s[48:49]\n\ts_add_u32 s48, s48, 8192\n\ts_addc_u32 s49, s49, 0\n\t"
        "v_exp_f32 v69, v69\n\tv_exp_f32 v70, v70\n\tv_exp_f32 v71, v71\n\tv_exp_f32 v72, v72\n\tv_exp_f32 v73, v73\n\tv_smfmac_f32_32x32x32_bf16 %[o1], v[104:107], v[64:71], v128\n\t"
        "v_exp_f32 v74, v74\n\tv_exp_f32 v75, v75\n\tv_exp_f32 v76, v76\n\tv_exp_f32 v77, v77\n\tv_exp_f32 v78, v78\n\tv_exp_f32 v79, v79\n\tv_exp_f32 v48, v48\n\tv_exp_f32 v49, v49\n\t"
        "v_smfmac_f32_32x32x32_bf16 %[o1], v[120:123], v[72:79], v128\n\tv_exp_f32 v50, v50\n\tv_exp_f32 v51, v51\n\tv_exp_f32 v52, v52\n\t"
        "v_mfma_f32_32x32x16_bf16 v[64:79], v[92:95], %[q1], 0\n\tglobal_load_dwordx4 v[92:95], %[koff], s[40:41] offset:3072 sc1\n\ts_mov_b64 exec, %[vm]\n\t"
        "global_load_dwordx4 v[104:107], %[voff], s[42:43] offset:2048 sc1\n\tglobal_load_dwordx4 v[120:123], %[voff], s[42:43] offset:2560 sc1\n\ts_mov_b64 exec, -1\n\t"
        "v_exp_f32 v53, v53\n\tv_exp_f32 v54, v54\n\tv_exp_f32 v55, v55\n\tv_exp_f32 v56, v56\n\tv_exp_f32 v57, v57\n\ts_waitcnt vmcnt(15)\n\t"
        "v_smfmac_f32_32x32x32_bf16 %[o0], v[108:111], v[48:55], v128\n\tv_exp_f32 v58, v58\n\tv_exp_f32 v59, v59\n\tv_exp_f32 v60, v60\n\tv_exp_f32 v61, v61\n\tv_exp_f32 v62, v62\n\t"
        "v_exp_f32 v63, v63\n\tv_exp_f32 v64, v64\n\tv_exp_f32 v65, v65\n\tv_smfmac_f32_32x32x32_bf16 %[o0], v[124:127], v[56:63], v128\n\tv_exp_f32 v66, v66\n\tv_exp_f32 v67, v67\n\t"
        "v_exp_f32 v68, v68\n\tv_mfma_f32_32x32x16_bf16 v[48:63], v[80:83], %[q0], 0\n\tglobal_load_dwordx4 v[200:203], %[vw], s[48:49]\n\ts_add_u32 s48, s48, 8192\n\t"
        "s_addc_u32 s49, s49, 0\n\tv_exp_f32 v69, v69\n\tv_exp_f32 v70, v70\n\tv_exp_f32 v71, v71\n\tv_exp_f32 v72, v72\n\tv_exp_f32 v73, v73\n\t"
        "v_smfmac_f32_32x32x32_bf16 %[o1], v[108:111], v[64:71], v128\n\tv_exp_f32 v74, v74\n\tv_exp_f32 v75, v75\n\tv_exp_f32 v76, v76\n\tv_exp_f32 v77, v77\n\tv_exp_f32 v78, v78\n\t"
        "v_exp_f32 v79, v79\n\tv_exp_f32 v48, v48\n\tv_exp_f32 v49, v49\n\tv_smfmac_f32_32x32x32_bf16 %[o1], v[124:127], v[72:79], v128\n\tv_exp_f32 v50, v50\n\tv_exp_f32 v51, v51\n\t"
        "v_exp_f32 v52, v52\n\tv_mfma_f32_32x32x16_bf16 v[64:79], v[80:83], %[q1], 0\n\ts_add_u32 s40, s40, 4096\n\ts_addc_u32 s41, s41, 0\n\t"
        "global_load_dwordx4 v[80:83], %[koff], s[40:41] offset:0 sc1\n\ts_mov_b64 exec, %[vm]\n\tglobal_load_dwordx4 v[108:111], %[voff], s[42:43] offset:3072 sc1\n\t"
        "global_load_dwordx4 v[124:127], %[voff], s[42:43] offset:3584 sc1\n\ts_mov_b64 exec, -1\n\tv_exp_f32 v53, v53\n\tv_exp_f32 v54, v54\n\tv_exp_f32 v55, v55\n\tv_exp_f32 v56, v56\n\t"
        "v_exp_f32 v57, v57\n\ts_waitcnt vmcnt(14)\n\tv_smfmac_f32_32x32x32_bf16 %[o0], v[96:99], v[48:55], v128\n\tv_exp_f32 v58, v58\n\tv_exp_f32 v59, v59\n\tv_exp_f32 v60, v60\n\t"
        "v_exp_f32 v61, v61\n\tv_exp_f32 v62, v62\n\tv_exp_f32 v63, v63\n\tv_exp_f32 v64, v64\n\tv_exp_f32 v65, v65\n\tv_smfmac_f32_32x32x32_bf16 %[o0], v[112:115], v[56:63], v128\n\t"
        "v_exp_f32 v66, v66\n\tv_exp_f32 v67, v67\n\tv_exp_f32 v68, v68\n\tv_mfma_f32_32x32x16_bf16 v[48:63], v[84:87], %[q0], 0\n\ts_add_u32 m0, %[ol], 7168\n\ts_add_u32 s50, s50, 8192\n\t"
        "s_addc_u32 s51, s51, 0\n\tglobal_load_lds_dwordx4 %[vw], s[50:51]\n\tglobal_load_dwordx4 v[204:207], %[vw], s[48:49]\n\ts_add_u32 s48, s48, 8192\n\ts_addc_u32 s49, s49, 0\n\t"
        "v_exp_f32 v69, v69\n\tv_exp_f32 v70, v70\n\tv_exp_f32 v71, v71\n\tv_exp_f32 v72, v72\n\tv_exp_f32 v73, v73\n\tv_smfmac_f32_32x32x32_bf16 %[o1], v[96:99], v[64:71], v128\n\t"
        "v_exp_f32 v74, v74\n\tv_exp_f32 v75, v75\n\tv_exp_f32 v76, v76\n\tv_exp_f32 v77, v77\n\tv_exp_f32 v78, v78\n\tv_exp_f32 v79, v79\n\tv_exp_f32 v48, v48\n\tv_exp_f32 v49, v49\n\t"
        "v_smfmac_f32_32x32x32_bf16 %[o1], v[112:115], v[72:79], v128\n\tv_exp_f32 v50, v50\n\tv_exp_f32 v51, v51\n\tv_exp_f32 v52, v52\n\t"
        "v_mfma_f32_32x32x16_bf16 v[64:79], v[84:87], %[q1], 0\n\tglobal_load_dwordx4 v[84:87], %[koff], s[40:41] offset:1024 sc1\n\ts_add_u32 s42, s42, 4096\n\ts_addc_u32 s43, s43, 0\n\t"
        "s_mov_b64 exec, %[vm]\n\tglobal_load_dwordx4 v[96:99], %[voff], s[42:43] offset:0 sc1\n\tglobal_load_dwordx4 v[112:115], %[voff], s[42:43] offset:512 sc1\n\ts_mov_b64 exec, -1\n\t"
        "v_exp_f32 v53, v53\n\tv_exp_f32 v54, v54\n\tv_exp_f32 v55, v55\n\tv_exp_f32 v56, v56\n\tv_exp_f32 v57, v57\n\ts_waitcnt vmcnt(14)\n\t"
        "v_smfmac_f32_32x32x32_bf16 %[o0], v[100:103], v[48:55], v128\n\tv_exp_f32 v58, v58\n\tv_exp_f32 v59, v59\n\tv_exp_f32 v60, v60\n\tv_exp_f32 v61, v61\n\tv_exp_f32 v62, v62\n\t"
        "v_exp_f32 v63, v63\n\tv_exp_f32 v64, v64\n\tv_exp_f32 v65, v65\n\tv_smfmac_f32_32x32x32_bf16 %[o0], v[116:119], v[56:63], v128\n\tv_exp_f32 v66, v66\n\tv_exp_f32 v67, v67\n\t"
        "v_exp_f32 v68, v68\n\tv_mfma_f32_32x32x16_bf16 v[48:63], v[88:91], %[q0], 0\n\tglobal_load_dwordx4 v[208:211], %[vw], s[48:49]\n\ts_add_u32 s48, s48, 8192\n\t"
        "s_addc_u32 s49, s49, 0\n\ts_add_u32 m0, %[hl], 0\n\ts_nop 0\n\tglobal_load_lds_dwordx4 %[b0], s[52:53]\n\tv_exp_f32 v69, v69\n\tv_exp_f32 v70, v70\n\tv_exp_f32 v71, v71\n\t"
        "v_exp_f32 v72, v72\n\tv_exp_f32 v73, v73\n\tv_smfmac_f32_32x32x32_bf16 %[o1], v[100:103], v[64:71], v128\n\tv_exp_f32 v74, v74\n\tv_exp_f32 v75, v75\n\tv_exp_f32 v76, v76\n\t"
        "v_exp_f32 v77, v77\n\tv_exp_f32 v78, v78\n\tv_exp_f32 v79, v79\n\tv_exp_f32 v48, v48\n\tv_exp_f32 v49, v49\n\tv_smfmac_f32_32x32x32_bf16 %[o1], v[116:119], v[72:79], v128\n\t"
        "v_exp_f32 v50, v50\n\tv_exp_f32 v51, v51\n\tv_exp_f32 v52, v52\n\tv_mfma_f32_32x32x16_bf16 v[64:79], v[88:91], %[q1], 0\n\t"
        "global_load_dwordx4 v[88:91], %[koff], s[40:41] offset:2048 sc1\n\ts_mov_b64 exec, %[vm]\n\tglobal_load_dwordx4 v[100:103], %[voff], s[42:43] offset:1024 sc1\n\t"
        "global_load_dwordx4 v[116:119], %[voff], s[42:43] offset:1536 sc1\n\ts_mov_b64 exec, -1\n\tv_exp_f32 v53, v53\n\tv_exp_f32 v54, v54\n\tv_exp_f32 v55, v55\n\tv_exp_f32 v56, v56\n\t"
        "v_exp_f32 v57, v57\n\ts_waitcnt vmcnt(14)\n\tv_smfmac_f32_32x32x32_bf16 %[o0], v[104:107], v[48:55], v128\n\tv_exp_f32 v58, v58\n\tv_exp_f32 v59, v59\n\tv_exp_f32 v60, v60\n\t"
        "v_exp_f32 v61, v61\n\tv_exp_f32 v62, v62\n\tv_exp_f32 v63, v63\n\tv_exp_f32 v64, v64\n\tv_exp_f32 v65, v65\n\tv_smfmac_f32_32x32x32_bf16 %[o0], v[120:123], v[56:63], v128\n\t"
        "v_exp_f32 v66, v66\n\tv_exp_f32 v67, v67\n\tv_exp_f32 v68, v68\n\tv_mfma_f32_32x32x16_bf16 v[48:63], v[92:95], %[q0], 0\n\tglobal_load_dwordx4 v[212:215], %[vw], s[48:49]\n\t"
        "s_add_u32 s48, s48, 8192\n\ts_addc_u32 s49, s49, 0\n\tglobal_load_dwordx4 v[216:219], %[vw], s[48:49]\n\ts_add_u32 s48, s48, 8192\n\ts_addc_u32 s49, s49, 0\n\t"
        "v_exp_f32 v69, v69\n\tv_exp_f32 v70, v70\n\tv_exp_f32 v71, v71\n\tv_exp_f32 v72, v72\n\tv_exp_f32 v73, v73\n\tv_smfmac_f32_32x32x32_bf16 %[o1], v[104:107], v[64:71], v128\n\t"
        "v_exp_f32 v74, v74\n\tv_exp_f32 v75, v75\n\tv_exp_f32 v76, v76\n\tv_exp_f32 v77, v77\n\tv_exp_f32 v78, v78\n\tv_exp_f32 v79, v79\n\tv_exp_f32 v48, v48\n\tv_exp_f32 v49, v49\n\t"
        "v_smfmac_f32_32x32x32_bf16 %[o1], v[120:123], v[72:79], v128\n\tv_exp_f32 v50, v50\n\tv_exp_f32 v51, v51\n\tv_exp_f32 v52, v52\n\t"
        "v_mfma_f32_32x32x16_bf16 v[64:79], v[92:95], %[q1], 0\n\tglobal_load_dwordx4 v[92:95], %[koff], s[40:41] offset:3072 sc1\n\ts_mov_b64 exec, %[vm]\n\t"
        "global_load_dwordx4 v[104:107], %[voff], s[42:43] offset:2048 sc1\n\tglobal_load_dwordx4 v[120:123], %[voff], s[42:43] offset:2560 sc1\n\ts_mov_b64 exec, -1\n\t"
        "v_exp_f32 v53, v53\n\tv_exp_f32 v54, v54\n\tv_exp_f32 v55, v55\n\tv_exp_f32 v56, v56\n\tv_exp_f32 v57, v57\n\ts_waitcnt vmcnt(15)\n\t"
        "v_smfmac_f32_32x32x32_bf16 %[o0], v[108:111], v[48:55], v128\n\tv_exp_f32 v58, v58\n\tv_exp_f32 v59, v59\n\tv_exp_f32 v60, v60\n\tv_exp_f32 v61, v61\n\tv_exp_f32 v62, v62\n\t"
        "v_exp_f32 v63, v63\n\tv_exp_f32 v64, v64\n\tv_exp_f32 v65, v65\n\tv_smfmac_f32_32x32x32_bf16 %[o0], v[124:127], v[56:63], v128\n\tv_exp_f32 v66, v66\n\tv_exp_f32 v67, v67\n\t"
        "v_exp_f32 v68, v68\n\tv_mfma_f32_32x32x16_bf16 v[48:63], v[80:83], %[q0], 0\n\ts_add_u32 m0, %[hl], 1024\n\ts_nop 0\n\tglobal_load_lds_dwordx4 %[b1], s[52:53]\n\t"
        "global_load_dwordx4 v[220:223], %[vw], s[48:49]\n\ts_add_u32 s48, s48, 8192\n\ts_addc_u32 s49, s49, 0\n\tv_exp_f32 v69, v69\n\tv_exp_f32 v70, v70\n\tv_exp_f32 v71, v71\n\t"
        "v_exp_f32 v72, v72\n\tv_exp_f32 v73, v73\n\tv_smfmac_f32_32x32x32_bf16 %[o1], v[108:111], v[64:71], v128\n\tv_exp_f32 v74, v74\n\tv_exp_f32 v75, v75\n\tv_exp_f32 v76, v76\n\t"
        "v_exp_f32 v77, v77\n\tv_exp_f32 v78, v78\n\tv_exp_f32 v79, v79\n\tv_exp_f32 v48, v48\n\tv_exp_f32 v49, v49\n\tv_smfmac_f32_32x32x32_bf16 %[o1], v[124:127], v[72:79], v128\n\t"
        "v_exp_f32 v50, v50\n\tv_exp_f32 v51, v51\n\tv_exp_f32 v52, v52\n\tv_mfma_f32_32x32x16_bf16 v[64:79], v[80:83], %[q1], 0\n\ts_add_u32 s40, s40, 4096\n\ts_addc_u32 s41, s41, 0\n\t"
        "global_load_dwordx4 v[80:83], %[koff], s[40:41] offset:0 sc1\n\ts_mov_b64 exec, %[vm]\n\tglobal_load_dwordx4 v[108:111], %[voff], s[42:43] offset:3072 sc1\n\t"
        "global_load_dwordx4 v[124:127], %[voff], s[42:43] offset:3584 sc1\n\ts_mov_b64 exec, -1\n\tv_exp_f32 v53, v53\n\tv_exp_f32 v54, v54\n\tv_exp_f32 v55, v55\n\tv_exp_f32 v56, v56\n\t"
        "v_exp_f32 v57, v57\n\ts_waitcnt vmcnt(15)\n\tv_smfmac_f32_32x32x32_bf16 %[o0], v[96:99], v[48:55], v128\n\tv_exp_f32 v58, v58\n\tv_exp_f32 v59, v59\n\tv_exp_f32 v60, v60\n\t"
        "v_exp_f32 v61, v61\n\tv_exp_f32 v62, v62\n\tv_exp_f32 v63, v63\n\tv_exp_f32 v64, v64\n\tv_exp_f32 v65, v65\n\tv_smfmac_f32_32x32x32_bf16 %[o0], v[112:115], v[56:63], v128\n\t"
        "v_exp_f32 v66, v66\n\tv_exp_f32 v67, v67\n\tv_exp_f32 v68, v68\n\tv_mfma_f32_32x32x16_bf16 v[48:63], v[84:87], %[q0], 0\n\tglobal_load_dwordx4 v[224:227], %[vw], s[48:49]\n\t"
        "s_add_u32 s48, s48, 8192\n\ts_addc_u32 s49, s49, 0\n\ts_add_u32 m0, %[hl], 2048\n\ts_nop 0\n\tglobal_load_lds_dwordx4 %[b2], s[52:53]\n\tv_exp_f32 v69, v69\n\t"
        "v_exp_f32 v70, v70\n\tv_exp_f32 v71, v71\n\tv_exp_f32 v72, v72\n\tv_exp_f32 v73, v73\n\tv_smfmac_f32_32x32x32_bf16 %[o1], v[96:99], v[64:71], v128\n\tv_exp_f32 v74, v74\n\t"
        "v_exp_f32 v75, v75\n\tv_exp_f32 v76, v76\n\tv_exp_f32 v77, v77\n\tv_exp_f32 v78, v78\n\tv_exp_f32 v79, v79\n\tv_exp_f32 v48, v48\n\tv_exp_f32 v49, v49\n\t"
        "v_smfmac_f32_32x32x32_bf16 %[o1], v[112:115], v[72:79], v128\n\tv_exp_f32 v50, v50\n\tv_exp_f32 v51, v51\n\tv_exp_f32 v52, v52\n\t"
        "v_mfma_f32_32x32x16_bf16 v[64:79], v[84:87], %[q1], 0\n\tglobal_load_dwordx4 v[84:87], %[koff], s[40:41] offset:1024 sc1\n\ts_add_u32 s42, s42, 4096\n\ts_addc_u32 s43, s43, 0\n\t"
        "s_mov_b64 exec, %[vm]\n\tglobal_load_dwordx4 v[96:99], %[voff], s[42:43] offset:0 sc1\n\tglobal_load_dwordx4 v[112:115], %[voff], s[42:43] offset:512 sc1\n\ts_mov_b64 exec, -1\n\t"
        "v_exp_f32 v53, v53\n\tv_exp_f32 v54, v54\n\tv_exp_f32 v55, v55\n\tv_exp_f32 v56, v56\n\tv_exp_f32 v57, v57\n\ts_waitcnt vmcnt(15)\n\t"
        "v_smfmac_f32_32x32x32_bf16 %[o0], v[100:103], v[48:55], v128\n\tv_exp_f32 v58, v58\n\tv_exp_f32 v59, v59\n\tv_exp_f32 v60, v60\n\tv_exp_f32 v61, v61\n\tv_exp_f32 v62, v62\n\t"
        "v_exp_f32 v63, v63\n\tv_exp_f32 v64, v64\n\tv_exp_f32 v65, v65\n\tv_smfmac_f32_32x32x32_bf16 %[o0], v[116:119], v[56:63], v128\n\tv_exp_f32 v66, v66\n\tv_exp_f32 v67, v67\n\t"
        "v_exp_f32 v68, v68\n\tv_mfma_f32_32x32x16_bf16 v[48:63], v[88:91], %[q0], 0\n\tglobal_load_dwordx4 v[228:231], %[vw], s[48:49]\n\ts_add_u32 s48, s48, 8192\n\t"
        "s_addc_u32 s49, s49, 0\n\tglobal_load_dwordx4 v[232:235], %[vw], s[48:49]\n\ts_add_u32 s48, s48, 8192\n\ts_addc_u32 s49, s49, 0\n\tv_exp_f32 v69, v69\n\tv_exp_f32 v70, v70\n\t"
        "v_exp_f32 v71, v71\n\tv_exp_f32 v72, v72\n\tv_exp_f32 v73, v73\n\tv_smfmac_f32_32x32x32_bf16 %[o1], v[100:103], v[64:71], v128\n\tv_exp_f32 v74, v74\n\tv_exp_f32 v75, v75\n\t"
        "v_exp_f32 v76, v76\n\tv_exp_f32 v77, v77\n\tv_exp_f32 v78, v78\n\tv_exp_f32 v79, v79\n\tv_exp_f32 v48, v48\n\tv_exp_f32 v49, v49\n\t"
        "v_smfmac_f32_32x32x32_bf16 %[o1], v[116:119], v[72:79], v128\n\tv_exp_f32 v50, v50\n\tv_exp_f32 v51, v51\n\tv_exp_f32 v52, v52\n\t"
        "v_mfma_f32_32x32x16_bf16 v[64:79], v[88:91], %[q1], 0\n\tglobal_load_dwordx4 v[88:91], %[koff], s[40:41] offset:2048 sc1\n\ts_mov_b64 exec, %[vm]\n\t"
        "global_load_dwordx4 v[100:103], %[voff], s[42:43] offset:1024 sc1\n\tglobal_load_dwordx4 v[116:119], %[voff], s[42:43] offset:1536 sc1\n\ts_mov_b64 exec, -1\n\t"
        "v_exp_f32 v53, v53\n\tv_exp_f32 v54, v54\n\tv_exp_f32 v55, v55\n\tv_exp_f32 v56, v56\n\tv_exp_f32 v57, v57\n\ts_waitcnt vmcnt(15)\n\t"
        "v_smfmac_f32_32x32x32_bf16 %[o0], v[104:107], v[48:55], v128\n\tv_exp_f32 v58, v58\n\tv_exp_f32 v59, v59\n\tv_exp_f32 v60, v60\n\tv_exp_f32 v61, v61\n\tv_exp_f32 v62, v62\n\t"
        "v_exp_f32 v63, v63\n\tv_exp_f32 v64, v64\n\tv_exp_f32 v65, v65\n\tv_smfmac_f32_32x32x32_bf16 %[o0], v[120:123], v[56:63], v128\n\tv_exp_f32 v66, v66\n\tv_exp_f32 v67, v67\n\t"
        "v_exp_f32 v68, v68\n\tv_mfma_f32_32x32x16_bf16 v[48:63], v[92:95], %[q0], 0\n\ts_add_u32 m0, %[hl], 3072\n\ts_add_u32 s52, s52, 44032\n\ts_addc_u32 s53, s53, 0\n\t"
        "global_load_lds_dwordx4 %[b0], s[52:53]\n\tglobal_load_dwordx4 v[236:239], %[vw], s[48:49]\n\ts_add_u32 s48, s48, 8192\n\ts_addc_u32 s49, s49, 0\n\ts_cmp_eq_u32 %[drop], 0\n\t"
        "s_cbranch_scc1 2f\n\ts_setprio 0\n\t2:\n\tv_exp_f32 v69, v69\n\tv_exp_f32 v70, v70\n\tv_exp_f32 v71, v71\n\tv_exp_f32 v72, v72\n\tv_exp_f32 v73, v73\n\t"
        "v_smfmac_f32_32x32x32_bf16 %[o1], v[104:107], v[64:71], v128\n\tv_exp_f32 v74, v74\n\tv_exp_f32 v75, v75\n\tv_exp_f32 v76, v76\n\tv_exp_f32 v77, v77\n\tv_exp_f32 v78, v78\n\t"
        "v_exp_f32 v79, v79\n\tv_exp_f32 v48, v48\n\tv_exp_f32 v49, v49\n\tv_smfmac_f32_32x32x32_bf16 %[o1], v[120:123], v[72:79], v128\n\tv_exp_f32 v50, v50\n\tv_exp_f32 v51, v51\n\t"
        "v_exp_f32 v52, v52\n\tv_mfma_f32_32x32x16_bf16 v[64:79], v[92:95], %[q1], 0\n\tglobal_load_dwordx4 v[92:95], %[koff], s[40:41] offset:3072 sc1\n\ts_mov_b64 exec, %[vm]\n\t"
        "global_load_dwordx4 v[104:107], %[voff], s[42:43] offset:2048 sc1\n\tglobal_load_dwordx4 v[120:123], %[voff], s[42:43] offset:2560 sc1\n\ts_mov_b64 exec, -1\n\t"
        "v_exp_f32 v53, v53\n\tv_exp_f32 v54, v54\n\tv_exp_f32 v55, v55\n\tv_exp_f32 v56, v56\n\tv_exp_f32 v57, v57\n\ts_waitcnt vmcnt(15)\n\t"
        "v_smfmac_f32_32x32x32_bf16 %[o0], v[108:111], v[48:55], v128\n\tv_exp_f32 v58, v58\n\tv_exp_f32 v59, v59\n\tv_exp_f32 v60, v60\n\tv_exp_f32 v61, v61\n\tv_exp_f32 v62, v62\n\t"
        "v_exp_f32 v63, v63\n\tv_exp_f32 v64, v64\n\tv_exp_f32 v65, v65\n\tv_smfmac_f32_32x32x32_bf16 %[o0], v[124:127], v[56:63], v128\n\tv_exp_f32 v66, v66\n\tv_exp_f32 v67, v67\n\t"
        "v_exp_f32 v68, v68\n\tv_mfma_f32_32x32x16_bf16 v[48:63], v[80:83], %[q0], 0\n\tglobal_load_dwordx4 v[240:243], %[vw], s[48:49]\n\ts_add_u32 s48, s48, 8192\n\t"
        "s_addc_u32 s49, s49, 0\n\ts_add_u32 m0, %[hl], 4096\n\ts_nop 0\n\tglobal_load_lds_dwordx4 %[b1], s[52:53]\n\tv_exp_f32 v69, v69\n\tv_exp_f32 v70, v70\n\tv_exp_f32 v71, v71\n\t"
        "v_exp_f32 v72, v72\n\tv_exp_f32 v73, v73\n\tv_smfmac_f32_32x32x32_bf16 %[o1], v[108:111], v[64:71], v128\n\tv_exp_f32 v74, v74\n\tv_exp_f32 v75, v75\n\tv_exp_f32 v76, v76\n\t"
        "v_exp_f32 v77, v77\n\tv_exp_f32 v78, v78\n\tv_exp_f32 v79, v79\n\tv_exp_f32 v48, v48\n\tv_exp_f32 v49, v49\n\tv_smfmac_f32_32x32x32_bf16 %[o1], v[124:127], v[72:79], v128\n\t"
        "v_exp_f32 v50, v50\n\tv_exp_f32 v51, v51\n\tv_exp_f32 v52, v52\n\tv_mfma_f32_32x32x16_bf16 v[64:79], v[80:83], %[q1], 0\n\ts_mov_b64 exec, %[vm]\n\t"
        "global_load_dwordx4 v[108:111], %[voff], s[42:43] offset:3072 sc1\n\tglobal_load_dwordx4 v[124:127], %[voff], s[42:43] offset:3584 sc1\n\ts_mov_b64 exec, -1\n\t"
        "v_exp_f32 v53, v53\n\tv_exp_f32 v54, v54\n\tv_exp_f32 v55, v55\n\tv_exp_f32 v56, v56\n\tv_exp_f32 v57, v57\n\ts_waitcnt vmcnt(14)\n\t"
        "v_smfmac_f32_32x32x32_bf16 %[o0], v[96:99], v[48:55], v128\n\tv_exp_f32 v58, v58\n\tv_exp_f32 v59, v59\n\tv_exp_f32 v60, v60\n\tv_exp_f32 v61, v61\n\tv_exp_f32 v62, v62\n\t"
        "v_exp_f32 v63, v63\n\tv_exp_f32 v64, v64\n\tv_exp_f32 v65, v65\n\tv_smfmac_f32_32x32x32_bf16 %[o0], v[112:115], v[56:63], v128\n\tv_exp_f32 v66, v66\n\tv_exp_f32 v67, v67\n\t"
        "v_exp_f32 v68, v68\n\tv_mfma_f32_32x32x16_bf16 v[48:63], v[84:87], %[q0], 0\n\tglobal_load_dwordx4 v[244:247], %[vw], s[48:49]\n\ts_add_u32 s48, s48, 8192\n\t"
        "s_addc_u32 s49, s49, 0\n\tglobal_load_dwordx4 v[248:251], %[vw], s[48:49]\n\ts_add_u32 s48, s48, 8192\n\ts_addc_u32 s49, s49, 0\n\tv_exp_f32 v69, v69\n\tv_exp_f32 v70, v70\n\t"
        "v_exp_f32 v71, v71\n\tv_exp_f32 v72, v72\n\tv_exp_f32 v73, v73\n\tv_smfmac_f32_32x32x32_bf16 %[o1], v[96:99], v[64:71], v128\n\tv_exp_f32 v74, v74\n\tv_exp_f32 v75, v75\n\t"
        "v_exp_f32 v76, v76\n\tv_exp_f32 v77, v77\n\tv_exp_f32 v78, v78\n\tv_exp_f32 v79, v79\n\tv_exp_f32 v48, v48\n\tv_exp_f32 v49, v49\n\t"
        "v_smfmac_f32_32x32x32_bf16 %[o1], v[112:115], v[72:79], v128\n\tv_exp_f32 v50, v50\n\tv_exp_f32 v51, v51\n\tv_exp_f32 v52, v52\n\t"
        "v_mfma_f32_32x32x16_bf16 v[64:79], v[84:87], %[q1], 0\n\tv_exp_f32 v53, v53\n\tv_exp_f32 v54, v54\n\tv_exp_f32 v55, v55\n\tv_exp_f32 v56, v56\n\tv_exp_f32 v57, v57\n\t"
        "s_waitcnt vmcnt(11)\n\tv_smfmac_f32_32x32x32_bf16 %[o0], v[100:103], v[48:55], v128\n\tv_exp_f32 v58, v58\n\tv_exp_f32 v59, v59\n\tv_exp_f32 v60, v60\n\tv_exp_f32 v61, v61\n\t"
        "v_exp_f32 v62, v62\n\tv_exp_f32 v63, v63\n\tv_exp_f32 v64, v64\n\tv_exp_f32 v65, v65\n\tv_smfmac_f32_32x32x32_bf16 %[o0], v[116:119], v[56:63], v128\n\tv_exp_f32 v66, v66\n\t"
        "v_exp_f32 v67, v67\n\tv_exp_f32 v68, v68\n\tv_mfma_f32_32x32x16_bf16 v[48:63], v[88:91], %[q0], 0\n\ts_add_u32 m0, %[hl], 5120\n\ts_nop 0\n\t"
        "global_load_lds_dwordx4 %[b2], s[52:53]\n\tglobal_load_dwordx4 v[252:255], %[vw], s[48:49]\n\ts_add_u32 s48, s48, 8192\n\ts_addc_u32 s49, s49, 0\n\tv_exp_f32 v69, v69\n\t"
        "v_exp_f32 v70, v70\n\tv_exp_f32 v71, v71\n\tv_exp_f32 v72, v72\n\tv_exp_f32 v73, v73\n\tv_smfmac_f32_32x32x32_bf16 %[o1], v[100:103], v[64:71], v128\n\tv_exp_f32 v74, v74\n\t"
        "v_exp_f32 v75, v75\n\tv_exp_f32 v76, v76\n\tv_exp_f32 v77, v77\n\tv_exp_f32 v78, v78\n\tv_exp_f32 v79, v79\n\tv_exp_f32 v48, v48\n\tv_exp_f32 v49, v49\n\t"
        "v_smfmac_f32_32x32x32_bf16 %[o1], v[116:119], v[72:79], v128\n\tv_exp_f32 v50, v50\n\tv_exp_f32 v51, v51\n\tv_exp_f32 v52, v52\n\t"
        "v_mfma_f32_32x32x16_bf16 v[64:79], v[88:91], %[q1], 0\n\tv_exp_f32 v53, v53\n\tv_exp_f32 v54, v54\n\tv_exp_f32 v55, v55\n\tv_exp_f32 v56, v56\n\tv_exp_f32 v57, v57\n\t"
        "s_waitcnt vmcnt(8)\n\tv_smfmac_f32_32x32x32_bf16 %[o0], v[104:107], v[48:55], v128\n\tv_exp_f32 v58, v58\n\tv_exp_f32 v59, v59\n\tv_exp_f32 v60, v60\n\tv_exp_f32 v61, v61\n\t"
        "v_exp_f32 v62, v62\n\tv_exp_f32 v63, v63\n\tv_exp_f32 v64, v64\n\tv_exp_f32 v65, v65\n\tv_smfmac_f32_32x32x32_bf16 %[o0], v[120:123], v[56:63], v128\n\tv_exp_f32 v66, v66\n\t"
        "v_exp_f32 v67, v67\n\tv_exp_f32 v68, v68\n\tv_mfma_f32_32x32x16_bf16 v[48:63], v[92:95], %[q0], 0\n\tv_exp_f32 v69, v69\n\tv_exp_f32 v70, v70\n\tv_exp_f32 v71, v71\n\t"
        "v_exp_f32 v72, v72\n\tv_exp_f32 v73, v73\n\tv_smfmac_f32_32x32x32_bf16 %[o1], v[104:107], v[64:71], v128\n\tv_exp_f32 v74, v74\n\tv_exp_f32 v75, v75\n\tv_exp_f32 v76, v76\n\t"
        "v_exp_f32 v77, v77\n\tv_exp_f32 v78, v78\n\tv_exp_f32 v79, v79\n\tv_exp_f32 v48, v48\n\tv_exp_f32 v49, v49\n\tv_smfmac_f32_32x32x32_bf16 %[o1], v[120:123], v[72:79], v128\n\t"
        "v_exp_f32 v50, v50\n\tv_exp_f32 v51, v51\n\tv_exp_f32 v52, v52\n\tv_mfma_f32_32x32x16_bf16 v[64:79], v[92:95], %[q1], 0\n\tv_exp_f32 v53, v53\n\tv_exp_f32 v54, v54\n\t"
        "v_exp_f32 v55, v55\n\tv_exp_f32 v56, v56\n\tv_exp_f32 v57, v57\n\ts_waitcnt vmcnt(4)\n\tv_smfmac_f32_32x32x32_bf16 %[o0], v[108:111], v[48:55], v128\n\tv_exp_f32 v58, v58\n\t"
        "v_exp_f32 v59, v59\n\tv_exp_f32 v60, v60\n\tv_exp_f32 v61, v61\n\tv_exp_f32 v62, v62\n\tv_exp_f32 v63, v63\n\tv_exp_f32 v64, v64\n\tv_exp_f32 v65, v65\n\t"
        "v_smfmac_f32_32x32x32_bf16 %[o0], v[124:127], v[56:63], v128\n\tv_exp_f32 v66, v66\n\tv_exp_f32 v67, v67\n\tv_exp_f32 v68, v68\n\tv_exp_f32 v69, v69\n\tv_exp_f32 v70, v70\n\t"
        "v_exp_f32 v71, v71\n\tv_exp_f32 v72, v72\n\tv_exp_f32 v73, v73\n\tv_smfmac_f32_32x32x32_bf16 %[o1], v[108:111], v[64:71], v128\n\tv_exp_f32 v74, v74\n\tv_exp_f32 v75, v75\n\t"
        "v_exp_f32 v76, v76\n\tv_exp_f32 v77, v77\n\tv_exp_f32 v78, v78\n\tv_exp_f32 v79, v79\n\ts_setprio 0\n\ts_nop 0\n\tv_smfmac_f32_32x32x32_bf16 %[o1], v[124:127], v[72:79], v128\n\t"
        "s_waitcnt vmcnt(0)\n\ts_nop 15\n\t"
        : [o0] "+v"(o0), [o1] "+v"(o1), [q0] "=&v"(q0), [q1] "=&v"(q1), "=&{v[144:159]}"(sg[0]), "=&{v[160:175]}"(sg[1]), "=&{v[176:191]}"(sg[2]), "=&{v[192:207]}"(sg[3]), "=&{v[208:223]}"(sg[4]), "=&{v[224:239]}"(sg[5]), "+{v[240:255]}"(sg[6])
        : [qa] "v"(qa), [qb] "v"(qb), [koff] "v"(koff), [voff] "v"(voff), [vinit] "v"(vinit), [vm] "s"(0x0000ffff0000ffffull), [kin] "s"(kb), [vin] "s"(vb), [drop] "s"(drop),
          [a0] "v"(a0), [a1] "v"(a1), [a2] "v"(a2), [b0] "v"(v30), [b1] "v"(v31), [b2] "v"(v32), [vw] "v"(vw), [sa] "s"(sa), [sb] "s"(sb), [so] "s"(so), [sh] "s"(sh), [ga] "s"(ga), [ol] "s"(ol), [hl] "s"(hl)
        : "memory", "scc", "s40", "s41", "s42", "s43", "s46", "s47", "s48", "s49", "s50", "s51", "s52", "s53",
          "v48","v49","v50","v51","v52","v53","v54","v55","v56","v57","v58","v59","v60","v61","v62","v63","v64","v65","v66","v67","v68","v69","v70","v71","v72","v73","v74","v75","v76","v77","v78","v79",
          "v80","v81","v82","v83","v84","v85","v86","v87","v88","v89","v90","v91","v92","v93","v94","v95","v96","v97","v98","v99","v100","v101","v102","v103","v104","v105","v106","v107","v108","v109","v110","v111",
          "v112","v113","v114","v115","v116","v117","v118","v119","v120","v121","v122","v123","v124","v125","v126","v127","v128");
    }
    STAMP(7); STAMPC(27);
    lane = __builtin_amdgcn_mbcnt_hi(~0u, __builtin_amdgcn_mbcnt_lo(~0u, 0u)); asm volatile("" : "+v"(lane));
    r32 = lane & 31; h = lane >> 5; q = (lane & 15) >> 2; p = lane & 3; blk = (lane >> 4) & 1;
    rowoff = ((rg * 2 + (lw >> 1)) * 32 + r32) * D_ + cs * 64 + (lw & 1) * 32 + 8 * h;
    if (wave < 4) { res[0] = *(const u32x4*)(x + rowoff); res[1] = *(const u32x4*)(x + rowoff + 16); }
    {
      LAS char* d = smem + L_RED + wave * 2304 + lane * 16;
      *(LAS f32x4*)d = f32x4{o1[0], o1[1], o1[2], o1[3]}; *(LAS f32x4*)(d + 1024) = f32x4{o1[4], o1[5], o1[6], o1[7]};
      *(LAS float*)(smem + L_RED + wave * 2304 + 2048 + lane * 4) = o1[8];
    }
    BARRIER();
    float o[9];
    {
      LAS char* d = smem + L_RED + (wave ^ 1) * 2304 + lane * 16;
      const f32x4 e0 = *(LAS f32x4*)d, e1 = *(LAS f32x4*)(d + 1024);
      o[8] = o0[8] + *(LAS float*)(smem + L_RED + (wave ^ 1) * 2304 + 2048 + lane * 4);
#pragma unroll
      for (int i = 0; i < 4; i++) { o[i] = o0[i] + e0[i]; o[4 + i] = o0[4 + i] + e1[i]; }
    }
    const bool ok = o[8] >= 0x1p-40f && o[8] <= 0x1p40f;
    if (__builtin_expect(!__all(ok), 0)) {
      const u32x4 qq = q0;
      f32x16 z, oc;
#pragma unroll
      for (int i = 0; i < 16; i++) { z[i] = 0.f; oc[i] = 0.f; }
      float m = -__builtin_inff();
      const bool vl = (lane & 16) == 0;
      const u32x4 ones = {0x3f803f80u, 0x3f803f80u, 0x3f803f80u, 0x3f803f80u};
#pragma unroll 1
      for (int t = 0; t < 64; t++) {
        const u32x4 kk = ld16(rs, lane * 16, WS_K + head * 65536 + t * 1024);
        u32x4 wa = ld16(rs, h * 256 + (lane & 15) * 16, WS_V + head * 65536 + t * 1024), wb = ld16(rs, h * 256 + (lane & 15) * 16, WS_V + head * 65536 + t * 1024 + 512);
        wa = vl ? wa : ones; wb = vl ? wb : ones;
        f32x16 s = mfma(kk, qq, z);
        float tm = s[0];
#pragma unroll
        for (int i = 1; i < 16; i++) tm = fmaxf(tm, s[i]);
        tm = fmaxf(tm, __shfl_xor(tm, 32));
        const float mn = fmaxf(m, tm), al = __builtin_amdgcn_exp2f(m - mn);
        m = mn;
#pragma unroll
        for (int i = 0; i < 16; i++) { oc[i] *= al; s[i] -= mn; }
        oc = pv(wa, exp8(s, 0), oc); oc = pv(wb, exp8(s, 8), oc);
      }
#pragma unroll
      for (int i = 0; i < 9; i++) o[i] = oc[i];
    }
    const float rl = __builtin_amdgcn_rcpf(o[8]);
    u32x4 r;
#pragma unroll
    for (int j = 0; j < 4; j++) r[j] = pack2(o[2 * j] * rl, o[2 * j + 1] * rl);
    st16(rs, lane * 16, WS_O + ((rg1 * 2 + kh) * 32 + head) * 1024, r);
    STAMP(8);
    DRAIN(2);
    BARRIER();
    if (wave == 4) publish(ws, WS_F2 + (rg1 * 8 + hg) * 128, tag, lane);
    STAMP(9);
  }

  auto opq = [](int v) { asm volatile("" : "+v"(v)); return v; };
  const int bb1 = L_B1 + (8 * h + q) * 192 + blk * 32 + p * 8;
  if (wave < 4) {
  const int rb = wave >> 1, kh = wave & 1;
  const int tile = rg * 2 + rb;
  LAS char* bsq[2];
#pragma unroll
  for (int u = 0; u < 2; u++) bsq[u] = smem + (4 * h + q) * 128 + (((4 * (u ^ kh) + 2 * blk + (p & 1)) ^ ((q >> 1) << 2)) << 4) + 8 * (p >> 1);
  auto nohook = [](auto) {};
  auto exchange = [&](f32x16 (&acc)[2], int lds) {
#pragma unroll
    for (int i = 0; i < 4; i++) *(LAS f32x4*)(smem + lds + (wave * 4 + i) * 1024 + lane * 16) = f32x4{acc[1][4 * i], acc[1][4 * i + 1], acc[1][4 * i + 2], acc[1][4 * i + 3]};
    BARRIER();
#pragma unroll
    for (int i = 0; i < 4; i++) {
      const f32x4 v = *(LAS f32x4*)(smem + lds + ((wave ^ 1) * 4 + i) * 1024 + lane * 16);
      for (int j = 0; j < 4; j++) acc[0][4 * i + j] += v[j];
    }
  };
  {
    f32x16 acc[2];
#pragma unroll
    for (int i = 0; i < 16; i++) { acc[0][i] = (i & 1) ? bfhi(res[i >> 3][(i >> 1) & 3]) : bflo(res[i >> 3][(i >> 1) & 3]); acc[1][i] = 0.f; }
    asm volatile("" : "+v"(acc[0]));
    gemm_direct<16, 2, 0, Run4>(rs, lane * 16, [&](int pos) { return WS_O + (tile * 32 + kh * 16 + ((pos + off3) & 15)) * 1024; },
                          [&](int pos, int u) { return tr_frag(bsq[u] + kh * 32768 + pos * 2048, 1024); }, (const u32x4(*)[2]) nullptr, acc,
                                [&]() { BARRIER(); STAMP(10); },
                                nohook);
    STAMP(11);
    exchange(acc, L_Q);
    f32x2 t[8];
#pragma unroll
    for (int i = 0; i < 8; i++) t[i] = f32x2{acc[0][2 * i], acc[0][2 * i + 1]};
    FENCE();
#pragma unroll
    for (int i = 0; i < 8; i++) res[i >> 2][i & 3] = pack2v(t[i]);
    FENCE();
    st16(rs, lane * 16, WS_H + (tile * 32 + cs * 4 + kh * 2) * 1024, res[0]);
    st16(rs, lane * 16, WS_H + (tile * 32 + cs * 4 + kh * 2 + 1) * 1024, res[1]);
    f32x2 s2[4], q2[4];
#pragma unroll
    for (int i = 0; i < 4; i++) { s2[i] = t[i] + t[i + 4]; q2[i] = pfma(t[i], t[i], t[i + 4] * t[i + 4]); }
    const f32x2 sa = (s2[0] + s2[1]) + (s2[2] + s2[3]), qa = (q2[0] + q2[1]) + (q2[2] + q2[3]);
    const float sx = xhalf_sum(sa[0] + sa[1]), sxx = xhalf_sum(qa[0] + qa[1]);
    if (h == 0) __builtin_amdgcn_raw_buffer_store_b64(u32x2{asu(sx), asu(sxx)}, rs, r32 * 8, WS_ST + (tile * 16 + cs * 2 + kh) * 256, 16);
    STAMP(12);
    BARRIER();
#pragma unroll
    for (int i = 0; i < 12; i++) *(LAS u32x4*)(smem + L_B1 + lw * 12288 + i * 1024 + lane * 16) = stg(i);
    DRAIN(4);
    BARRIER();
#pragma unroll
    for (int i = 0; i < 8; i++) {
      LAS char* dst = smem + (2 * ((lw >> 1) * 7 + i) + (lw & 1)) * 2048 + lane * 16;
      *(LAS u32x4*)dst = stg(12 + 2 * i); *(LAS u32x4*)(dst + 1024) = stg(13 + 2 * i);
    }
    STAMP(13);
  }
  u32x4 pre[4][2];
  {
    const int t4 = rp * 4 + wave;
    f32x16 acc[3];
#pragma unroll
    for (int u = 0; u < 3; u++) for (int i = 0; i < 16; i++) acc[u][i] = 0.f;
    u32x4 wx[3];
    const int b4[2] = {opq(bb1), opq(bb1 + 49152)};
    unsigned sto = WS_ST + t4 * 4096; asm volatile("" : "+s"(sto));
    u32x2 st[8];
    float rs2 = 0.f, sx = 0.f, sxx = 0.f, nm = 0.f, sd = 0.f;
    u32x4 ax = {0u, 0u, 0u, 0u};
    gemm_direct<32, 3, 0, Run4>(rs, lane * 16, [&](int pos) { return WS_H + (t4 * 32 + ((pos + off4) & 31)) * 1024; },
        [&](int pos, int u) { return tr_frag(smem + b4[pos >> 4] + (pos & 15) * 3072 + u * 64, 768); }, (const u32x4(*)[3]) nullptr, acc,
        [&]() { BARRIER(); STAMP(16); },
        [&](auto pp) {
          constexpr int pos = pp.value;
          if constexpr (pos < 8) st[pos] = __builtin_amdgcn_raw_buffer_load_b64(rs, h * 2048 + r32 * 8 + pos * 256, sto, 16);
          if constexpr (pos >= 12 && pos < 20) { sx = add1(asf(st[pos - 12][0]), sx); sxx = add1(asf(st[pos - 12][1]), sxx); }
          if constexpr (pos == 20) sx = xhalf_sum(sx);
          if constexpr (pos == 21) sxx = xhalf_sum(sxx);
          if constexpr (pos == 22) { nm = sx * (-1.f / D_); sd = fmaxf(fma1(sxx, 1.f / D_, -nm * nm), 0.f) + 1e-5f; }
          if constexpr (pos == 23) { rs2 = __builtin_amdgcn_rsqf(sd); asm("" : "+v"(rs2)); }
          if constexpr (pos == 24) { sd *= rs2; rs2 *= LOG2E; nm = h ? 0.f : nm; sd = h ? 0.f : sd; asm("" : "+v"(nm), "+v"(sd), "+v"(rs2)); }
          if constexpr (pos == 25) { ax[0] = pack2(nm, nm); ax[3] = pack2(sd, sd); }
          if constexpr (pos == 26) { nm = add1(nm, -bflo(ax[0])); sd = add1(sd, -bflo(ax[3])); }
          if constexpr (pos >= 28 && pos < 31) wx[pos - 28] = *(LAS u32x4*)(smem + L_RES + 256 + (pos - 28) * 512 + r32 * 16);
          if constexpr (pos == 27) { ax[1] = (pack2(nm, 0.f) & 0xffffu) | (ax[3] & 0xffff0000u); ax[2] = pack2(sd, 0.f); ax[3] = 0u; }
        });
#pragma unroll
    for (int u = 0; u < 3; u++) acc[u] = mfma(wx[u], ax, acc[u]);
    STAMP(17);
#pragma unroll
    for (int i = 0; i < 4; i++) for (int u = 0; u < 2; u++) pre[i][u] = tr_frag(bsq[u] + (2 * i + kh) * 2048, 1024);
    FENCE();
    prog[wave] = 34;
    FENCE();
#pragma unroll
    for (int u = 0; u < 3; u++) {
      if (u < nunit) {
        f32x2 w[8], t[8];
#pragma unroll
        for (int i = 0; i < 8; i++) w[i] = f32x2{acc[u][2 * i], acc[u][2 * i + 1]} * pk(rs2);
        FENCE();
#pragma unroll
        for (int i = 0; i < 8; i++) t[i] = f32x2{__builtin_amdgcn_exp2f(w[i][0]), __builtin_amdgcn_exp2f(w[i][1])};
        FENCE();
        f32x2 a2[8];
#pragma unroll
        for (int i = 0; i < 8; i++) a2[i] = t[i] + pk(2.f);
        FENCE();
#pragma unroll
        for (int i = 0; i < 8; i++) t[i] = pfma(t[i], a2[i], pk(2.f));
        FENCE();
#pragma unroll
        for (int i = 0; i < 8; i++) t[i] = f32x2{__builtin_amdgcn_rcpf(t[i][0]), __builtin_amdgcn_rcpf(t[i][1])};
        FENCE();
#pragma unroll
        for (int i = 0; i < 8; i++) t[i] = pfma(t[i], pk(-2.f / LOG2E), pk(1.f / LOG2E));
        FENCE();
#pragma unroll
        for (int i = 0; i < 8; i++) t[i] = t[i] * w[i];
        FENCE();
        unsigned o[8];
#pragma unroll
        for (int i = 0; i < 8; i++) o[i] = pack2v(t[i]);
        FENCE();
        const unsigned so = WS_ACT + (t4 * 86 + (unit0 + u) * 2) * 1024;
        st16(rs, lane * 16, so, u32x4{o[0], o[1], o[2], o[3]});
        st16(rs, lane * 16, so + 1024, u32x4{o[4], o[5], o[6], o[7]});
      }
    }
    STAMP(18);
    DRAIN(8);
    STAMP(19);
  }
  {
    f32x16 acc[2];
#pragma unroll
    for (int i = 0; i < 16; i++) { acc[0][i] = (i & 1) ? bfhi(res[i >> 3][(i >> 1) & 3]) : bflo(res[i >> 3][(i >> 1) & 3]); acc[1][i] = 0.f; }
    asm volatile("" : "+v"(acc[0]));
    int b5[2][3];
    unsigned fl;
#pragma unroll
    for (int i = 0; i < 6; i++) b5[i / 3][i % 3] = opq((int)(bsq[i / 3] - smem) + kh * 2048 + (i % 3) * 13 * 4096);
    BARRIER();
    gemm_direct<43, 2, 4, Run5>(rs, lane * 16, [&](int pos) { int ks = pos + off5; ks = ks >= 43 ? ks - 43 : ks; return WS_ACT + (tile * 86 + kh * 43 + ks) * 1024; },
                          [&](int pos, int u) { const int sp = pos >= 39 ? pos - 39 : pos; return tr_frag(smem + b5[u][sp / 13] + (sp % 13) * 4096, 1024); }, pre, acc,
                                [&]() { BARRIER(); STAMP(20); },
                                [&](auto pp) {
                                  if constexpr (pp.value == 8) prog[wave] = 35;
                                  if constexpr (pp.value == 40) fl = *(volatile LAS unsigned*)(smem + L_RES + p * 4);
                                });
    STAMP(21);
    if (__builtin_expect(__any(fl < 35), 0)) lds_spin<0>(L_RES + p * 4, 35);
    exchange(acc, 16384);
#pragma unroll
    for (int s = 0; s < 2; s++) {
      u32x4 o;
#pragma unroll
      for (int j = 0; j < 4; j++) o[j] = pack2(acc[0][8 * s + 2 * j], acc[0][8 * s + 2 * j + 1]);
      __builtin_amdgcn_raw_buffer_store_b128(o, __builtin_amdgcn_make_buffer_rsrc(out, 0, 2048 * D_ * 2, 0x00020000), (unsigned)rowoff * 2 + 32 * s, 0, OUTAUX);
    }
    STAMP(22);
    STAMP_FLUSH();
  }
  } else {
    const unsigned bv = b1[(unit0 + max(lw - 1, 0)) * 32 + r32];
    const unsigned selv = ((lane & 15) == 4 * blk) ? 0x3f803f80u : 0u;
    const u32x4 sel = {selv, selv, selv, selv};
    f32x4 c4 = {0.f, 0.f, 0.f, 0.f};
    auto colsum = [&](int k0) {
      u32x4 f[16];
      const int bc = opq(bb1 + k0 * 3072 + max(lw - 1, 0) * 64);
#pragma unroll
      for (int i = 0; i < 16; i++) f[i] = tr_frag(smem + bc + i * 3072, 768);
#pragma unroll
      for (int i = 0; i < 16; i++) c4 = mfma16(sel, f[i], c4);
    };
    if (wave == 4) wait_flags<PD2>(ws, WS_F2 + (rg * 8 + (lane & 7)) * 128, tag); else colsum(16);
    BARRIER();
    BARRIER();
    BARRIER();
    BARRIER();
    STAMP(14);
    if (wave == 4) { publish(ws, WS_F3 + (rg * 8 + cs) * 128, tag, lane); wait_flags<PD3>(ws, WS_F3 + (rp * 16 + (lane & 15)) * 128, tag); }
    else colsum(0);
    if (lw > 0 && h == 0) {
      const unsigned ch = pack2(c4[0], 0.f), cl = pack2(c4[0] - bflo(ch), 0.f);
      *(LAS u32x4*)(smem + L_RES + 256 + (lw - 1) * 512 + r32 * 16) = u32x4{ch | (cl << 16), ch | (bv << 16), bv, 0u};
    }
    STAMP(15);
    BARRIER();
    lds_spin<SL4>(L_RES + p * 4, 34);
    LAS char* dst = smem + (2 * (15 + (lw >> 1) * 14) + (lw & 1)) * 2048 + lane * 16;
    LAS char* dst2 = (lw >> 1) ? dst - 78 * 2048 : dst;
#pragma unroll
    for (int i = 0; i < 14; i++) { LAS char* d = (i >= 10 ? dst2 : dst) + i * 4096; *(LAS u32x4*)d = stg(2 * i); *(LAS u32x4*)(d + 1024) = stg(2 * i + 1); }
    STAMP(19);
    BARRIER();
    if (wave == 4) {
      publish(ws, WS_F4 + (rp * 16 + sl) * 128, tag, lane); wait_flags<PD4>(ws, WS_F4 + ((rg >> 1) * 16 + (lane & 15)) * 128, tag);
      if (b == 0 && lane == 0) *(u64*)(ws + WS_EP) = tag;
    }
    BARRIER();
    STAMP_FLUSH();
  }
}

extern "C" void kernel_launch(void* const* d_in, const int* in_sizes, int n_in, void* d_out, int out_size, void* d_ws, size_t ws_size, hipStream_t stream) {
  fused<<<256, 512, 0, stream>>>((const u16*)d_in[0], (const u16*)d_in[1], (const u16*)d_in[2], (const u16*)d_in[4], (const u16*)d_in[5], (const u16*)d_in[6], (char*)d_ws, (u16*)d_out);
}
```

```cpp
#include <hip/hip_runtime.h>
#include <hip/hip_bf16.h>
#include <stdint.h>

typedef __attribute__((ext_vector_type(16))) float f32x16;
typedef __attribute__((ext_vector_type(8))) float f32x8;
typedef __attribute__((ext_vector_type(4))) float f32x4;
typedef __attribute__((ext_vector_type(2))) float f32x2;
typedef __attribute__((ext_vector_type(8))) __bf16 bf16x8;
typedef __attribute__((ext_vector_type(16))) __bf16 bf16x16;
typedef __attribute__((ext_vector_type(2))) __bf16 bf16x2;
typedef __attribute__((ext_vector_type(4))) short s16x4;
typedef __attribute__((ext_vector_type(4))) unsigned u32x4;
typedef __attribute__((ext_vector_type(2))) unsigned u32x2;
typedef __attribute__((ext_vector_type(16))) unsigned u32x16;
typedef unsigned short u16;
typedef unsigned long long u64;
#define LAS __attribute__((address_space(3)))
#define GAS __attribute__((address_space(1)))
#define DEV __device__ __forceinline__

constexpr int D_ = 512, F_ = 1376;
constexpr float LOG2E = 1.4426950408889634f;

constexpr unsigned WS_K = 0, WS_V = 2u << 20, WS_O = 4u << 20, WS_H = 6u << 20, WS_ACT = 8u << 20, WS_ST = 14u << 20;
constexpr unsigned WS_EP = 15u << 20, WS_F1 = WS_EP + 4096, WS_F2 = WS_F1 + 32768, WS_F3 = WS_F2 + 32768, WS_F4 = WS_F3 + 32768, WS_END = 16u << 20;

DEV unsigned pack2(float a, float b) { f32x2 p = {a, b}; return __builtin_bit_cast(unsigned, __builtin_convertvector(p, bf16x2)); }
DEV float asf(unsigned u) { return __builtin_bit_cast(float, u); }
DEV unsigned asu(float f) { return __builtin_bit_cast(unsigned, f); }
DEV float bflo(unsigned u) { return __builtin_bit_cast(float, u << 16); }
DEV float bfhi(unsigned u) { return __builtin_bit_cast(float, u & 0xffff0000u); }
DEV f32x16 mfma(u32x4 a, u32x4 b, f32x16 c) { return __builtin_amdgcn_mfma_f32_32x32x16_bf16(__builtin_bit_cast(bf16x8, a), __builtin_bit_cast(bf16x8, b), c, 0, 0, 0); }
DEV f32x4 mfma16(u32x4 a, u32x4 b, f32x4 c) { return __builtin_amdgcn_mfma_f32_16x16x32_bf16(__builtin_bit_cast(bf16x8, a), __builtin_bit_cast(bf16x8, b), c, 0, 0, 0); }
DEV u32x2 tr_read(LAS char* p) { return __builtin_bit_cast(u32x2, __builtin_amdgcn_ds_read_tr16_b64_v4i16((LAS s16x4*)p)); }
DEV u32x4 tr_frag(LAS char* p, int step) { const u32x2 w0 = tr_read(p), w1 = tr_read(p + step); return u32x4{w0[0], w0[1], w1[0], w1[1]}; }
DEV float xhalf_sum(float v) {
  const unsigned u = __builtin_bit_cast(unsigned, v);
  const auto r = __builtin_amdgcn_permlane32_swap(u, u, false, false);
  return asf(r[0]) + asf(r[1]);
}
#define BARRIER() do { __builtin_amdgcn_sched_barrier(0); __builtin_amdgcn_s_waitcnt(0xC07F); asm volatile("s_barrier" ::: "memory"); __builtin_amdgcn_sched_barrier(0); } while (0)
template <int N> DEV void wait_vm() { asm volatile("s_waitcnt vmcnt(%0)" ::"n"(N) : "memory"); }
#define SGB(m, n) __builtin_amdgcn_sched_group_barrier(m, n, 0)
template <int I, int N, class Fn> DEV void static_for(Fn&& f) {
  if constexpr (I < N) { f(std::integral_constant<int, I>{}); static_for<I + 1, N>(f); }
}

#define STAMP(i)
#define STAMPC(i)
#define STAMP_DECL
#define STAMP_FLUSH()

DEV void dma_kv(u64 sw, unsigned v, int lds) {
  asm volatile("s_mov_b32 m0, %[m]\n\ts_nop 0\n\tglobal_load_lds_dwordx4 %[v], %[sw]\n\t"
               "s_add_u32 m0, %[m], 0x1c00\n\ts_nop 0\n\tglobal_load_lds_dwordx4 %[v], %[sw] offset:1024"
               :: [m] "s"(lds), [sw] "s"(sw), [v] "v"(v) : "memory", "scc");
}
DEV void dma_slot(u64 sw, unsigned v, int lds) {
#define P1(dm) "s_add_u32 m0, %[m], " #dm "\n\ts_add_u32 s44, s44, 0x6000\n\ts_addc_u32 s45, s45, 0\n\tglobal_load_lds_dwordx4 %[v], s[44:45]\n\t"
  asm volatile("s_mov_b64 s[44:45], %[sw]\n\t" P1(0) P1(0x400) P1(0x800) P1(0xc00) P1(0x1000) P1(0x1400) P1(0x1800) P1(0x1c00)
               :: [m] "s"(lds), [sw] "s"(sw - 0x6000), [v] "v"(v) : "memory", "scc", "s44", "s45");
}
#define RLX_AGENT __ATOMIC_RELAXED, __HIP_MEMORY_SCOPE_AGENT
DEV void publish(char* ws, unsigned off, u64 tag, int lane) {
  if (lane == 0) __hip_atomic_store((GAS u64*)(ws + off), tag, RLX_AGENT);
}
#define PSLEEP ""
DEV void wait_flags(char* ws, unsigned off, u64 tag) {
  u64 v; int n = 1 << 18;
  asm volatile("1:\n\tglobal_load_dwordx2 %[v], %[off], %[ws] sc1\n\ts_sub_u32 %[n], %[n], 1\n\ts_cbranch_scc1 2f\n\t"
               "s_waitcnt vmcnt(0)\n\tv_cmp_ne_u64 vcc, %[tag], %[v]\n\t" PSLEEP "s_cbranch_vccnz 1b\n\t2:\n\ts_waitcnt vmcnt(0)"
               : [v] "=&v"(v), [n] "+s"(n) : [off] "v"(off), [ws] "s"(ws), [tag] "s"(tag) : "memory", "vcc", "scc");
}
typedef __amdgpu_buffer_rsrc_t rsrc_t;
DEV void st16(rsrc_t r, unsigned voff, unsigned soff, u32x4 v) { __builtin_amdgcn_raw_buffer_store_b128(v, r, voff, soff, 16); }
DEV u32x4 ld16(rsrc_t r, unsigned voff, unsigned soff) { return __builtin_amdgcn_raw_buffer_load_b128(r, voff, soff, 16); }
#define LSLEEP 1
#define THR 6
#define W2POS 12
#define OUTAUX 0
#define FIRST 4
#define FSLEEP 6
#define FLPOS 1
#define EARLY 0
#define DRAIN(m) do { if (!(EARLY & m)) wait_vm<0>(); } while (0)
#define SL1 1
#define SL4 1
#define QREL 0
#define XRX 0
#define QT 0
#define NINIT 3
#define WDELAY 4
#define DROPI 4
template <int SL> DEV void lds_spin(unsigned addr, unsigned need) {
  unsigned a;
  asm volatile("1:\n\tds_read_b32 %[a], %[p]\n\ts_waitcnt lgkmcnt(0)\n\tv_cmp_gt_u32 vcc, %[n], %[a]\n\ts_cbranch_vccz 2f\n\ts_sleep %[s]\n\ts_branch 1b\n\t2:"
               : [a] "=&v"(a) : [p] "v"(addr), [n] "s"(need), [s] "n"(SL) : "memory", "vcc");
}
DEV f32x16 pv(u32x4 v, f32x8 pr, f32x16 o) {
  return __builtin_amdgcn_smfmac_f32_32x32x32_bf16(__builtin_bit_cast(bf16x8, v), __builtin_bit_cast(bf16x16, pr), o, 0xDDDD, 0, 0);
}
DEV f32x8 exp8(f32x16 s, int o) { f32x8 r; for (int i = 0; i < 8; i++) r[i] = __builtin_amdgcn_exp2f(s[o + i]); return r; }
DEV float fma1(float a, float b, float c) { float r = __builtin_fmaf(a, b, c); asm("" : "+v"(r)); return r; }
DEV float add1(float a, float b) { float r = a + b; asm("" : "+v"(r)); return r; }
DEV float dot2(unsigned a, unsigned b, float c) { return __builtin_amdgcn_fdot2_f32_bf16(__builtin_bit_cast(bf16x2, a), __builtin_bit_cast(bf16x2, b), c, false); }
DEV float rbf(float a) { return bflo(pack2(a, 0.f)); }
DEV f32x2 pk(float a) { return f32x2{a, a}; }
DEV f32x2 pfma(f32x2 a, f32x2 b, f32x2 c) { return __builtin_elementwise_fma(a, b, c); }
DEV unsigned pack2v(f32x2 p) { return __builtin_bit_cast(unsigned, __builtin_convertvector(p, bf16x2)); }
#define FENCE() __builtin_amdgcn_sched_barrier(0)

struct WFrag { u32x4 a[8]; u32x4 w[8]; };
constexpr int L_RES = 159744;
constexpr int L_Q = 67584, L_RED = 75776, L_DUM = 94208;
constexpr int L_W1 = 65536, L_Q1 = 98304;
constexpr int L_B1 = 61440;
constexpr int CODE_BYTES = 30720;
#define RD_ 10
constexpr int RD = RD_;

struct Run4 { static constexpr int start(int pos) { return pos & ~3; } };
struct Run5 { static constexpr int start(int pos) { const int s0 = pos >= 35 ? 35 : pos >= 29 ? 29 : pos >= 21 ? 21 : pos >= 15 ? 15 : 0; return s0 + ((pos - s0) & ~3); } };
template <int NK, int NU, int NPRE, class GS, class ASO, class BRD, class RDY, class H1>
DEV void gemm_direct(rsrc_t rs, unsigned voff, ASO&& aso, BRD&& brd, const u32x4 (*pre)[NU], f32x16 (&acc)[NU], RDY&& ready, H1&& hook) {
  u32x4 a[RD], w[3][NU];
  unsigned so[NK];
  static_for<0, NK>([&](auto i) { if constexpr (GS::start(i.value) == i.value) { so[i.value] = aso(i.value); asm volatile("" : "+s"(so[i.value])); } });
  auto lda = [&](auto i) { return ld16(rs, voff + (i.value - GS::start(i.value)) * 1024, so[GS::start(i.value)]); };
  static_for<0, 2>([&](auto i) {
    if constexpr (i.value >= NPRE) { for (int u = 0; u < NU; u++) w[i.value][u] = brd(i.value, u); }
  });
  ready();
  static_for<0, RD>([&](auto i) { a[i.value] = lda(i); FENCE(); });
  FENCE();
  static_for<0, NK>([&](auto pp) {
    constexpr int pos = pp.value;
    static_for<0, NU>([&](auto uu) {
      constexpr int u = uu.value;
      if constexpr (pos < NPRE) acc[u] = mfma(pre[pos][u], a[pos % RD], acc[u]); else acc[u] = mfma(w[pos % 3][u], a[pos % RD], acc[u]);
      FENCE();
      if constexpr (u == 0) hook(pp);
      if constexpr (pos + 2 < NK && pos + 2 >= NPRE) w[(pos + 2) % 3][u] = brd(pos + 2, u);
      if constexpr (u == NU - 1) {
        if constexpr (pos + RD < NK) a[pos % RD] = lda(std::integral_constant<int, pos + RD>{});
      }
      FENCE();
    });
  });
}

__global__ __launch_bounds__(512, 2) void fused(const u16* __restrict__ x, const u16* __restrict__ wqkv, const u16* __restrict__ wo, const u16* __restrict__ w1,
                                                const u16* __restrict__ b1, const u16* __restrict__ w2, char* __restrict__ ws, u16* __restrict__ out) {
  __shared__ __attribute__((aligned(16))) char smem_[163840];
  LAS char* smem = (LAS char*)smem_;
  const int wave = __builtin_amdgcn_readfirstlane(threadIdx.x >> 6);
  int lane = threadIdx.x & 63;
  const int b = blockIdx.x, xcd = b & 7, bi = b >> 3;
  STAMP_DECL; STAMP(0);
  u64 pc0; asm volatile("s_getpc_b64 %0" : "=s"(pc0));
  asm volatile("" ::"s"(x), "s"(wqkv), "s"(wo), "s"(w1), "s"(b1), "s"(w2), "s"(ws), "s"(out));
  u64 tag = 0;
  auto load_tag = [&]() { asm volatile("s_load_dwordx2 %0, %1, 0x0\n\ts_waitcnt lgkmcnt(0)" : "=s"(tag) : "s"(ws + WS_EP) : "memory"); tag += 1; };
  const rsrc_t rs = __builtin_amdgcn_make_buffer_rsrc(ws, 0, WS_END, 0x00020000);
  int r32 = lane & 31, h = lane >> 5, q = (lane & 15) >> 2, p = lane & 3, blk = (lane >> 4) & 1;
  const int lw = wave & 3;
  const unsigned vrow128 = 16 * ((lane & 7) ^ (((lane >> 4) & 1) << 2));
  volatile LAS unsigned* prog = (volatile LAS unsigned*)(smem + L_RES);

#define MAPB 1
  constexpr int HS = 3 - MAPB;
  const int hg = ((xcd & ((1 << MAPB) - 1)) << HS) + (bi & ((1 << HS) - 1)), rg1 = (xcd >> MAPB) * (32 >> HS) + (bi >> HS);
  const int c0 = bi >> HS;
  const u64 sw0 = (u64)(wqkv + hg * 64);
  const unsigned vw0 = (lane >> 3) * (3 * D_ * 2) + vrow128;
  const int seg = lane & 7, xr = (8 >> HS) * (bi & ((1 << HS) - 1)) + XRX * (xcd & 1);
  u32x4 xv[8];
  auto kv8 = [&](int c) { dma_kv(sw0 + 1024 + (unsigned)((((c + c0) & 7) * 8 + wave) * (8 * 3 * D_ * 2)), vw0, L_W1 + c * 16384 + wave * 1024); };
  {
    const char* xb = (const char*)x + (size_t)(rg1 * 64 + wave * 8) * 1024;
    const unsigned xo = (lane >> 3) * 1024 + seg * 16;
#pragma unroll
    for (int i = 0; i < 8; i++) asm volatile("global_load_dwordx4 %0, %1, %2" : "=v"(xv[i]) : "v"(xo), "s"(xb + ((i + xr) & 7) * 128) : "memory");
    asm volatile("s_barrier" ::: "memory");
    kv8(0);
    const char* tp = lane == 2 ? ws + WS_F1 + lw * 32768 + b * 128 : (const char*)(pc0 & ~127ull) + min(((bi * 8 + wave) * 2 + (lane & 1)) * 128, CODE_BYTES - 128);
    asm volatile("s_mov_b32 m0, %1\n\ts_nop 0\n\tglobal_load_lds_dword %0, off" ::"v"(tp), "s"(L_RES + 2048) : "memory");
  }

  const int rg = xcd * 4 + (bi >> 3), cs = bi & 7;
  const int rp = xcd * 2 + (bi >> 4), sl = bi & 15;
  const int unit0 = sl < 5 ? 2 * sl : 3 * sl - 5, nunit = sl < 5 ? 2 : 3;
  const int off3 = (cs & 3) * 4, off4 = (sl & 3) * 8, off5 = (0x080e161c00ull >> (8 * (cs % 5))) & 0xff;
  u32x16 sg[7];
  u32x4 res[2];
  auto stg = [&](int i) { return u32x4{sg[i >> 2][(i & 3) * 4], sg[i >> 2][(i & 3) * 4 + 1], sg[i >> 2][(i & 3) * 4 + 2], sg[i >> 2][(i & 3) * 4 + 3]}; };
  unsigned rowoff;
  const int head = hg * 4 + (wave >> 1), kh = wave & 1;
  unsigned koff, voff, vinit, a0, a1, a2, v30, v31, v32, vw, tpl, tph;
  const char *kb, *vb, *sa, *sb, *so, *sh;
  auto setup = [&](auto first) {
    auto v192 = [&](int j) { const int idx = j * 64 + lane, row = idx / 12; return (unsigned)(row * (F_ * 2) + (idx - row * 12) * 16); };
    auto w2at = [&](int pos) {
      int ks = pos + off5; ks = ks >= 43 ? ks - 43 : ks;
      return (const char*)w2 + (unsigned)(((lw & 1) * 43 + ks) * 16 * D_ * 2 + cs * 128);
    };
    auto w1at = [&](int pos) { return (const char*)w1 + (unsigned)(((pos + off4) & 31) * 16 * F_ * 2 + unit0 * 64); };
    vw = (lane >> 3) * (D_ * 2) + vrow128;
    v30 = v192(0); v31 = v192(1); v32 = v192(2);
    koff = lane * 16; voff = h * 256 + (lane & 15) * 16;
    kb = ws + WS_K + (unsigned)(head * 65536 + kh * 32768); vb = kb + (WS_V - WS_K);
    vinit = (r32 == 16 || r32 == 20) ? 0x3f803f80u : 0u;
    so = (const char*)wo + (unsigned)(((wave >> 2) * 256 + ((lw * 4 + off3) & 15) * 16) * D_ * 2 + cs * 128) - 8192;
    sh = w1at(16 + wave * 2);
    const char* tp;
    if constexpr (first.value) {
      a0 = v30; a1 = v31; a2 = v32; sa = w1at(lw * 4); sb = w2at((lw >> 1) * 7);
      const char* t0 = (const char*)((lw & 1) ? w2 : w1) + (unsigned)(b * 5504 + (lw >> 1) * 21 * 128);
      const char* t1 = (const char*)wo + (unsigned)(b * 2048 + lw * 512) - 22 * 128;
      const char* t2 = (const char*)b1 + (unsigned)(((b * 4 + lw) & 15) * 168) - 26 * 128;
      const int l = min(lane, 26);
      tp = (l < 22 ? t0 : l < 26 ? t1 : t2) + l * 128;
    } else {
      a0 = vw; a1 = vw + 8 * D_ * 2; a2 = vw + 16 * D_ * 2; sa = w2at(15 + (lw >> 1) * 14); sb = w2at(21 + (lw >> 1) * 14);
      tp = kb;
    }
    tpl = (unsigned)(u64)tp; tph = (unsigned)((u64)tp >> 32);
    asm volatile("" : "+v"(koff), "+v"(voff), "+v"(vinit), "+v"(a0), "+v"(a1), "+v"(a2), "+v"(v30), "+v"(v31), "+v"(v32), "+v"(vw), "+v"(tpl), "+v"(tph));
    asm volatile("" : "+s"(kb), "+s"(vb), "+s"(sa), "+s"(sb), "+s"(so), "+s"(sh));
  };
  {
    prog[wave] = wave < 4 ? 0 : 2 * NINIT + lw; prog[8 + lw] = 0;
    STAMP(1);
    float s4[4] = {0.f, 0.f, 0.f, 0.f}, q4[4] = {0.f, 0.f, 0.f, 0.f};
    static_for<0, 8>([&](auto ii) {
      constexpr int i = ii.value;
      asm volatile("s_waitcnt vmcnt(%1)" : "+v"(xv[i]) : "n"(10 - i));
#pragma unroll
      for (int j = 0; j < 4; j++) { s4[j] = dot2(xv[i][j], 0x3f803f80u, s4[j]); q4[j] = dot2(xv[i][j], xv[i][j], q4[j]); }
    });
    FENCE();
#pragma unroll
    for (int c = 1; c < NINIT; c++) kv8(c);
    asm volatile("s_mov_b32 m0, %2\n\ts_nop 0\n\tglobal_load_lds_dword %0, %1" ::"v"((lane & 7) * (3 * D_ * 2)), "s"(sw0 + (unsigned)(((c0 & 7) * 8 + wave) * (8 * 3 * D_ * 2))), "s"(L_RES + 2048) : "memory");
    STAMP(25);
    float sx = (s4[0] + s4[1]) + (s4[2] + s4[3]), sxx = (q4[0] + q4[1]) + (q4[2] + q4[3]);
    sx += asf(__builtin_amdgcn_mov_dpp(asu(sx), 0xB1, 0xf, 0xf, true)); sxx += asf(__builtin_amdgcn_mov_dpp(asu(sxx), 0xB1, 0xf, 0xf, true));
    sx += asf(__builtin_amdgcn_mov_dpp(asu(sx), 0x4E, 0xf, 0xf, true)); sxx += asf(__builtin_amdgcn_mov_dpp(asu(sxx), 0x4E, 0xf, 0xf, true));
    sx += asf(__builtin_amdgcn_mov_dpp(asu(sx), 0x141, 0xf, 0xf, true)); sxx += asf(__builtin_amdgcn_mov_dpp(asu(sxx), 0x141, 0xf, 0xf, true));
    const float mean = sx * (1.f / D_);
    const float var = rbf(fmaxf(sxx * (1.f / D_) - mean * mean, 0.f));
    const float rstd = __builtin_amdgcn_rcpf(rbf(__builtin_amdgcn_sqrtf(rbf(var + 1e-5f)))), nmr = -rbf(mean) * rstd;
    const int r = lw * 8 + (lane >> 3);
    LAS char* dst = smem + (wave >> 2) * 32768 + seg * 512 + ((r ^ seg) << 4);
#pragma unroll
    for (int i0 = 0; i0 < 8; i0 += 2) {
      f32x2 y[8];
#pragma unroll
      for (int j = 0; j < 8; j++) { const unsigned u = xv[i0 + (j >> 2)][j & 3]; y[j] = f32x2{bflo(u), bfhi(u)}; }
      FENCE();
#pragma unroll
      for (int j = 0; j < 8; j++) y[j] = pfma(y[j], pk(rstd), pk(nmr));
      FENCE();
      const u32x4 o0 = {pack2v(y[0]), pack2v(y[1]), pack2v(y[2]), pack2v(y[3])}, o1 = {pack2v(y[4]), pack2v(y[5]), pack2v(y[6]), pack2v(y[7])};
      *(LAS u32x4*)(dst + ((i0 + xr) & 7) * 4096) = o0;
      *(LAS u32x4*)(dst + ((i0 + 1 + xr) & 7) * 4096) = o1;
      FENCE();
    }
    STAMP(23);
    if (wave < 4) setup(std::true_type{});
    wait_vm<(QT < 2)>();
    BARRIER();
  }
  if (wave >= 4) {
    load_tag();
#pragma unroll 1
    for (int n = lw + 2 * NINIT; n < 24; n += 4) {
      if (n >= 10) lds_spin<SL1>(L_RES + p * 4, n == 23 ? max(15, QREL) : n >= 16 ? max(n - 9, QREL) : n - 9);
      const int c = n < 16 ? n >> 1 : n - 16;
      dma_slot(sw0 + (unsigned)(((c + c0) & 7) * (64 * 3 * D_ * 2) + (n < 16 ? 1024 + (n & 1) * 1024 : 0)), vw0, L_W1 + (n % 10) * 8192);
      wait_vm<0>(); prog[wave] = n + 4;
    }
    STAMP(2);
    setup(std::false_type{});
    if (wave == 4 + ((20 - 2 * NINIT) & 3)) {
      lds_spin<SL1>(L_RES + 32 + p * 4, 1);
      publish(ws, WS_F1 + (hg * 32 + rg1) * 128, tag, lane);
      STAMP(5);
      wait_flags(ws, WS_F1 + (hg * 32 + r32) * 128, tag);
    }
  } else {
    const int rb = wave >> 1, g = wave & 1;
    int aoff[4];
#pragma unroll
    for (int i = 0; i < 4; i++) aoff[i] = rb * 32768 + i * 1024 + h * 512 + ((r32 ^ (2 * i + h)) << 4);
    int boff[2];
    boff[0] = L_W1 + (8 * h + q) * 128 + (((4 * g + 2 * blk + (p >> 1)) ^ ((q >> 1) << 2)) << 4) + 8 * (p & 1); boff[1] = boff[0] + 40960;
    asm volatile("" : "+v"(boff[0]), "+v"(boff[1]));
    f32x16 acc[3];
#pragma unroll
    for (int u = 0; u < 3; u++) for (int i = 0; i < 16; i++) acc[u][i] = 0.f;
    unsigned fl;
    auto rd = [&](WFrag& f, int st) {
#pragma unroll
      for (int i = 0; i < 8; i++) {
        if (st >= 8) f.a[i] = *(LAS u32x4*)(smem + aoff[i & 3] + ((2 * st + (i >> 2) + c0) & 7) * 4096);
        else if (i < 4) f.a[i] = *(LAS u32x4*)(smem + aoff[i] + ((st + c0) & 7) * 4096);
        f.w[i] = tr_frag(smem + boff[((2 * st + (i >> 2)) % 10) / 5] + ((2 * st + (i >> 2)) % 5) * 8192 + (i & 3) * 2048, 512);
        if ((i & 3) == 3) prog[wave] = 2 * st + (i >> 2) + 1;
        if (i == 3) fl = *(volatile LAS unsigned*)(smem + L_RES + 16 + p * 4);
      }
    };
    auto mm = [&](const WFrag& f, int st) {
#pragma unroll
      for (int i = 0; i < 8; i++) {
        if (st >= 8) acc[0] = mfma(f.w[i], f.a[i], acc[0]);
        else if (i < 4) acc[1] = mfma(f.w[i], f.a[i], acc[1]);
        else acc[2] = mfma(f.a[i & 3], f.w[i], acc[2]);
      }
    };
    auto sched = [&]() {
      SGB(0x8, 1); SGB(0x2, 6);
#pragma unroll
      for (int i = 0; i < 7; i++) { SGB(0x80, 3); SGB(0x8, 1); }
    };
    auto step = [&](int st) { FENCE(); if (st >= NINIT && __builtin_expect(__any(fl < 2 * st + 2), 0)) lds_spin<0>(L_RES + 16 + p * 4, 2 * st + 2); FENCE(); };
    WFrag fa, fb;
    STAMP(24);
    rd(fa, 0);
    static_for<0, 3>([&](auto cc) {
      constexpr int c = 2 * cc.value + 1;
      step(c);
      rd(fb, c); mm(fa, c - 1); sched();
      step(c + 1);
      rd(fa, c + 1); mm(fb, c); sched();
    });
    step(7);
    STAMP(2);
    rd(fb, 7); mm(fa, 6); sched();
    FENCE();
    mm(fb, 7);
    STAMP(3);
    const int tile = rg1 * 2 + rb;
    const unsigned so1 = ((hg * 4 + 2 * g) * 64 + tile) * 1024;
    {
      unsigned o[8];
#pragma unroll
      for (int i = 0; i < 8; i++) o[i] = pack2(acc[1][2 * i], acc[1][2 * i + 1]);
      const unsigned pos = h * 32 + ((r32 & 19) | ((r32 & 4) << 1) | ((r32 & 8) >> 1));
      st16(rs, pos * 16, WS_K + so1, u32x4{o[0], o[1], o[2], o[3]});
      st16(rs, pos * 16, WS_K + so1 + 65536, u32x4{o[4], o[5], o[6], o[7]});
#pragma unroll
      for (int i = 0; i < 8; i++) o[i] = pack2(acc[2][2 * i], acc[2][2 * i + 1]);
      const unsigned vo = (r32 >> 4) * 65536 + h * 256 + (r32 & 15) * 16;
      st16(rs, vo, WS_V + so1, u32x4{o[0], o[1], o[2], o[3]});
      st16(rs, vo, WS_V + so1 + 512, u32x4{o[4], o[5], o[6], o[7]});
      STAMP(4);
      step(8); rd(fa, 8);
      step(9); rd(fb, 9); mm(fa, 8); sched();
      FENCE(); wait_vm<0>(); prog[8 + wave] = 1;
      STAMP(5);
      step(10); rd(fa, 10); mm(fb, 9); sched();
      step(11); rd(fb, 11); mm(fa, 10); sched();
      FENCE();
      mm(fb, 11);
#pragma unroll
      for (int i = 0; i < 8; i++) o[i] = pack2v(f32x2{acc[0][2 * i], acc[0][2 * i + 1]} * pk(0.25f * LOG2E));
      LAS char* d = smem + L_Q1 + (4 * g + rb) * 1024 + lane * 16;
      *(LAS u32x4*)d = u32x4{o[0], o[1], o[2], o[3]};
      *(LAS u32x4*)(d + 2048) = u32x4{o[4], o[5], o[6], o[7]};
    }
  }

  {
    u32x4 q0, q1;
    const unsigned qa = L_Q1 + wave * 1024 + lane * 16, qb = qa ^ 1024;
    const int drop = (wave >> 2) ^ 1;
    if (wave < 4) __builtin_amdgcn_s_setprio(2); else __builtin_amdgcn_s_setprio(1);
    f32x16 o0, o1;
#pragma unroll
    for (int i = 0; i < 16; i++) { o0[i] = 0.f; o1[i] = 0.f; }
    {
      const int ol = wave * 8192, hl = L_B1 + (16 + wave * 2) * 3072, ga = 16 * F_ * 2 - (wave >> 2) * (16 * F_ * 2 - 24 * D_ * 2);
      sg[6][14] = tpl; sg[6][15] = tph;
      STAMP(6); STAMPC(26);
      asm volatile(
        "s_mov_b64 s[40:41], %[kin]\n\ts_mov_b64 s[42:43], %[vin]\n\ts_mov_b64 s[46:47], %[sa]\n\ts_mov_b64 s[48:49], %[sb]\n\ts_mov_b64 s[50:51], %[so]\n\ts_mov_b64 s[52:53], %[sh]\n\t"
        "v_mov_b32 v96, %[vinit]\n\tv_mov_b32 v112, %[vinit]\n\tv_mov_b32 v97, %[vinit]\n\tv_mov_b32 v113, %[vinit]\n\tv_mov_b32 v98, %[vinit]\n\tv_mov_b32 v114, %[vinit]\n\t"
        "v_mov_b32 v99, %[vinit]\n\tv_mov_b32 v115, %[vinit]\n\tv_mov_b32 v100, %[vinit]\n\tv_mov_b32 v116, %[vinit]\n\tv_mov_b32 v101, %[vinit]\n\tv_mov_b32 v117, %[vinit]\n\t"
        "v_mov_b32 v102, %[vinit]\n\tv_mov_b32 v118, %[vinit]\n\tv_mov_b32 v103, %[vinit]\n\tv_mov_b32 v119, %[vinit]\n\tv_mov_b32 v104, %[vinit]\n\tv_mov_b32 v120, %[vinit]\n\t"
        "v_mov_b32 v105, %[vinit]\n\tv_mov_b32 v121, %[vinit]\n\tv_mov_b32 v106, %[vinit]\n\tv_mov_b32 v122, %[vinit]\n\tv_mov_b32 v107, %[vinit]\n\tv_mov_b32 v123, %[vinit]\n\t"
        "v_mov_b32 v108, %[vinit]\n\tv_mov_b32 v124, %[vinit]\n\tv_mov_b32 v109, %[vinit]\n\tv_mov_b32 v125, %[vinit]\n\tv_mov_b32 v110, %[vinit]\n\tv_mov_b32 v126, %[vinit]\n\t"
        "v_mov_b32 v111, %[vinit]\n\tv_mov_b32 v127, %[vinit]\n\tv_mov_b32 v128, 0xdddd\n\ts_waitcnt lgkmcnt(0)\n\ts_barrier\n\t"
        "global_load_dwordx4 v[80:83], %[koff], s[40:41] offset:0 sc1\n\tds_read_b128 %[q0], %[qa]\n\tds_read_b128 %[q1], %[qb]\n\t"
        "global_load_dwordx4 v[84:87], %[koff], s[40:41] offset:1024 sc1\n\ts_mov_b64 exec, %[vm]\n\tglobal_load_dwordx4 v[96:99], %[voff], s[42:43] offset:0 sc1\n\t"
        "global_load_dwordx4 v[112:115], %[voff], s[42:43] offset:512 sc1\n\ts_mov_b64 exec, -1\n\tglobal_load_dwordx4 v[88:91], %[koff], s[40:41] offset:2048 sc1\n\t"
        "s_mov_b64 exec, %[vm]\n\tglobal_load_dwordx4 v[100:103], %[voff], s[42:43] offset:1024 sc1\n\tglobal_load_dwordx4 v[116:119], %[voff], s[42:43] offset:1536 sc1\n\t"
        "s_mov_b64 exec, -1\n\tglobal_load_dwordx4 v[92:95], %[koff], s[40:41] offset:3072 sc1\n\ts_mov_b64 exec, %[vm]\n\t"
        "global_load_dwordx4 v[104:107], %[voff], s[42:43] offset:2048 sc1\n\tglobal_load_dwordx4 v[120:123], %[voff], s[42:43] offset:2560 sc1\n\ts_mov_b64 exec, -1\n\t"
        "s_waitcnt lgkmcnt(0)\n\ts_waitcnt vmcnt(9)\n\tv_mfma_f32_32x32x16_bf16 v[48:63], v[80:83], %[q0], 0\n\ts_nop 15\n\tv_exp_f32 v48, v48\n\tv_exp_f32 v49, v49\n\t"
        "v_exp_f32 v50, v50\n\tv_exp_f32 v51, v51\n\tv_exp_f32 v52, v52\n\tv_mfma_f32_32x32x16_bf16 v[64:79], v[80:83], %[q1], 0\n\ts_add_u32 s40, s40, 4096\n\ts_addc_u32 s41, s41, 0\n\t"
        "global_load_dwordx4 v[80:83], %[koff], s[40:41] offset:0 sc1\n\ts_mov_b64 exec, %[vm]\n\tglobal_load_dwordx4 v[108:111], %[voff], s[42:43] offset:3072 sc1\n\t"
        "global_load_dwordx4 v[124:127], %[voff], s[42:43] offset:3584 sc1\n\ts_mov_b64 exec, -1\n\tv_exp_f32 v53, v53\n\tv_exp_f32 v54, v54\n\tv_exp_f32 v55, v55\n\tv_exp_f32 v56, v56\n\t"
        "v_exp_f32 v57, v57\n\ts_waitcnt vmcnt(9)\n\tv_smfmac_f32_32x32x32_bf16 %[o0], v[96:99], v[48:55], v128\n\tv_exp_f32 v58, v58\n\tv_exp_f32 v59, v59\n\tv_exp_f32 v60, v60\n\t"
        "v_exp_f32 v61, v61\n\tv_exp_f32 v62, v62\n\tv_exp_f32 v63, v63\n\tv_exp_f32 v64, v64\n\tv_exp_f32 v65, v65\n\tv_smfmac_f32_32x32x32_bf16 %[o0], v[112:115], v[56:63], v128\n\t"
        "v_exp_f32 v66, v66\n\tv_exp_f32 v67, v67\n\tv_exp_f32 v68, v68\n\tv_mfma_f32_32x32x16_bf16 v[48:63], v[84:87], %[q0], 0\n\tv_exp_f32 v69, v69\n\tv_exp_f32 v70, v70\n\t"
        "v_exp_f32 v71, v71\n\tv_exp_f32 v72, v72\n\tv_exp_f32 v73, v73\n\tv_smfmac_f32_32x32x32_bf16 %[o1], v[96:99], v[64:71], v128\n\tv_exp_f32 v74, v74\n\tv_exp_f32 v75, v75\n\t"
        "v_exp_f32 v76, v76\n\tv_exp_f32 v77, v77\n\tv_exp_f32 v78, v78\n\tv_exp_f32 v79, v79\n\tv_exp_f32 v48, v48\n\tv_exp_f32 v49, v49\n\t"
        "v_smfmac_f32_32x32x32_bf16 %[o1], v[112:115], v[72:79], v128\n\tv_exp_f32 v50, v50\n\tv_exp_f32 v51, v51\n\tv_exp_f32 v52, v52\n\t"
        "v_mfma_f32_32x32x16_bf16 v[64:79], v[84:87], %[q1], 0\n\tglobal_load_dwordx4 v[84:87], %[koff], s[40:41] offset:1024 sc1\n\ts_add_u32 s42, s42, 4096\n\ts_addc_u32 s43, s43, 0\n\t"
        "s_mov_b64 exec, %[vm]\n\tglobal_load_dwordx4 v[96:99], %[voff], s[42:43] offset:0 sc1\n\tglobal_load_dwordx4 v[112:115], %[voff], s[42:43] offset:512 sc1\n\ts_mov_b64 exec, -1\n\t"
        "v_exp_f32 v53, v53\n\tv_exp_f32 v54, v54\n\tv_exp_f32 v55, v55\n\tv_exp_f32 v56, v56\n\tv_exp_f32 v57, v57\n\ts_waitcnt vmcnt(9)\n\t"
        "v_smfmac_f32_32x32x32_bf16 %[o0], v[100:103], v[48:55], v128\n\tv_exp_f32 v58, v58\n\tv_exp_f32 v59, v59\n\tv_exp_f32 v60, v60\n\tv_exp_f32 v61, v61\n\tv_exp_f32 v62, v62\n\t"
        "v_exp_f32 v63, v63\n\tv_exp_f32 v64, v64\n\tv_exp_f32 v65, v65\n\tv_smfmac_f32_32x32x32_bf16 %[o0], v[116:119], v[56:63], v128\n\tv_exp_f32 v66, v66\n\tv_exp_f32 v67, v67\n\t"
        "v_exp_f32 v68, v68\n\tv_mfma_f32_32x32x16_bf16 v[48:63], v[88:91], %[q0], 0\n\tv_exp_f32 v69, v69\n\tv_exp_f32 v70, v70\n\tv_exp_f32 v71, v71\n\tv_exp_f32 v72, v72\n\t"
        "v_exp_f32 v73, v73\n\tv_smfmac_f32_32x32x32_bf16 %[o1], v[100:103], v[64:71], v128\n\tv_exp_f32 v74, v74\n\tv_exp_f32 v75, v75\n\tv_exp_f32 v76, v76\n\tv_exp_f32 v77, v77\n\t"
        "v_exp_f32 v78, v78\n\tv_exp_f32 v79, v79\n\tv_exp_f32 v48, v48\n\tv_exp_f32 v49, v49\n\tv_smfmac_f32_32x32x32_bf16 %[o1], v[116:119], v[72:79], v128\n\tv_exp_f32 v50, v50\n\t"
        "v_exp_f32 v51, v51\n\tv_exp_f32 v52, v52\n\tv_mfma_f32_32x32x16_bf16 v[64:79], v[88:91], %[q1], 0\n\tglobal_load_dwordx4 v[88:91], %[koff], s[40:41] offset:2048 sc1\n\t"
        "s_mov_b64 exec, %[vm]\n\tglobal_load_dwordx4 v[100:103], %[voff], s[42:43] offset:1024 sc1\n\tglobal_load_dwordx4 v[116:119], %[voff], s[42:43] offset:1536 sc1\n\t"
        "s_mov_b64 exec, -1\n\tv_exp_f32 v53, v53\n\tv_exp_f32 v54, v54\n\tv_exp_f32 v55, v55\n\tv_exp_f32 v56, v56\n\tv_exp_f32 v57, v57\n\ts_waitcnt vmcnt(9)\n\t"
        "v_smfmac_f32_32x32x32_bf16 %[o0], v[104:107], v[48:55], v128\n\tv_exp_f32 v58, v58\n\tv_exp_f32 v59, v59\n\tv_exp_f32 v60, v60\n\tv_exp_f32 v61, v61\n\tv_exp_f32 v62, v62\n\t"
        "v_exp_f32 v63, v63\n\tv_exp_f32 v64, v64\n\tv_exp_f32 v65, v65\n\tv_smfmac_f32_32x32x32_bf16 %[o0], v[120:123], v[56:63], v128\n\tv_exp_f32 v66, v66\n\tv_exp_f32 v67, v67\n\t"
        "v_exp_f32 v68, v68\n\tv_mfma_f32_32x32x16_bf16 v[48:63], v[92:95], %[q0], 0\n\tv_exp_f32 v69, v69\n\tv_exp_f32 v70, v70\n\tv_exp_f32 v71, v71\n\tv_exp_f32 v72, v72\n\t"
        "v_exp_f32 v73, v73\n\tv_smfmac_f32_32x32x32_bf16 %[o1], v[104:107], v[64:71], v128\n\tv_exp_f32 v74, v74\n\tv_exp_f32 v75, v75\n\tv_exp_f32 v76, v76\n\tv_exp_f32 v77, v77\n\t"
        "v_exp_f32 v78, v78\n\tv_exp_f32 v79, v79\n\tv_exp_f32 v48, v48\n\tv_exp_f32 v49, v49\n\tv_smfmac_f32_32x32x32_bf16 %[o1], v[120:123], v[72:79], v128\n\tv_exp_f32 v50, v50\n\t"
        "v_exp_f32 v51, v51\n\tv_exp_f32 v52, v52\n\tv_mfma_f32_32x32x16_bf16 v[64:79], v[92:95], %[q1], 0\n\tglobal_load_dwordx4 v[92:95], %[koff], s[40:41] offset:3072 sc1\n\t"
        "s_mov_b64 exec, %[vm]\n\tglobal_load_dwordx4 v[104:107], %[voff], s[42:43] offset:2048 sc1\n\tglobal_load_dwordx4 v[120:123], %[voff], s[42:43] offset:2560 sc1\n\t"
        "s_mov_b64 exec, -1\n\tv_exp_f32 v53, v53\n\tv_exp_f32 v54, v54\n\tv_exp_f32 v55, v55\n\tv_exp_f32 v56, v56\n\tv_exp_f32 v57, v57\n\ts_waitcnt vmcnt(9)\n\t"
        "v_smfmac_f32_32x32x32_bf16 %[o0], v[108:111], v[48:55], v128\n\tv_exp_f32 v58, v58\n\tv_exp_f32 v59, v59\n\tv_exp_f32 v60, v60\n\tv_exp_f32 v61, v61\n\tv_exp_f32 v62, v62\n\t"
        "v_exp_f32 v63, v63\n\tv_exp_f32 v64, v64\n\tv_exp_f32 v65, v65\n\tv_smfmac_f32_32x32x32_bf16 %[o0], v[124:127], v[56:63], v128\n\tv_exp_f32 v66, v66\n\tv_exp_f32 v67, v67\n\t"
        "v_exp_f32 v68, v68\n\tv_mfma_f32_32x32x16_bf16 v[48:63], v[80:83], %[q0], 0\n\tglobal_load_dword v253, v[254:255], off\n\tv_exp_f32 v69, v69\n\tv_exp_f32 v70, v70\n\t"
        "v_exp_f32 v71, v71\n\tv_exp_f32 v72, v72\n\tv_exp_f32 v73, v73\n\tv_smfmac_f32_32x32x32_bf16 %[o1], v[108:111], v[64:71], v128\n\tv_exp_f32 v74, v74\n\tv_exp_f32 v75, v75\n\t"
        "v_exp_f32 v76, v76\n\tv_exp_f32 v77, v77\n\tv_exp_f32 v78, v78\n\tv_exp_f32 v79, v79\n\tv_exp_f32 v48, v48\n\tv_exp_f32 v49, v49\n\t"
        "v_smfmac_f32_32x32x32_bf16 %[o1], v[124:127], v[72:79], v128\n\tv_exp_f32 v50, v50\n\tv_exp_f32 v51, v51\n\tv_exp_f32 v52, v52\n\t"
        "v_mfma_f32_32x32x16_bf16 v[64:79], v[80:83], %[q1], 0\n\ts_add_u32 s40, s40, 4096\n\ts_addc_u32 s41, s41, 0\n\tglobal_load_dwordx4 v[80:83], %[koff], s[40:41] offset:0 sc1\n\t"
        "s_mov_b64 exec, %[vm]\n\tglobal_load_dwordx4 v[108:111], %[voff], s[42:43] offset:3072 sc1\n\tglobal_load_dwordx4 v[124:127], %[voff], s[42:43] offset:3584 sc1\n\t"
        "s_mov_b64 exec, -1\n\tv_exp_f32 v53, v53\n\tv_exp_f32 v54, v54\n\tv_exp_f32 v55, v55\n\tv_exp_f32 v56, v56\n\tv_exp_f32 v57, v57\n\ts_waitcnt vmcnt(10)\n\t"
        "v_smfmac_f32_32x32x32_bf16 %[o0], v[96:99], v[48:55], v128\n\tv_exp_f32 v58, v58\n\tv_exp_f32 v59, v59\n\tv_exp_f32 v60, v60\n\tv_exp_f32 v61, v61\n\tv_exp_f32 v62, v62\n\t"
        "v_exp_f32 v63, v63\n\tv_exp_f32 v64, v64\n\tv_exp_f32 v65, v65\n\tv_smfmac_f32_32x32x32_bf16 %[o0], v[112:115], v[56:63], v128\n\tv_exp_f32 v66, v66\n\tv_exp_f32 v67, v67\n\t"
        "v_exp_f32 v68, v68\n\tv_mfma_f32_32x32x16_bf16 v[48:63], v[84:87], %[q0], 0\n\tv_exp_f32 v69, v69\n\tv_exp_f32 v70, v70\n\tv_exp_f32 v71, v71\n\tv_exp_f32 v72, v72\n\t"
        "v_exp_f32 v73, v73\n\tv_smfmac_f32_32x32x32_bf16 %[o1], v[96:99], v[64:71], v128\n\tv_exp_f32 v74, v74\n\tv_exp_f32 v75, v75\n\tv_exp_f32 v76, v76\n\tv_exp_f32 v77, v77\n\t"
        "v_exp_f32 v78, v78\n\tv_exp_f32 v79, v79\n\tv_exp_f32 v48, v48\n\tv_exp_f32 v49, v49\n\tv_smfmac_f32_32x32x32_bf16 %[o1], v[112:115], v[72:79], v128\n\tv_exp_f32 v50, v50\n\t"
        "v_exp_f32 v51, v51\n\tv_exp_f32 v52, v52\n\tv_mfma_f32_32x32x16_bf16 v[64:79], v[84:87], %[q1], 0\n\tglobal_load_dwordx4 v[84:87], %[koff], s[40:41] offset:1024 sc1\n\t"
        "s_add_u32 s42, s42, 4096\n\ts_addc_u32 s43, s43, 0\n\ts_mov_b64 exec, %[vm]\n\tglobal_load_dwordx4 v[96:99], %[voff], s[42:43] offset:0 sc1\n\t"
        "global_load_dwordx4 v[112:115], %[voff], s[42:43] offset:512 sc1\n\ts_mov_b64 exec, -1\n\tv_exp_f32 v53, v53\n\tv_exp_f32 v54, v54\n\tv_exp_f32 v55, v55\n\tv_exp_f32 v56, v56\n\t"
        "v_exp_f32 v57, v57\n\ts_waitcnt vmcnt(10)\n\tv_smfmac_f32_32x32x32_bf16 %[o0], v[100:103], v[48:55], v128\n\tv_exp_f32 v58, v58\n\tv_exp_f32 v59, v59\n\tv_exp_f32 v60, v60\n\t"
        "v_exp_f32 v61, v61\n\tv_exp_f32 v62, v62\n\tv_exp_f32 v63, v63\n\tv_exp_f32 v64, v64\n\tv_exp_f32 v65, v65\n\tv_smfmac_f32_32x32x32_bf16 %[o0], v[116:119], v[56:63], v128\n\t"
        "v_exp_f32 v66, v66\n\tv_exp_f32 v67, v67\n\tv_exp_f32 v68, v68\n\tv_mfma_f32_32x32x16_bf16 v[48:63], v[88:91], %[q0], 0\n\tv_exp_f32 v69, v69\n\tv_exp_f32 v70, v70\n\t"
        "v_exp_f32 v71, v71\n\tv_exp_f32 v72, v72\n\tv_exp_f32 v73, v73\n\tv_smfmac_f32_32x32x32_bf16 %[o1], v[100:103], v[64:71], v128\n\tv_exp_f32 v74, v74\n\tv_exp_f32 v75, v75\n\t"
        "v_exp_f32 v76, v76\n\tv_exp_f32 v77, v77\n\tv_exp_f32 v78, v78\n\tv_exp_f32 v79, v79\n\tv_exp_f32 v48, v48\n\tv_exp_f32 v49, v49\n\t"
        "v_smfmac_f32_32x32x32_bf16 %[o1], v[116:119], v[72:79], v128\n\tv_exp_f32 v50, v50\n\tv_exp_f32 v51, v51\n\tv_exp_f32 v52, v52\n\t"
        "v_mfma_f32_32x32x16_bf16 v[64:79], v[88:91], %[q1], 0\n\tglobal_load_dwordx4 v[88:91], %[koff], s[40:41] offset:2048 sc1\n\ts_mov_b64 exec, %[vm]\n\t"
        "global_load_dwordx4 v[100:103], %[voff], s[42:43] offset:1024 sc1\n\tglobal_load_dwordx4 v[116:119], %[voff], s[42:43] offset:1536 sc1\n\ts_mov_b64 exec, -1\n\t"
        "v_exp_f32 v53, v53\n\tv_exp_f32 v54, v54\n\tv_exp_f32 v55, v55\n\tv_exp_f32 v56, v56\n\tv_exp_f32 v57, v57\n\ts_waitcnt vmcnt(10)\n\t"
        "v_smfmac_f32_32x32x32_bf16 %[o0], v[104:107], v[48:55], v128\n\tv_exp_f32 v58, v58\n\tv_exp_f32 v59, v59\n\tv_exp_f32 v60, v60\n\tv_exp_f32 v61, v61\n\tv_exp_f32 v62, v62\n\t"
        "v_exp_f32 v63, v63\n\tv_exp_f32 v64, v64\n\tv_exp_f32 v65, v65\n\tv_smfmac_f32_32x32x32_bf16 %[o0], v[120:123], v[56:63], v128\n\tv_exp_f32 v66, v66\n\tv_exp_f32 v67, v67\n\t"
        "v_exp_f32 v68, v68\n\tv_mfma_f32_32x32x16_bf16 v[48:63], v[92:95], %[q0], 0\n\tv_exp_f32 v69, v69\n\tv_exp_f32 v70, v70\n\tv_exp_f32 v71, v71\n\tv_exp_f32 v72, v72\n\t"
        "v_exp_f32 v73, v73\n\tv_smfmac_f32_32x32x32_bf16 %[o1], v[104:107], v[64:71], v128\n\tv_exp_f32 v74, v74\n\tv_exp_f32 v75, v75\n\tv_exp_f32 v76, v76\n\tv_exp_f32 v77, v77\n\t"
        "v_exp_f32 v78, v78\n\tv_exp_f32 v79, v79\n\tv_exp_f32 v48, v48\n\tv_exp_f32 v49, v49\n\tv_smfmac_f32_32x32x32_bf16 %[o1], v[120:123], v[72:79], v128\n\tv_exp_f32 v50, v50\n\t"
        "v_exp_f32 v51, v51\n\tv_exp_f32 v52, v52\n\tv_mfma_f32_32x32x16_bf16 v[64:79], v[92:95], %[q1], 0\n\tglobal_load_dwordx4 v[92:95], %[koff], s[40:41] offset:3072 sc1\n\t"
        "s_mov_b64 exec, %[vm]\n\tglobal_load_dwordx4 v[104:107], %[voff], s[42:43] offset:2048 sc1\n\tglobal_load_dwordx4 v[120:123], %[voff], s[42:43] offset:2560 sc1\n\t"
        "s_mov_b64 exec, -1\n\tv_exp_f32 v53, v53\n\tv_exp_f32 v54, v54\n\tv_exp_f32 v55, v55\n\tv_exp_f32 v56, v56\n\tv_exp_f32 v57, v57\n\ts_waitcnt vmcnt(9)\n\t"
        "v_smfmac_f32_32x32x32_bf16 %[o0], v[108:111], v[48:55], v128\n\tv_exp_f32 v58, v58\n\tv_exp_f32 v59, v59\n\tv_exp_f32 v60, v60\n\tv_exp_f32 v61, v61\n\tv_exp_f32 v62, v62\n\t"
        "v_exp_f32 v63, v63\n\tv_exp_f32 v64, v64\n\tv_exp_f32 v65, v65\n\tv_smfmac_f32_32x32x32_bf16 %[o0], v[124:127], v[56:63], v128\n\tv_exp_f32 v66, v66\n\tv_exp_f32 v67, v67\n\t"
        "v_exp_f32 v68, v68\n\tv_mfma_f32_32x32x16_bf16 v[48:63], v[80:83], %[q0], 0\n\tv_exp_f32 v69, v69\n\tv_exp_f32 v70, v70\n\tv_exp_f32 v71, v71\n\tv_exp_f32 v72, v72\n\t"
        "v_exp_f32 v73, v73\n\tv_smfmac_f32_32x32x32_bf16 %[o1], v[108:111], v[64:71], v128\n\tv_exp_f32 v74, v74\n\tv_exp_f32 v75, v75\n\tv_exp_f32 v76, v76\n\tv_exp_f32 v77, v77\n\t"
        "v_exp_f32 v78, v78\n\tv_exp_f32 v79, v79\n\tv_exp_f32 v48, v48\n\tv_exp_f32 v49, v49\n\tv_smfmac_f32_32x32x32_bf16 %[o1], v[124:127], v[72:79], v128\n\tv_exp_f32 v50, v50\n\t"
        "v_exp_f32 v51, v51\n\tv_exp_f32 v52, v52\n\tv_mfma_f32_32x32x16_bf16 v[64:79], v[80:83], %[q1], 0\n\ts_add_u32 s40, s40, 4096\n\ts_addc_u32 s41, s41, 0\n\t"
        "global_load_dwordx4 v[80:83], %[koff], s[40:41] offset:0 sc1\n\ts_mov_b64 exec, %[vm]\n\tglobal_load_dwordx4 v[108:111], %[voff], s[42:43] offset:3072 sc1\n\t"
        "global_load_dwordx4 v[124:127], %[voff], s[42:43] offset:3584 sc1\n\ts_mov_b64 exec, -1\n\tv_exp_f32 v53, v53\n\tv_exp_f32 v54, v54\n\tv_exp_f32 v55, v55\n\tv_exp_f32 v56, v56\n\t"
        "v_exp_f32 v57, v57\n\ts_waitcnt vmcnt(9)\n\tv_smfmac_f32_32x32x32_bf16 %[o0], v[96:99], v[48:55], v128\n\tv_exp_f32 v58, v58\n\tv_exp_f32 v59, v59\n\tv_exp_f32 v60, v60\n\t"
        "v_exp_f32 v61, v61\n\tv_exp_f32 v62, v62\n\tv_exp_f32 v63, v63\n\tv_exp_f32 v64, v64\n\tv_exp_f32 v65, v65\n\tv_smfmac_f32_32x32x32_bf16 %[o0], v[112:115], v[56:63], v128\n\t"
        "v_exp_f32 v66, v66\n\tv_exp_f32 v67, v67\n\tv_exp_f32 v68, v68\n\tv_mfma_f32_32x32x16_bf16 v[48:63], v[84:87], %[q0], 0\n\tglobal_load_dwordx4 v[144:147], %[a0], s[46:47]\n\t"
        "v_exp_f32 v69, v69\n\tv_exp_f32 v70, v70\n\tv_exp_f32 v71, v71\n\tv_exp_f32 v72, v72\n\tv_exp_f32 v73, v73\n\tv_smfmac_f32_32x32x32_bf16 %[o1], v[96:99], v[64:71], v128\n\t"
        "v_exp_f32 v74, v74\n\tv_exp_f32 v75, v75\n\tv_exp_f32 v76, v76\n\tv_exp_f32 v77, v77\n\tv_exp_f32 v78, v78\n\tv_exp_f32 v79, v79\n\tv_exp_f32 v48, v48\n\tv_exp_f32 v49, v49\n\t"
        "v_smfmac_f32_32x32x32_bf16 %[o1], v[112:115], v[72:79], v128\n\tv_exp_f32 v50, v50\n\tv_exp_f32 v51, v51\n\tv_exp_f32 v52, v52\n\t"
        "v_mfma_f32_32x32x16_bf16 v[64:79], v[84:87], %[q1], 0\n\tglobal_load_dwordx4 v[84:87], %[koff], s[40:41] offset:1024 sc1\n\ts_add_u32 s42, s42, 4096\n\ts_addc_u32 s43, s43, 0\n\t"
        "s_mov_b64 exec, %[vm]\n\tglobal_load_dwordx4 v[96:99], %[voff], s[42:43] offset:0 sc1\n\tglobal_load_dwordx4 v[112:115], %[voff], s[42:43] offset:512 sc1\n\ts_mov_b64 exec, -1\n\t"
        "v_exp_f32 v53, v53\n\tv_exp_f32 v54, v54\n\tv_exp_f32 v55, v55\n\tv_exp_f32 v56, v56\n\tv_exp_f32 v57, v57\n\ts_waitcnt vmcnt(10)\n\t"
        "v_smfmac_f32_32x32x32_bf16 %[o0], v[100:103], v[48:55], v128\n\tv_exp_f32 v58, v58\n\tv_exp_f32 v59, v59\n\tv_exp_f32 v60, v60\n\tv_exp_f32 v61, v61\n\tv_exp_f32 v62, v62\n\t"
        "v_exp_f32 v63, v63\n\tv_exp_f32 v64, v64\n\tv_exp_f32 v65, v65\n\tv_smfmac_f32_32x32x32_bf16 %[o0], v[116:119], v[56:63], v128\n\tv_exp_f32 v66, v66\n\tv_exp_f32 v67, v67\n\t"
        "v_exp_f32 v68, v68\n\tv_mfma_f32_32x32x16_bf16 v[48:63], v[88:91], %[q0], 0\n\ts_add_u32 m0, %[ol], 0\n\ts_add_u32 s50, s50, 8192\n\ts_addc_u32 s51, s51, 0\n\t"
        "global_load_lds_dwordx4 %[vw], s[50:51]\n\tglobal_load_dwordx4 v[148:151], %[a1], s[46:47]\n\tv_exp_f32 v69, v69\n\tv_exp_f32 v70, v70\n\tv_exp_f32 v71, v71\n\t"
        "v_exp_f32 v72, v72\n\tv_exp_f32 v73, v73\n\tv_smfmac_f32_32x32x32_bf16 %[o1], v[100:103], v[64:71], v128\n\tv_exp_f32 v74, v74\n\tv_exp_f32 v75, v75\n\tv_exp_f32 v76, v76\n\t"
        "v_exp_f32 v77, v77\n\tv_exp_f32 v78, v78\n\tv_exp_f32 v79, v79\n\tv_exp_f32 v48, v48\n\tv_exp_f32 v49, v49\n\tv_smfmac_f32_32x32x32_bf16 %[o1], v[116:119], v[72:79], v128\n\t"
        "v_exp_f32 v50, v50\n\tv_exp_f32 v51, v51\n\tv_exp_f32 v52, v52\n\tv_mfma_f32_32x32x16_bf16 v[64:79], v[88:91], %[q1], 0\n\t"
        "global_load_dwordx4 v[88:91], %[koff], s[40:41] offset:2048 sc1\n\ts_mov_b64 exec, %[vm]\n\tglobal_load_dwordx4 v[100:103], %[voff], s[42:43] offset:1024 sc1\n\t"
        "global_load_dwordx4 v[116:119], %[voff], s[42:43] offset:1536 sc1\n\ts_mov_b64 exec, -1\n\tv_exp_f32 v53, v53\n\tv_exp_f32 v54, v54\n\tv_exp_f32 v55, v55\n\tv_exp_f32 v56, v56\n\t"
        "v_exp_f32 v57, v57\n\ts_waitcnt vmcnt(12)\n\tv_smfmac_f32_32x32x32_bf16 %[o0], v[104:107], v[48:55], v128\n\tv_exp_f32 v58, v58\n\tv_exp_f32 v59, v59\n\tv_exp_f32 v60, v60\n\t"
        "v_exp_f32 v61, v61\n\tv_exp_f32 v62, v62\n\tv_exp_f32 v63, v63\n\tv_exp_f32 v64, v64\n\tv_exp_f32 v65, v65\n\tv_smfmac_f32_32x32x32_bf16 %[o0], v[120:123], v[56:63], v128\n\t"
        "v_exp_f32 v66, v66\n\tv_exp_f32 v67, v67\n\tv_exp_f32 v68, v68\n\tv_mfma_f32_32x32x16_bf16 v[48:63], v[92:95], %[q0], 0\n\tglobal_load_dwordx4 v[152:155], %[a2], s[46:47]\n\t"
        "s_add_u32 s46, s46, %[ga]\n\ts_addc_u32 s47, s47, 0\n\ts_add_u32 m0, %[ol], 1024\n\ts_add_u32 s50, s50, 8192\n\ts_addc_u32 s51, s51, 0\n\t"
        "global_load_lds_dwordx4 %[vw], s[50:51]\n\tv_exp_f32 v69, v69\n\tv_exp_f32 v70, v70\n\tv_exp_f32 v71, v71\n\tv_exp_f32 v72, v72\n\tv_exp_f32 v73, v73\n\t"
        "v_smfmac_f32_32x32x32_bf16 %[o1], v[104:107], v[64:71], v128\n\tv_exp_f32 v74, v74\n\tv_exp_f32 v75, v75\n\tv_exp_f32 v76, v76\n\tv_exp_f32 v77, v77\n\tv_exp_f32 v78, v78\n\t"
        "v_exp_f32 v79, v79\n\tv_exp_f32 v48, v48\n\tv_exp_f32 v49, v49\n\tv_smfmac_f32_32x32x32_bf16 %[o1], v[120:123], v[72:79], v128\n\tv_exp_f32 v50, v50\n\tv_exp_f32 v51, v51\n\t"
        "v_exp_f32 v52, v52\n\tv_mfma_f32_32x32x16_bf16 v[64:79], v[92:95], %[q1], 0\n\tglobal_load_dwordx4 v[92:95], %[koff], s[40:41] offset:3072 sc1\n\ts_mov_b64 exec, %[vm]\n\t"
        "global_load_dwordx4 v[104:107], %[voff], s[42:43] offset:2048 sc1\n\tglobal_load_dwordx4 v[120:123], %[voff], s[42:43] offset:2560 sc1\n\ts_mov_b64 exec, -1\n\t"
        "v_exp_f32 v53, v53\n\tv_exp_f32 v54, v54\n\tv_exp_f32 v55, v55\n\tv_exp_f32 v56, v56\n\tv_exp_f32 v57, v57\n\ts_waitcnt vmcnt(14)\n\t"
        "v_smfmac_f32_32x32x32_bf16 %[o0], v[108:111], v[48:55], v128\n\tv_exp_f32 v58, v58\n\tv_exp_f32 v59, v59\n\tv_exp_f32 v60, v60\n\tv_exp_f32 v61, v61\n\tv_exp_f32 v62, v62\n\t"
        "v_exp_f32 v63, v63\n\tv_exp_f32 v64, v64\n\tv_exp_f32 v65, v65\n\tv_smfmac_f32_32x32x32_bf16 %[o0], v[124:127], v[56:63], v128\n\tv_exp_f32 v66, v66\n\tv_exp_f32 v67, v67\n\t"
        "v_exp_f32 v68, v68\n\tv_mfma_f32_32x32x16_bf16 v[48:63], v[80:83], %[q0], 0\n\tglobal_load_dwordx4 v[156:159], %[a0], s[46:47]\n\t"
        "global_load_dwordx4 v[160:163], %[a1], s[46:47]\n\tv_exp_f32 v69, v69\n\tv_exp_f32 v70, v70\n\tv_exp_f32 v71, v71\n\tv_exp_f32 v72, v72\n\tv_exp_f32 v73, v73\n\t"
        "v_smfmac_f32_32x32x32_bf16 %[o1], v[108:111], v[64:71], v128\n\tv_exp_f32 v74, v74\n\tv_exp_f32 v75, v75\n\tv_exp_f32 v76, v76\n\tv_exp_f32 v77, v77\n\tv_exp_f32 v78, v78\n\t"
        "v_exp_f32 v79, v79\n\tv_exp_f32 v48, v48\n\tv_exp_f32 v49, v49\n\tv_smfmac_f32_32x32x32_bf16 %[o1], v[124:127], v[72:79], v128\n\tv_exp_f32 v50, v50\n\tv_exp_f32 v51, v51\n\t"
        "v_exp_f32 v52, v52\n\tv_mfma_f32_32x32x16_bf16 v[64:79], v[80:83], %[q1], 0\n\ts_add_u32 s40, s40, 4096\n\ts_addc_u32 s41, s41, 0\n\t"
        "global_load_dwordx4 v[80:83], %[koff], s[40:41] offset:0 sc1\n\ts_mov_b64 exec, %[vm]\n\tglobal_load_dwordx4 v[108:111], %[voff], s[42:43] offset:3072 sc1\n\t"
        "global_load_dwordx4 v[124:127], %[voff], s[42:43] offset:3584 sc1\n\ts_mov_b64 exec, -1\n\tv_exp_f32 v53, v53\n\tv_exp_f32 v54, v54\n\tv_exp_f32 v55, v55\n\tv_exp_f32 v56, v56\n\t"
        "v_exp_f32 v57, v57\n\ts_waitcnt vmcnt(15)\n\tv_smfmac_f32_32x32x32_bf16 %[o0], v[96:99], v[48:55], v128\n\tv_exp_f32 v58, v58\n\tv_exp_f32 v59, v59\n\tv_exp_f32 v60, v60\n\t"
        "v_exp_f32 v61, v61\n\tv_exp_f32 v62, v62\n\tv_exp_f32 v63, v63\n\tv_exp_f32 v64, v64\n\tv_exp_f32 v65, v65\n\tv_smfmac_f32_32x32x32_bf16 %[o0], v[112:115], v[56:63], v128\n\t"
        "v_exp_f32 v66, v66\n\tv_exp_f32 v67, v67\n\tv_exp_f32 v68, v68\n\tv_mfma_f32_32x32x16_bf16 v[48:63], v[84:87], %[q0], 0\n\ts_add_u32 m0, %[ol], 2048\n\ts_add_u32 s50, s50, 8192\n\t"
        "s_addc_u32 s51, s51, 0\n\tglobal_load_lds_dwordx4 %[vw], s[50:51]\n\tglobal_load_dwordx4 v[164:167], %[a2], s[46:47]\n\ts_add_u32 s46, s46, %[ga]\n\ts_addc_u32 s47, s47, 0\n\t"
        "v_exp_f32 v69, v69\n\tv_exp_f32 v70, v70\n\tv_exp_f32 v71, v71\n\tv_exp_f32 v72, v72\n\tv_exp_f32 v73, v73\n\tv_smfmac_f32_32x32x32_bf16 %[o1], v[96:99], v[64:71], v128\n\t"
        "v_exp_f32 v74, v74\n\tv_exp_f32 v75, v75\n\tv_exp_f32 v76, v76\n\tv_exp_f32 v77, v77\n\tv_exp_f32 v78, v78\n\tv_exp_f32 v79, v79\n\tv_exp_f32 v48, v48\n\tv_exp_f32 v49, v49\n\t"
        "v_smfmac_f32_32x32x32_bf16 %[o1], v[112:115], v[72:79], v128\n\tv_exp_f32 v50, v50\n\tv_exp_f32 v51, v51\n\tv_exp_f32 v52, v52\n\t"
        "v_mfma_f32_32x32x16_bf16 v[64:79], v[84:87], %[q1], 0\n\tglobal_load_dwordx4 v[84:87], %[koff], s[40:41] offset:1024 sc1\n\ts_add_u32 s42, s42, 4096\n\ts_addc_u32 s43, s43, 0\n\t"
        "s_mov_b64 exec, %[vm]\n\tglobal_load_dwordx4 v[96:99], %[voff], s[42:43] offset:0 sc1\n\tglobal_load_dwordx4 v[112:115], %[voff], s[42:43] offset:512 sc1\n\ts_mov_b64 exec, -1\n\t"
        "v_exp_f32 v53, v53\n\tv_exp_f32 v54, v54\n\tv_exp_f32 v55, v55\n\tv_exp_f32 v56, v56\n\tv_exp_f32 v57, v57\n\ts_waitcnt vmcnt(15)\n\t"
        "v_smfmac_f32_32x32x32_bf16 %[o0], v[100:103], v[48:55], v128\n\tv_exp_f32 v58, v58\n\tv_exp_f32 v59, v59\n\tv_exp_f32 v60, v60\n\tv_exp_f32 v61, v61\n\tv_exp_f32 v62, v62\n\t"
        "v_exp_f32 v63, v63\n\tv_exp_f32 v64, v64\n\tv_exp_f32 v65, v65\n\tv_smfmac_f32_32x32x32_bf16 %[o0], v[116:119], v[56:63], v128\n\tv_exp_f32 v66, v66\n\tv_exp_f32 v67, v67\n\t"
        "v_exp_f32 v68, v68\n\tv_mfma_f32_32x32x16_bf16 v[48:63], v[88:91], %[q0], 0\n\tglobal_load_dwordx4 v[168:171], %[a0], s[46:47]\n\ts_add_u32 m0, %[ol], 3072\n\t"
        "s_add_u32 s50, s50, 8192\n\ts_addc_u32 s51, s51, 0\n\tglobal_load_lds_dwordx4 %[vw], s[50:51]\n\tv_exp_f32 v69, v69\n\tv_exp_f32 v70, v70\n\tv_exp_f32 v71, v71\n\t"
        "v_exp_f32 v72, v72\n\tv_exp_f32 v73, v73\n\tv_smfmac_f32_32x32x32_bf16 %[o1], v[100:103], v[64:71], v128\n\tv_exp_f32 v74, v74\n\tv_exp_f32 v75, v75\n\tv_exp_f32 v76, v76\n\t"
        "v_exp_f32 v77, v77\n\tv_exp_f32 v78, v78\n\tv_exp_f32 v79, v79\n\tv_exp_f32 v48, v48\n\tv_exp_f32 v49, v49\n\tv_smfmac_f32_32x32x32_bf16 %[o1], v[116:119], v[72:79], v128\n\t"
        "v_exp_f32 v50, v50\n\tv_exp_f32 v51, v51\n\tv_exp_f32 v52, v52\n\tv_mfma_f32_32x32x16_bf16 v[64:79], v[88:91], %[q1], 0\n\t"
        "global_load_dwordx4 v[88:91], %[koff], s[40:41] offset:2048 sc1\n\ts_mov_b64 exec, %[vm]\n\tglobal_load_dwordx4 v[100:103], %[voff], s[42:43] offset:1024 sc1\n\t"
        "global_load_dwordx4 v[116:119], %[voff], s[42:43] offset:1536 sc1\n\ts_mov_b64 exec, -1\n\tv_exp_f32 v53, v53\n\tv_exp_f32 v54, v54\n\tv_exp_f32 v55, v55\n\tv_exp_f32 v56, v56\n\t"
        "v_exp_f32 v57, v57\n\ts_waitcnt vmcnt(15)\n\tv_smfmac_f32_32x32x32_bf16 %[o0], v[104:107], v[48:55], v128\n\tv_exp_f32 v58, v58\n\tv_exp_f32 v59, v59\n\tv_exp_f32 v60, v60\n\t"
        "v_exp_f32 v61, v61\n\tv_exp_f32 v62, v62\n\tv_exp_f32 v63, v63\n\tv_exp_f32 v64, v64\n\tv_exp_f32 v65, v65\n\tv_smfmac_f32_32x32x32_bf16 %[o0], v[120:123], v[56:63], v128\n\t"
        "v_exp_f32 v66, v66\n\tv_exp_f32 v67, v67\n\tv_exp_f32 v68, v68\n\tv_mfma_f32_32x32x16_bf16 v[48:63], v[92:95], %[q0], 0\n\tglobal_load_dwordx4 v[172:175], %[a1], s[46:47]\n\t"
        "global_load_dwordx4 v[176:179], %[a2], s[46:47]\n\ts_add_u32 s46, s46, %[ga]\n\ts_addc_u32 s47, s47, 0\n\tv_exp_f32 v69, v69\n\tv_exp_f32 v70, v70\n\tv_exp_f32 v71, v71\n\t"
        "v_exp_f32 v72, v72\n\tv_exp_f32 v73, v73\n\tv_smfmac_f32_32x32x32_bf16 %[o1], v[104:107], v[64:71], v128\n\tv_exp_f32 v74, v74\n\tv_exp_f32 v75, v75\n\tv_exp_f32 v76, v76\n\t"
        "v_exp_f32 v77, v77\n\tv_exp_f32 v78, v78\n\tv_exp_f32 v79, v79\n\tv_exp_f32 v48, v48\n\tv_exp_f32 v49, v49\n\tv_smfmac_f32_32x32x32_bf16 %[o1], v[120:123], v[72:79], v128\n\t"
        "v_exp_f32 v50, v50\n\tv_exp_f32 v51, v51\n\tv_exp_f32 v52, v52\n\tv_mfma_f32_32x32x16_bf16 v[64:79], v[92:95], %[q1], 0\n\t"
        "global_load_dwordx4 v[92:95], %[koff], s[40:41] offset:3072 sc1\n\ts_mov_b64 exec, %[vm]\n\tglobal_load_dwordx4 v[104:107], %[voff], s[42:43] offset:2048 sc1\n\t"
        "global_load_dwordx4 v[120:123], %[voff], s[42:43] offset:2560 sc1\n\ts_mov_b64 exec, -1\n\tv_exp_f32 v53, v53\n\tv_exp_f32 v54, v54\n\tv_exp_f32 v55, v55\n\tv_exp_f32 v56, v56\n\t"
        "v_exp_f32 v57, v57\n\ts_waitcnt vmcnt(15)\n\tv_smfmac_f32_32x32x32_bf16 %[o0], v[108:111], v[48:55], v128\n\tv_exp_f32 v58, v58\n\tv_exp_f32 v59, v59\n\tv_exp_f32 v60, v60\n\t"
        "v_exp_f32 v61, v61\n\tv_exp_f32 v62, v62\n\tv_exp_f32 v63, v63\n\tv_exp_f32 v64, v64\n\tv_exp_f32 v65, v65\n\tv_smfmac_f32_32x32x32_bf16 %[o0], v[124:127], v[56:63], v128\n\t"
        "v_exp_f32 v66, v66\n\tv_exp_f32 v67, v67\n\tv_exp_f32 v68, v68\n\tv_mfma_f32_32x32x16_bf16 v[48:63], v[80:83], %[q0], 0\n\ts_add_u32 m0, %[ol], 4096\n\ts_add_u32 s50, s50, 8192\n\t"
        "s_addc_u32 s51, s51, 0\n\tglobal_load_lds_dwordx4 %[vw], s[50:51]\n\tglobal_load_dwordx4 v[180:183], %[a0], s[46:47]\n\tv_exp_f32 v69, v69\n\tv_exp_f32 v70, v70\n\t"
        "v_exp_f32 v71, v71\n\tv_exp_f32 v72, v72\n\tv_exp_f32 v73, v73\n\tv_smfmac_f32_32x32x32_bf16 %[o1], v[108:111], v[64:71], v128\n\tv_exp_f32 v74, v74\n\tv_exp_f32 v75, v75\n\t"
        "v_exp_f32 v76, v76\n\tv_exp_f32 v77, v77\n\tv_exp_f32 v78, v78\n\tv_exp_f32 v79, v79\n\tv_exp_f32 v48, v48\n\tv_exp_f32 v49, v49\n\t"
        "v_smfmac_f32_32x32x32_bf16 %[o1], v[124:127], v[72:79], v128\n\tv_exp_f32 v50, v50\n\tv_exp_f32 v51, v51\n\tv_exp_f32 v52, v52\n\t"
        "v_mfma_f32_32x32x16_bf16 v[64:79], v[80:83], %[q1], 0\n\ts_add_u32 s40, s40, 4096\n\ts_addc_u32 s41, s41, 0\n\tglobal_load_dwordx4 v[80:83], %[koff], s[40:41] offset:0 sc1\n\t"
        "s_mov_b64 exec, %[vm]\n\tglobal_load_dwordx4 v[108:111], %[voff], s[42:43] offset:3072 sc1\n\tglobal_load_dwordx4 v[124:127], %[voff], s[42:43] offset:3584 sc1\n\t"
        "s_mov_b64 exec, -1\n\tv_exp_f32 v53, v53\n\tv_exp_f32 v54, v54\n\tv_exp_f32 v55, v55\n\tv_exp_f32 v56, v56\n\tv_exp_f32 v57, v57\n\ts_waitcnt vmcnt(15)\n\t"
        "v_smfmac_f32_32x32x32_bf16 %[o0], v[96:99], v[48:55], v128\n\tv_exp_f32 v58, v58\n\tv_exp_f32 v59, v59\n\tv_exp_f32 v60, v60\n\tv_exp_f32 v61, v61\n\tv_exp_f32 v62, v62\n\t"
        "v_exp_f32 v63, v63\n\tv_exp_f32 v64, v64\n\tv_exp_f32 v65, v65\n\tv_smfmac_f32_32x32x32_bf16 %[o0], v[112:115], v[56:63], v128\n\tv_exp_f32 v66, v66\n\tv_exp_f32 v67, v67\n\t"
        "v_exp_f32 v68, v68\n\tv_mfma_f32_32x32x16_bf16 v[48:63], v[84:87], %[q0], 0\n\tglobal_load_dwordx4 v[184:187], %[a1], s[46:47]\n\ts_add_u32 m0, %[ol], 5120\n\t"
        "s_add_u32 s50, s50, 8192\n\ts_addc_u32 s51, s51, 0\n\tglobal_load_lds_dwordx4 %[vw], s[50:51]\n\tv_exp_f32 v69, v69\n\tv_exp_f32 v70, v70\n\tv_exp_f32 v71, v71\n\t"
        "v_exp_f32 v72, v72\n\tv_exp_f32 v73, v73\n\tv_smfmac_f32_32x32x32_bf16 %[o1], v[96:99], v[64:71], v128\n\tv_exp_f32 v74, v74\n\tv_exp_f32 v75, v75\n\tv_exp_f32 v76, v76\n\t"
        "v_exp_f32 v77, v77\n\tv_exp_f32 v78, v78\n\tv_exp_f32 v79, v79\n\tv_exp_f32 v48, v48\n\tv_exp_f32 v49, v49\n\tv_smfmac_f32_32x32x32_bf16 %[o1], v[112:115], v[72:79], v128\n\t"
        "v_exp_f32 v50, v50\n\tv_exp_f32 v51, v51\n\tv_exp_f32 v52, v52\n\tv_mfma_f32_32x32x16_bf16 v[64:79], v[84:87], %[q1], 0\n\t"
        "global_load_dwordx4 v[84:87], %[koff], s[40:41] offset:1024 sc1\n\ts_add_u32 s42, s42, 4096\n\ts_addc_u32 s43, s43, 0\n\ts_mov_b64 exec, %[vm]\n\t"
        "global_load_dwordx4 v[96:99], %[voff], s[42:43] offset:0 sc1\n\tglobal_load_dwordx4 v[112:115], %[voff], s[42:43] offset:512 sc1\n\ts_mov_b64 exec, -1\n\tv_exp_f32 v53, v53\n\t"
        "v_exp_f32 v54, v54\n\tv_exp_f32 v55, v55\n\tv_exp_f32 v56, v56\n\tv_exp_f32 v57, v57\n\ts_waitcnt vmcnt(15)\n\tv_smfmac_f32_32x32x32_bf16 %[o0], v[100:103], v[48:55], v128\n\t"
        "v_exp_f32 v58, v58\n\tv_exp_f32 v59, v59\n\tv_exp_f32 v60, v60\n\tv_exp_f32 v61, v61\n\tv_exp_f32 v62, v62\n\tv_exp_f32 v63, v63\n\tv_exp_f32 v64, v64\n\tv_exp_f32 v65, v65\n\t"
        "v_smfmac_f32_32x32x32_bf16 %[o0], v[116:119], v[56:63], v128\n\tv_exp_f32 v66, v66\n\tv_exp_f32 v67, v67\n\tv_exp_f32 v68, v68\n\t"
        "v_mfma_f32_32x32x16_bf16 v[48:63], v[88:91], %[q0], 0\n\tglobal_load_dwordx4 v[188:191], %[a2], s[46:47]\n\ts_add_u32 s46, s46, %[ga]\n\ts_addc_u32 s47, s47, 0\n\t"
        "global_load_dwordx4 v[192:195], %[vw], s[48:49]\n\ts_add_u32 s48, s48, 8192\n\ts_addc_u32 s49, s49, 0\n\tv_exp_f32 v69, v69\n\tv_exp_f32 v70, v70\n\tv_exp_f32 v71, v71\n\t"
        "v_exp_f32 v72, v72\n\tv_exp_f32 v73, v73\n\tv_smfmac_f32_32x32x32_bf16 %[o1], v[100:103], v[64:71], v128\n\tv_exp_f32 v74, v74\n\tv_exp_f32 v75, v75\n\tv_exp_f32 v76, v76\n\t"
        "v_exp_f32 v77, v77\n\tv_exp_f32 v78, v78\n\tv_exp_f32 v79, v79\n\tv_exp_f32 v48, v48\n\tv_exp_f32 v49, v49\n\tv_smfmac_f32_32x32x32_bf16 %[o1], v[116:119], v[72:79], v128\n\t"
        "v_exp_f32 v50, v50\n\tv_exp_f32 v51, v51\n\tv_exp_f32 v52, v52\n\tv_mfma_f32_32x32x16_bf16 v[64:79], v[88:91], %[q1], 0\n\t"
        "global_load_dwordx4 v[88:91], %[koff], s[40:41] offset:2048 sc1\n\ts_mov_b64 exec, %[vm]\n\tglobal_load_dwordx4 v[100:103], %[voff], s[42:43] offset:1024 sc1\n\t"
        "global_load_dwordx4 v[116:119], %[voff], s[42:43] offset:1536 sc1\n\ts_mov_b64 exec, -1\n\tv_exp_f32 v53, v53\n\tv_exp_f32 v54, v54\n\tv_exp_f32 v55, v55\n\tv_exp_f32 v56, v56\n\t"
        "v_exp_f32 v57, v57\n\ts_waitcnt vmcnt(15)\n\tv_smfmac_f32_32x32x32_bf16 %[o0], v[104:107], v[48:55], v128\n\tv_exp_f32 v58, v58\n\tv_exp_f32 v59, v59\n\tv_exp_f32 v60, v60\n\t"
        "v_exp_f32 v61, v61\n\tv_exp_f32 v62, v62\n\tv_exp_f32 v63, v63\n\tv_exp_f32 v64, v64\n\tv_exp_f32 v65, v65\n\tv_smfmac_f32_32x32x32_bf16 %[o0], v[120:123], v[56:63], v128\n\t"
        "v_exp_f32 v66, v66\n\tv_exp_f32 v67, v67\n\tv_exp_f32 v68, v68\n\tv_mfma_f32_32x32x16_bf16 v[48:63], v[92:95], %[q0], 0\n\ts_add_u32 m0, %[ol], 6144\n\ts_add_u32 s50, s50, 8192\n\t"
        "s_addc_u32 s51, s51, 0\n\tglobal_load_lds_dwordx4 %[vw], s[50:51]\n\tglobal_load_dwordx4 v[196:199], %[vw], s[48:49]\n\ts_add_u32 s48, s48, 8192\n\ts_addc_u32 s49, s49, 0\n\t"
        "v_exp_f32 v69, v69\n\tv_exp_f32 v70, v70\n\tv_exp_f32 v71, v71\n\tv_exp_f32 v72, v72\n\tv_exp_f32 v73, v73\n\tv_smfmac_f32_32x32x32_bf16 %[o1], v[104:107], v[64:71], v128\n\t"
        "v_exp_f32 v74, v74\n\tv_exp_f32 v75, v75\n\tv_exp_f32 v76, v76\n\tv_exp_f32 v77, v77\n\tv_exp_f32 v78, v78\n\tv_exp_f32 v79, v79\n\tv_exp_f32 v48, v48\n\tv_exp_f32 v49, v49\n\t"
        "v_smfmac_f32_32x32x32_bf16 %[o1], v[120:123], v[72:79], v128\n\tv_exp_f32 v50, v50\n\tv_exp_f32 v51, v51\n\tv_exp_f32 v52, v52\n\t"
        "v_mfma_f32_32x32x16_bf16 v[64:79], v[92:95], %[q1], 0\n\tglobal_load_dwordx4 v[92:95], %[koff], s[40:41] offset:3072 sc1\n\ts_mov_b64 exec, %[vm]\n\t"
        "global_load_dwordx4 v[104:107], %[voff], s[42:43] offset:2048 sc1\n\tglobal_load_dwordx4 v[120:123], %[voff], s[42:43] offset:2560 sc1\n\ts_mov_b64 exec, -1\n\t"
        "v_exp_f32 v53, v53\n\tv_exp_f32 v54, v54\n\tv_exp_f32 v55, v55\n\tv_exp_f32 v56, v56\n\tv_exp_f32 v57, v57\n\ts_waitcnt vmcnt(15)\n\t"
        "v_smfmac_f32_32x32x32_bf16 %[o0], v[108:111], v[48:55], v128\n\tv_exp_f32 v58, v58\n\tv_exp_f32 v59, v59\n\tv_exp_f32 v60, v60\n\tv_exp_f32 v61, v61\n\tv_exp_f32 v62, v62\n\t"
        "v_exp_f32 v63, v63\n\tv_exp_f32 v64, v64\n\tv_exp_f32 v65, v65\n\tv_smfmac_f32_32x32x32_bf16 %[o0], v[124:127], v[56:63], v128\n\tv_exp_f32 v66, v66\n\tv_exp_f32 v67, v67\n\t"
        "v_exp_f32 v68, v68\n\tv_mfma_f32_32x32x16_bf16 v[48:63], v[80:83], %[q0], 0\n\tglobal_load_dwordx4 v[200:203], %[vw], s[48:49]\n\ts_add_u32 s48, s48, 8192\n\t"
        "s_addc_u32 s49, s49, 0\n\tv_exp_f32 v69, v69\n\tv_exp_f32 v70, v70\n\tv_exp_f32 v71, v71\n\tv_exp_f32 v72, v72\n\tv_exp_f32 v73, v73\n\t"
        "v_smfmac_f32_32x32x32_bf16 %[o1], v[108:111], v[64:71], v128\n\tv_exp_f32 v74, v74\n\tv_exp_f32 v75, v75\n\tv_exp_f32 v76, v76\n\tv_exp_f32 v77, v77\n\tv_exp_f32 v78, v78\n\t"
        "v_exp_f32 v79, v79\n\tv_exp_f32 v48, v48\n\tv_exp_f32 v49, v49\n\tv_smfmac_f32_32x32x32_bf16 %[o1], v[124:127], v[72:79], v128\n\tv_exp_f32 v50, v50\n\tv_exp_f32 v51, v51\n\t"
        "v_exp_f32 v52, v52\n\tv_mfma_f32_32x32x16_bf16 v[64:79], v[80:83], %[q1], 0\n\ts_add_u32 s40, s40, 4096\n\ts_addc_u32 s41, s41, 0\n\t"
        "global_load_dwordx4 v[80:83], %[koff], s[40:41] offset:0 sc1\n\ts_mov_b64 exec, %[vm]\n\tglobal_load_dwordx4 v[108:111], %[voff], s[42:43] offset:3072 sc1\n\t"
        "global_load_dwordx4 v[124:127], %[voff], s[42:43] offset:3584 sc1\n\ts_mov_b64 exec, -1\n\tv_exp_f32 v53, v53\n\tv_exp_f32 v54, v54\n\tv_exp_f32 v55, v55\n\tv_exp_f32 v56, v56\n\t"
        "v_exp_f32 v57, v57\n\ts_waitcnt vmcnt(14)\n\tv_smfmac_f32_32x32x32_bf16 %[o0], v[96:99], v[48:55], v128\n\tv_exp_f32 v58, v58\n\tv_exp_f32 v59, v59\n\tv_exp_f32 v60, v60\n\t"
        "v_exp_f32 v61, v61\n\tv_exp_f32 v62, v62\n\tv_exp_f32 v63, v63\n\tv_exp_f32 v64, v64\n\tv_exp_f32 v65, v65\n\tv_smfmac_f32_32x32x32_bf16 %[o0], v[112:115], v[56:63], v128\n\t"
        "v_exp_f32 v66, v66\n\tv_exp_f32 v67, v67\n\tv_exp_f32 v68, v68\n\tv_mfma_f32_32x32x16_bf16 v[48:63], v[84:87], %[q0], 0\n\ts_add_u32 m0, %[ol], 7168\n\ts_add_u32 s50, s50, 8192\n\t"
        "s_addc_u32 s51, s51, 0\n\tglobal_load_lds_dwordx4 %[vw], s[50:51]\n\tglobal_load_dwordx4 v[204:207], %[vw], s[48:49]\n\ts_add_u32 s48, s48, 8192\n\ts_addc_u32 s49, s49, 0\n\t"
        "v_exp_f32 v69, v69\n\tv_exp_f32 v70, v70\n\tv_exp_f32 v71, v71\n\tv_exp_f32 v72, v72\n\tv_exp_f32 v73, v73\n\tv_smfmac_f32_32x32x32_bf16 %[o1], v[96:99], v[64:71], v128\n\t"
        "v_exp_f32 v74, v74\n\tv_exp_f32 v75, v75\n\tv_exp_f32 v76, v76\n\tv_exp_f32 v77, v77\n\tv_exp_f32 v78, v78\n\tv_exp_f32 v79, v79\n\tv_exp_f32 v48, v48\n\tv_exp_f32 v49, v49\n\t"
        "v_smfmac_f32_32x32x32_bf16 %[o1], v[112:115], v[72:79], v128\n\tv_exp_f32 v50, v50\n\tv_exp_f32 v51, v51\n\tv_exp_f32 v52, v52\n\t"
        "v_mfma_f32_32x32x16_bf16 v[64:79], v[84:87], %[q1], 0\n\tglobal_load_dwordx4 v[84:87], %[koff], s[40:41] offset:1024 sc1\n\ts_add_u32 s42, s42, 4096\n\ts_addc_u32 s43, s43, 0\n\t"
        "s_mov_b64 exec, %[vm]\n\tglobal_load_dwordx4 v[96:99], %[voff], s[42:43] offset:0 sc1\n\tglobal_load_dwordx4 v[112:115], %[voff], s[42:43] offset:512 sc1\n\ts_mov_b64 exec, -1\n\t"
        "v_exp_f32 v53, v53\n\tv_exp_f32 v54, v54\n\tv_exp_f32 v55, v55\n\tv_exp_f32 v56, v56\n\tv_exp_f32 v57, v57\n\ts_waitcnt vmcnt(14)\n\t"
        "v_smfmac_f32_32x32x32_bf16 %[o0], v[100:103], v[48:55], v128\n\tv_exp_f32 v58, v58\n\tv_exp_f32 v59, v59\n\tv_exp_f32 v60, v60\n\tv_exp_f32 v61, v61\n\tv_exp_f32 v62, v62\n\t"
        "v_exp_f32 v63, v63\n\tv_exp_f32 v64, v64\n\tv_exp_f32 v65, v65\n\tv_smfmac_f32_32x32x32_bf16 %[o0], v[116:119], v[56:63], v128\n\tv_exp_f32 v66, v66\n\tv_exp_f32 v67, v67\n\t"
        "v_exp_f32 v68, v68\n\tv_mfma_f32_32x32x16_bf16 v[48:63], v[88:91], %[q0], 0\n\tglobal_load_dwordx4 v[208:211], %[vw], s[48:49]\n\ts_add_u32 s48, s48, 8192\n\t"
        "s_addc_u32 s49, s49, 0\n\ts_add_u32 m0, %[hl], 0\n\ts_nop 0\n\tglobal_load_lds_dwordx4 %[b0], s[52:53]\n\tv_exp_f32 v69, v69\n\tv_exp_f32 v70, v70\n\tv_exp_f32 v71, v71\n\t"
        "v_exp_f32 v72, v72\n\tv_exp_f32 v73, v73\n\tv_smfmac_f32_32x32x32_bf16 %[o1], v[100:103], v[64:71], v128\n\tv_exp_f32 v74, v74\n\tv_exp_f32 v75, v75\n\tv_exp_f32 v76, v76\n\t"
        "v_exp_f32 v77, v77\n\tv_exp_f32 v78, v78\n\tv_exp_f32 v79, v79\n\tv_exp_f32 v48, v48\n\tv_exp_f32 v49, v49\n\tv_smfmac_f32_32x32x32_bf16 %[o1], v[116:119], v[72:79], v128\n\t"
        "v_exp_f32 v50, v50\n\tv_exp_f32 v51, v51\n\tv_exp_f32 v52, v52\n\tv_mfma_f32_32x32x16_bf16 v[64:79], v[88:91], %[q1], 0\n\t"
        "global_load_dwordx4 v[88:91], %[koff], s[40:41] offset:2048 sc1\n\ts_mov_b64 exec, %[vm]\n\tglobal_load_dwordx4 v[100:103], %[voff], s[42:43] offset:1024 sc1\n\t"
        "global_load_dwordx4 v[116:119], %[voff], s[42:43] offset:1536 sc1\n\ts_mov_b64 exec, -1\n\tv_exp_f32 v53, v53\n\tv_exp_f32 v54, v54\n\tv_exp_f32 v55, v55\n\tv_exp_f32 v56, v56\n\t"
        "v_exp_f32 v57, v57\n\ts_waitcnt vmcnt(14)\n\tv_smfmac_f32_32x32x32_bf16 %[o0], v[104:107], v[48:55], v128\n\tv_exp_f32 v58, v58\n\tv_exp_f32 v59, v59\n\tv_exp_f32 v60, v60\n\t"
        "v_exp_f32 v61, v61\n\tv_exp_f32 v62, v62\n\tv_exp_f32 v63, v63\n\tv_exp_f32 v64, v64\n\tv_exp_f32 v65, v65\n\tv_smfmac_f32_32x32x32_bf16 %[o0], v[120:123], v[56:63], v128\n\t"
        "v_exp_f32 v66, v66\n\tv_exp_f32 v67, v67\n\tv_exp_f32 v68, v68\n\tv_mfma_f32_32x32x16_bf16 v[48:63], v[92:95], %[q0], 0\n\tglobal_load_dwordx4 v[212:215], %[vw], s[48:49]\n\t"
        "s_add_u32 s48, s48, 8192\n\ts_addc_u32 s49, s49, 0\n\tglobal_load_dwordx4 v[216:219], %[vw], s[48:49]\n\ts_add_u32 s48, s48, 8192\n\ts_addc_u32 s49, s49, 0\n\t"
        "v_exp_f32 v69, v69\n\tv_exp_f32 v70, v70\n\tv_exp_f32 v71, v71\n\tv_exp_f32 v72, v72\n\tv_exp_f32 v73, v73\n\tv_smfmac_f32_32x32x32_bf16 %[o1], v[104:107], v[64:71], v128\n\t"
        "v_exp_f32 v74, v74\n\tv_exp_f32 v75, v75\n\tv_exp_f32 v76, v76\n\tv_exp_f32 v77, v77\n\tv_exp_f32 v78, v78\n\tv_exp_f32 v79, v79\n\tv_exp_f32 v48, v48\n\tv_exp_f32 v49, v49\n\t"
        "v_smfmac_f32_32x32x32_bf16 %[o1], v[120:123], v[72:79], v128\n\tv_exp_f32 v50, v50\n\tv_exp_f32 v51, v51\n\tv_exp_f32 v52, v52\n\t"
        "v_mfma_f32_32x32x16_bf16 v[64:79], v[92:95], %[q1], 0\n\tglobal_load_dwordx4 v[92:95], %[koff], s[40:41] offset:3072 sc1\n\ts_mov_b64 exec, %[vm]\n\t"
        "global_load_dwordx4 v[104:107], %[voff], s[42:43] offset:2048 sc1\n\tglobal_load_dwordx4 v[120:123], %[voff], s[42:43] offset:2560 sc1\n\ts_mov_b64 exec, -1\n\t"
        "v_exp_f32 v53, v53\n\tv_exp_f32 v54, v54\n\tv_exp_f32 v55, v55\n\tv_exp_f32 v56, v56\n\tv_exp_f32 v57, v57\n\ts_waitcnt vmcnt(15)\n\t"
        "v_smfmac_f32_32x32x32_bf16 %[o0], v[108:111], v[48:55], v128\n\tv_exp_f32 v58, v58\n\tv_exp_f32 v59, v59\n\tv_exp_f32 v60, v60\n\tv_exp_f32 v61, v61\n\tv_exp_f32 v62, v62\n\t"
        "v_exp_f32 v63, v63\n\tv_exp_f32 v64, v64\n\tv_exp_f32 v65, v65\n\tv_smfmac_f32_32x32x32_bf16 %[o0], v[124:127], v[56:63], v128\n\tv_exp_f32 v66, v66\n\tv_exp_f32 v67, v67\n\t"
        "v_exp_f32 v68, v68\n\tv_mfma_f32_32x32x16_bf16 v[48:63], v[80:83], %[q0], 0\n\ts_add_u32 m0, %[hl], 1024\n\ts_nop 0\n\tglobal_load_lds_dwordx4 %[b1], s[52:53]\n\t"
        "global_load_dwordx4 v[220:223], %[vw], s[48:49]\n\ts_add_u32 s48, s48, 8192\n\ts_addc_u32 s49, s49, 0\n\tv_exp_f32 v69, v69\n\tv_exp_f32 v70, v70\n\tv_exp_f32 v71, v71\n\t"
        "v_exp_f32 v72, v72\n\tv_exp_f32 v73, v73\n\tv_smfmac_f32_32x32x32_bf16 %[o1], v[108:111], v[64:71], v128\n\tv_exp_f32 v74, v74\n\tv_exp_f32 v75, v75\n\tv_exp_f32 v76, v76\n\t"
        "v_exp_f32 v77, v77\n\tv_exp_f32 v78, v78\n\tv_exp_f32 v79, v79\n\tv_exp_f32 v48, v48\n\tv_exp_f32 v49, v49\n\tv_smfmac_f32_32x32x32_bf16 %[o1], v[124:127], v[72:79], v128\n\t"
        "v_exp_f32 v50, v50\n\tv_exp_f32 v51, v51\n\tv_exp_f32 v52, v52\n\tv_mfma_f32_32x32x16_bf16 v[64:79], v[80:83], %[q1], 0\n\ts_add_u32 s40, s40, 4096\n\ts_addc_u32 s41, s41, 0\n\t"
        "global_load_dwordx4 v[80:83], %[koff], s[40:41] offset:0 sc1\n\ts_mov_b64 exec, %[vm]\n\tglobal_load_dwordx4 v[108:111], %[voff], s[42:43] offset:3072 sc1\n\t"
        "global_load_dwordx4 v[124:127], %[voff], s[42:43] offset:3584 sc1\n\ts_mov_b64 exec, -1\n\tv_exp_f32 v53, v53\n\tv_exp_f32 v54, v54\n\tv_exp_f32 v55, v55\n\tv_exp_f32 v56, v56\n\t"
        "v_exp_f32 v57, v57\n\ts_waitcnt vmcnt(15)\n\tv_smfmac_f32_32x32x32_bf16 %[o0], v[96:99], v[48:55], v128\n\tv_exp_f32 v58, v58\n\tv_exp_f32 v59, v59\n\tv_exp_f32 v60, v60\n\t"
        "v_exp_f32 v61, v61\n\tv_exp_f32 v62, v62\n\tv_exp_f32 v63, v63\n\tv_exp_f32 v64, v64\n\tv_exp_f32 v65, v65\n\tv_smfmac_f32_32x32x32_bf16 %[o0], v[112:115], v[56:63], v128\n\t"
        "v_exp_f32 v66, v66\n\tv_exp_f32 v67, v67\n\tv_exp_f32 v68, v68\n\tv_mfma_f32_32x32x16_bf16 v[48:63], v[84:87], %[q0], 0\n\tglobal_load_dwordx4 v[224:227], %[vw], s[48:49]\n\t"
        "s_add_u32 s48, s48, 8192\n\ts_addc_u32 s49, s49, 0\n\ts_add_u32 m0, %[hl], 2048\n\ts_nop 0\n\tglobal_load_lds_dwordx4 %[b2], s[52:53]\n\tv_exp_f32 v69, v69\n\t"
        "v_exp_f32 v70, v70\n\tv_exp_f32 v71, v71\n\tv_exp_f32 v72, v72\n\tv_exp_f32 v73, v73\n\tv_smfmac_f32_32x32x32_bf16 %[o1], v[96:99], v[64:71], v128\n\tv_exp_f32 v74, v74\n\t"
        "v_exp_f32 v75, v75\n\tv_exp_f32 v76, v76\n\tv_exp_f32 v77, v77\n\tv_exp_f32 v78, v78\n\tv_exp_f32 v79, v79\n\tv_exp_f32 v48, v48\n\tv_exp_f32 v49, v49\n\t"
        "v_smfmac_f32_32x32x32_bf16 %[o1], v[112:115], v[72:79], v128\n\tv_exp_f32 v50, v50\n\tv_exp_f32 v51, v51\n\tv_exp_f32 v52, v52\n\t"
        "v_mfma_f32_32x32x16_bf16 v[64:79], v[84:87], %[q1], 0\n\tglobal_load_dwordx4 v[84:87], %[koff], s[40:41] offset:1024 sc1\n\ts_add_u32 s42, s42, 4096\n\ts_addc_u32 s43, s43, 0\n\t"
        "s_mov_b64 exec, %[vm]\n\tglobal_load_dwordx4 v[96:99], %[voff], s[42:43] offset:0 sc1\n\tglobal_load_dwordx4 v[112:115], %[voff], s[42:43] offset:512 sc1\n\ts_mov_b64 exec, -1\n\t"
        "v_exp_f32 v53, v53\n\tv_exp_f32 v54, v54\n\tv_exp_f32 v55, v55\n\tv_exp_f32 v56, v56\n\tv_exp_f32 v57, v57\n\ts_waitcnt vmcnt(15)\n\t"
        "v_smfmac_f32_32x32x32_bf16 %[o0], v[100:103], v[48:55], v128\n\tv_exp_f32 v58, v58\n\tv_exp_f32 v59, v59\n\tv_exp_f32 v60, v60\n\tv_exp_f32 v61, v61\n\tv_exp_f32 v62, v62\n\t"
        "v_exp_f32 v63, v63\n\tv_exp_f32 v64, v64\n\tv_exp_f32 v65, v65\n\tv_smfmac_f32_32x32x32_bf16 %[o0], v[116:119], v[56:63], v128\n\tv_exp_f32 v66, v66\n\tv_exp_f32 v67, v67\n\t"
        "v_exp_f32 v68, v68\n\tv_mfma_f32_32x32x16_bf16 v[48:63], v[88:91], %[q0], 0\n\tglobal_load_dwordx4 v[228:231], %[vw], s[48:49]\n\ts_add_u32 s48, s48, 8192\n\t"
        "s_addc_u32 s49, s49, 0\n\tglobal_load_dwordx4 v[232:235], %[vw], s[48:49]\n\ts_add_u32 s48, s48, 8192\n\ts_addc_u32 s49, s49, 0\n\tv_exp_f32 v69, v69\n\tv_exp_f32 v70, v70\n\t"
        "v_exp_f32 v71, v71\n\tv_exp_f32 v72, v72\n\tv_exp_f32 v73, v73\n\tv_smfmac_f32_32x32x32_bf16 %[o1], v[100:103], v[64:71], v128\n\tv_exp_f32 v74, v74\n\tv_exp_f32 v75, v75\n\t"
        "v_exp_f32 v76, v76\n\tv_exp_f32 v77, v77\n\tv_exp_f32 v78, v78\n\tv_exp_f32 v79, v79\n\tv_exp_f32 v48, v48\n\tv_exp_f32 v49, v49\n\t"
        "v_smfmac_f32_32x32x32_bf16 %[o1], v[116:119], v[72:79], v128\n\tv_exp_f32 v50, v50\n\tv_exp_f32 v51, v51\n\tv_exp_f32 v52, v52\n\t"
        "v_mfma_f32_32x32x16_bf16 v[64:79], v[88:91], %[q1], 0\n\tglobal_load_dwordx4 v[88:91], %[koff], s[40:41] offset:2048 sc1\n\ts_mov_b64 exec, %[vm]\n\t"
        "global_load_dwordx4 v[100:103], %[voff], s[42:43] offset:1024 sc1\n\tglobal_load_dwordx4 v[116:119], %[voff], s[42:43] offset:1536 sc1\n\ts_mov_b64 exec, -1\n\t"
        "v_exp_f32 v53, v53\n\tv_exp_f32 v54, v54\n\tv_exp_f32 v55, v55\n\tv_exp_f32 v56, v56\n\tv_exp_f32 v57, v57\n\ts_waitcnt vmcnt(15)\n\t"
        "v_smfmac_f32_32x32x32_bf16 %[o0], v[104:107], v[48:55], v128\n\tv_exp_f32 v58, v58\n\tv_exp_f32 v59, v59\n\tv_exp_f32 v60, v60\n\tv_exp_f32 v61, v61\n\tv_exp_f32 v62, v62\n\t"
        "v_exp_f32 v63, v63\n\tv_exp_f32 v64, v64\n\tv_exp_f32 v65, v65\n\tv_smfmac_f32_32x32x32_bf16 %[o0], v[120:123], v[56:63], v128\n\tv_exp_f32 v66, v66\n\tv_exp_f32 v67, v67\n\t"
        "v_exp_f32 v68, v68\n\tv_mfma_f32_32x32x16_bf16 v[48:63], v[92:95], %[q0], 0\n\ts_add_u32 m0, %[hl], 3072\n\ts_add_u32 s52, s52, 44032\n\ts_addc_u32 s53, s53, 0\n\t"
        "global_load_lds_dwordx4 %[b0], s[52:53]\n\tglobal_load_dwordx4 v[236:239], %[vw], s[48:49]\n\ts_add_u32 s48, s48, 8192\n\ts_addc_u32 s49, s49, 0\n\ts_cmp_eq_u32 %[drop], 0\n\t"
        "s_cbranch_scc1 2f\n\ts_setprio 0\n\t2:\n\tv_exp_f32 v69, v69\n\tv_exp_f32 v70, v70\n\tv_exp_f32 v71, v71\n\tv_exp_f32 v72, v72\n\tv_exp_f32 v73, v73\n\t"
        "v_smfmac_f32_32x32x32_bf16 %[o1], v[104:107], v[64:71], v128\n\tv_exp_f32 v74, v74\n\tv_exp_f32 v75, v75\n\tv_exp_f32 v76, v76\n\tv_exp_f32 v77, v77\n\tv_exp_f32 v78, v78\n\t"
        "v_exp_f32 v79, v79\n\tv_exp_f32 v48, v48\n\tv_exp_f32 v49, v49\n\tv_smfmac_f32_32x32x32_bf16 %[o1], v[120:123], v[72:79], v128\n\tv_exp_f32 v50, v50\n\tv_exp_f32 v51, v51\n\t"
        "v_exp_f32 v52, v52\n\tv_mfma_f32_32x32x16_bf16 v[64:79], v[92:95], %[q1], 0\n\tglobal_load_dwordx4 v[92:95], %[koff], s[40:41] offset:3072 sc1\n\ts_mov_b64 exec, %[vm]\n\t"
        "global_load_dwordx4 v[104:107], %[voff], s[42:43] offset:2048 sc1\n\tglobal_load_dwordx4 v[120:123], %[voff], s[42:43] offset:2560 sc1\n\ts_mov_b64 exec, -1\n\t"
        "v_exp_f32 v53, v53\n\tv_exp_f32 v54, v54\n\tv_exp_f32 v55, v55\n\tv_exp_f32 v56, v56\n\tv_exp_f32 v57, v57\n\ts_waitcnt vmcnt(15)\n\t"
        "v_smfmac_f32_32x32x32_bf16 %[o0], v[108:111], v[48:55], v128\n\tv_exp_f32 v58, v58\n\tv_exp_f32 v59, v59\n\tv_exp_f32 v60, v60\n\tv_exp_f32 v61, v61\n\tv_exp_f32 v62, v62\n\t"
        "v_exp_f32 v63, v63\n\tv_exp_f32 v64, v64\n\tv_exp_f32 v65, v65\n\tv_smfmac_f32_32x32x32_bf16 %[o0], v[124:127], v[56:63], v128\n\tv_exp_f32 v66, v66\n\tv_exp_f32 v67, v67\n\t"
        "v_exp_f32 v68, v68\n\tv_mfma_f32_32x32x16_bf16 v[48:63], v[80:83], %[q0], 0\n\tglobal_load_dwordx4 v[240:243], %[vw], s[48:49]\n\ts_add_u32 s48, s48, 8192\n\t"
        "s_addc_u32 s49, s49, 0\n\ts_add_u32 m0, %[hl], 4096\n\ts_nop 0\n\tglobal_load_lds_dwordx4 %[b1], s[52:53]\n\tv_exp_f32 v69, v69\n\tv_exp_f32 v70, v70\n\tv_exp_f32 v71, v71\n\t"
        "v_exp_f32 v72, v72\n\tv_exp_f32 v73, v73\n\tv_smfmac_f32_32x32x32_bf16 %[o1], v[108:111], v[64:71], v128\n\tv_exp_f32 v74, v74\n\tv_exp_f32 v75, v75\n\tv_exp_f32 v76, v76\n\t"
        "v_exp_f32 v77, v77\n\tv_exp_f32 v78, v78\n\tv_exp_f32 v79, v79\n\tv_exp_f32 v48, v48\n\tv_exp_f32 v49, v49\n\tv_smfmac_f32_32x32x32_bf16 %[o1], v[124:127], v[72:79], v128\n\t"
        "v_exp_f32 v50, v50\n\tv_exp_f32 v51, v51\n\tv_exp_f32 v52, v52\n\tv_mfma_f32_32x32x16_bf16 v[64:79], v[80:83], %[q1], 0\n\ts_mov_b64 exec, %[vm]\n\t"
        "global_load_dwordx4 v[108:111], %[voff], s[42:43] offset:3072 sc1\n\tglobal_load_dwordx4 v[124:127], %[voff], s[42:43] offset:3584 sc1\n\ts_mov_b64 exec, -1\n\t"
        "v_exp_f32 v53, v53\n\tv_exp_f32 v54, v54\n\tv_exp_f32 v55, v55\n\tv_exp_f32 v56, v56\n\tv_exp_f32 v57, v57\n\ts_waitcnt vmcnt(14)\n\t"
        "v_smfmac_f32_32x32x32_bf16 %[o0], v[96:99], v[48:55], v128\n\tv_exp_f32 v58, v58\n\tv_exp_f32 v59, v59\n\tv_exp_f32 v60, v60\n\tv_exp_f32 v61, v61\n\tv_exp_f32 v62, v62\n\t"
        "v_exp_f32 v63, v63\n\tv_exp_f32 v64, v64\n\tv_exp_f32 v65, v65\n\tv_smfmac_f32_32x32x32_bf16 %[o0], v[112:115], v[56:63], v128\n\tv_exp_f32 v66, v66\n\tv_exp_f32 v67, v67\n\t"
        "v_exp_f32 v68, v68\n\tv_mfma_f32_32x32x16_bf16 v[48:63], v[84:87], %[q0], 0\n\tglobal_load_dwordx4 v[244:247], %[vw], s[48:49]\n\ts_add_u32 s48, s48, 8192\n\t"
        "s_addc_u32 s49, s49, 0\n\tglobal_load_dwordx4 v[248:251], %[vw], s[48:49]\n\ts_add_u32 s48, s48, 8192\n\ts_addc_u32 s49, s49, 0\n\tv_exp_f32 v69, v69\n\tv_exp_f32 v70, v70\n\t"
        "v_exp_f32 v71, v71\n\tv_exp_f32 v72, v72\n\tv_exp_f32 v73, v73\n\tv_smfmac_f32_32x32x32_bf16 %[o1], v[96:99], v[64:71], v128\n\tv_exp_f32 v74, v74\n\tv_exp_f32 v75, v75\n\t"
        "v_exp_f32 v76, v76\n\tv_exp_f32 v77, v77\n\tv_exp_f32 v78, v78\n\tv_exp_f32 v79, v79\n\tv_exp_f32 v48, v48\n\tv_exp_f32 v49, v49\n\t"
        "v_smfmac_f32_32x32x32_bf16 %[o1], v[112:115], v[72:79], v128\n\tv_exp_f32 v50, v50\n\tv_exp_f32 v51, v51\n\tv_exp_f32 v52, v52\n\t"
        "v_mfma_f32_32x32x16_bf16 v[64:79], v[84:87], %[q1], 0\n\tv_exp_f32 v53, v53\n\tv_exp_f32 v54, v54\n\tv_exp_f32 v55, v55\n\tv_exp_f32 v56, v56\n\tv_exp_f32 v57, v57\n\t"
        "s_waitcnt vmcnt(11)\n\tv_smfmac_f32_32x32x32_bf16 %[o0], v[100:103], v[48:55], v128\n\tv_exp_f32 v58, v58\n\tv_exp_f32 v59, v59\n\tv_exp_f32 v60, v60\n\tv_exp_f32 v61, v61\n\t"
        "v_exp_f32 v62, v62\n\tv_exp_f32 v63, v63\n\tv_exp_f32 v64, v64\n\tv_exp_f32 v65, v65\n\tv_smfmac_f32_32x32x32_bf16 %[o0], v[116:119], v[56:63], v128\n\tv_exp_f32 v66, v66\n\t"
        "v_exp_f32 v67, v67\n\tv_exp_f32 v68, v68\n\tv_mfma_f32_32x32x16_bf16 v[48:63], v[88:91], %[q0], 0\n\ts_add_u32 m0, %[hl], 5120\n\ts_nop 0\n\t"
        "global_load_lds_dwordx4 %[b2], s[52:53]\n\tglobal_load_dwordx4 v[252:255], %[vw], s[48:49]\n\ts_add_u32 s48, s48, 8192\n\ts_addc_u32 s49, s49, 0\n\tv_exp_f32 v69, v69\n\t"
        "v_exp_f32 v70, v70\n\tv_exp_f32 v71, v71\n\tv_exp_f32 v72, v72\n\tv_exp_f32 v73, v73\n\tv_smfmac_f32_32x32x32_bf16 %[o1], v[100:103], v[64:71], v128\n\tv_exp_f32 v74, v74\n\t"
        "v_exp_f32 v75, v75\n\tv_exp_f32 v76, v76\n\tv_exp_f32 v77, v77\n\tv_exp_f32 v78, v78\n\tv_exp_f32 v79, v79\n\tv_exp_f32 v48, v48\n\tv_exp_f32 v49, v49\n\t"
        "v_smfmac_f32_32x32x32_bf16 %[o1], v[116:119], v[72:79], v128\n\tv_exp_f32 v50, v50\n\tv_exp_f32 v51, v51\n\tv_exp_f32 v52, v52\n\t"
        "v_mfma_f32_32x32x16_bf16 v[64:79], v[88:91], %[q1], 0\n\tv_exp_f32 v53, v53\n\tv_exp_f32 v54, v54\n\tv_exp_f32 v55, v55\n\tv_exp_f32 v56, v56\n\tv_exp_f32 v57, v57\n\t"
        "s_waitcnt vmcnt(8)\n\tv_smfmac_f32_32x32x32_bf16 %[o0], v[104:107], v[48:55], v128\n\tv_exp_f32 v58, v58\n\tv_exp_f32 v59, v59\n\tv_exp_f32 v60, v60\n\tv_exp_f32 v61, v61\n\t"
        "v_exp_f32 v62, v62\n\tv_exp_f32 v63, v63\n\tv_exp_f32 v64, v64\n\tv_exp_f32 v65, v65\n\tv_smfmac_f32_32x32x32_bf16 %[o0], v[120:123], v[56:63], v128\n\tv_exp_f32 v66, v66\n\t"
        "v_exp_f32 v67, v67\n\tv_exp_f32 v68, v68\n\tv_mfma_f32_32x32x16_bf16 v[48:63], v[92:95], %[q0], 0\n\tv_exp_f32 v69, v69\n\tv_exp_f32 v70, v70\n\tv_exp_f32 v71, v71\n\t"
        "v_exp_f32 v72, v72\n\tv_exp_f32 v73, v73\n\tv_smfmac_f32_32x32x32_bf16 %[o1], v[104:107], v[64:71], v128\n\tv_exp_f32 v74, v74\n\tv_exp_f32 v75, v75\n\tv_exp_f32 v76, v76\n\t"
        "v_exp_f32 v77, v77\n\tv_exp_f32 v78, v78\n\tv_exp_f32 v79, v79\n\tv_exp_f32 v48, v48\n\tv_exp_f32 v49, v49\n\tv_smfmac_f32_32x32x32_bf16 %[o1], v[120:123], v[72:79], v128\n\t"
        "v_exp_f32 v50, v50\n\tv_exp_f32 v51, v51\n\tv_exp_f32 v52, v52\n\tv_mfma_f32_32x32x16_bf16 v[64:79], v[92:95], %[q1], 0\n\tv_exp_f32 v53, v53\n\tv_exp_f32 v54, v54\n\t"
        "v_exp_f32 v55, v55\n\tv_exp_f32 v56, v56\n\tv_exp_f32 v57, v57\n\ts_waitcnt vmcnt(4)\n\tv_smfmac_f32_32x32x32_bf16 %[o0], v[108:111], v[48:55], v128\n\tv_exp_f32 v58, v58\n\t"
        "v_exp_f32 v59, v59\n\tv_exp_f32 v60, v60\n\tv_exp_f32 v61, v61\n\tv_exp_f32 v62, v62\n\tv_exp_f32 v63, v63\n\tv_exp_f32 v64, v64\n\tv_exp_f32 v65, v65\n\t"
        "v_smfmac_f32_32x32x32_bf16 %[o0], v[124:127], v[56:63], v128\n\tv_exp_f32 v66, v66\n\tv_exp_f32 v67, v67\n\tv_exp_f32 v68, v68\n\tv_exp_f32 v69, v69\n\tv_exp_f32 v70, v70\n\t"
        "v_exp_f32 v71, v71\n\tv_exp_f32 v72, v72\n\tv_exp_f32 v73, v73\n\tv_smfmac_f32_32x32x32_bf16 %[o1], v[108:111], v[64:71], v128\n\tv_exp_f32 v74, v74\n\tv_exp_f32 v75, v75\n\t"
        "v_exp_f32 v76, v76\n\tv_exp_f32 v77, v77\n\tv_exp_f32 v78, v78\n\tv_exp_f32 v79, v79\n\ts_setprio 0\n\ts_nop 0\n\tv_smfmac_f32_32x32x32_bf16 %[o1], v[124:127], v[72:79], v128\n\t"
        "s_waitcnt vmcnt(0)\n\ts_nop 15\n\t"
        : [o0] "+v"(o0), [o1] "+v"(o1), [q0] "=&v"(q0), [q1] "=&v"(q1), "=&{v[144:159]}"(sg[0]), "=&{v[160:175]}"(sg[1]), "=&{v[176:191]}"(sg[2]), "=&{v[192:207]}"(sg[3]), "=&{v[208:223]}"(sg[4]), "=&{v[224:239]}"(sg[5]), "+{v[240:255]}"(sg[6])
        : [qa] "v"(qa), [qb] "v"(qb), [koff] "v"(koff), [voff] "v"(voff), [vinit] "v"(vinit), [vm] "s"(0x0000ffff0000ffffull), [kin] "s"(kb), [vin] "s"(vb), [drop] "s"(drop),
          [a0] "v"(a0), [a1] "v"(a1), [a2] "v"(a2), [b0] "v"(v30), [b1] "v"(v31), [b2] "v"(v32), [vw] "v"(vw), [sa] "s"(sa), [sb] "s"(sb), [so] "s"(so), [sh] "s"(sh), [ga] "s"(ga), [ol] "s"(ol), [hl] "s"(hl)
        : "memory", "scc", "s40", "s41", "s42", "s43", "s46", "s47", "s48", "s49", "s50", "s51", "s52", "s53",
          "v48","v49","v50","v51","v52","v53","v54","v55","v56","v57","v58","v59","v60","v61","v62","v63","v64","v65","v66","v67","v68","v69","v70","v71","v72","v73","v74","v75","v76","v77","v78","v79",
          "v80","v81","v82","v83","v84","v85","v86","v87","v88","v89","v90","v91","v92","v93","v94","v95","v96","v97","v98","v99","v100","v101","v102","v103","v104","v105","v106","v107","v108","v109","v110","v111",
          "v112","v113","v114","v115","v116","v117","v118","v119","v120","v121","v122","v123","v124","v125","v126","v127","v128");
    }
    STAMP(7); STAMPC(27);
    lane = __builtin_amdgcn_mbcnt_hi(~0u, __builtin_amdgcn_mbcnt_lo(~0u, 0u)); asm volatile("" : "+v"(lane));
    r32 = lane & 31; h = lane >> 5; q = (lane & 15) >> 2; p = lane & 3; blk = (lane >> 4) & 1;
    rowoff = ((rg * 2 + (lw >> 1)) * 32 + r32) * D_ + cs * 64 + (lw & 1) * 32 + 8 * h;
    if (wave < 4) { res[0] = *(const u32x4*)(x + rowoff); res[1] = *(const u32x4*)(x + rowoff + 16); }
    {
      LAS char* d = smem + L_RED + wave * 2304 + lane * 16;
      *(LAS f32x4*)d = f32x4{o1[0], o1[1], o1[2], o1[3]}; *(LAS f32x4*)(d + 1024) = f32x4{o1[4], o1[5], o1[6], o1[7]};
      *(LAS float*)(smem + L_RED + wave * 2304 + 2048 + lane * 4) = o1[8];
    }
    BARRIER();
    float o[9];
    {
      LAS char* d = smem + L_RED + (wave ^ 1) * 2304 + lane * 16;
      const f32x4 e0 = *(LAS f32x4*)d, e1 = *(LAS f32x4*)(d + 1024);
      o[8] = o0[8] + *(LAS float*)(smem + L_RED + (wave ^ 1) * 2304 + 2048 + lane * 4);
#pragma unroll
      for (int i = 0; i < 4; i++) { o[i] = o0[i] + e0[i]; o[4 + i] = o0[4 + i] + e1[i]; }
    }
    const bool ok = o[8] >= 0x1p-40f && o[8] <= 0x1p40f;
    if (__builtin_expect(!__all(ok), 0)) {
      const u32x4 qq = q0;
      f32x16 z, oc;
#pragma unroll
      for (int i = 0; i < 16; i++) { z[i] = 0.f; oc[i] = 0.f; }
      float m = -__builtin_inff();
      const bool vl = (lane & 16) == 0;
      const u32x4 ones = {0x3f803f80u, 0x3f803f80u, 0x3f803f80u, 0x3f803f80u};
#pragma unroll 1
      for (int t = 0; t < 64; t++) {
        const u32x4 kk = ld16(rs, lane * 16, WS_K + head * 65536 + t * 1024);
        u32x4 wa = ld16(rs, h * 256 + (lane & 15) * 16, WS_V + head * 65536 + t * 1024), wb = ld16(rs, h * 256 + (lane & 15) * 16, WS_V + head * 65536 + t * 1024 + 512);
        wa = vl ? wa : ones; wb = vl ? wb : ones;
        f32x16 s = mfma(kk, qq, z);
        float tm = s[0];
#pragma unroll
        for (int i = 1; i < 16; i++) tm = fmaxf(tm, s[i]);
        tm = fmaxf(tm, __shfl_xor(tm, 32));
        const float mn = fmaxf(m, tm), al = __builtin_amdgcn_exp2f(m - mn);
        m = mn;
#pragma unroll
        for (int i = 0; i < 16; i++) { oc[i] *= al; s[i] -= mn; }
        oc = pv(wa, exp8(s, 0), oc); oc = pv(wb, exp8(s, 8), oc);
      }
#pragma unroll
      for (int i = 0; i < 9; i++) o[i] = oc[i];
    }
    const float rl = __builtin_amdgcn_rcpf(o[8]);
    u32x4 r;
#pragma unroll
    for (int j = 0; j < 4; j++) r[j] = pack2(o[2 * j] * rl, o[2 * j + 1] * rl);
    st16(rs, lane * 16, WS_O + ((rg1 * 2 + kh) * 32 + head) * 1024, r);
    STAMP(8);
    DRAIN(2);
    BARRIER();
    if (wave == 4) publish(ws, WS_F2 + (rg1 * 8 + hg) * 128, tag, lane);
    STAMP(9);
  }

  auto opq = [](int v) { asm volatile("" : "+v"(v)); return v; };
  const int bb1 = L_B1 + (8 * h + q) * 192 + blk * 32 + p * 8;
  if (wave < 4) {
  const int rb = wave >> 1, kh = wave & 1;
  const int tile = rg * 2 + rb;
  LAS char* bsq[2];
#pragma unroll
  for (int u = 0; u < 2; u++) bsq[u] = smem + (4 * h + q) * 128 + (((4 * (u ^ kh) + 2 * blk + (p & 1)) ^ ((q >> 1) << 2)) << 4) + 8 * (p >> 1);
  auto nohook = [](auto) {};
  auto exchange = [&](f32x16 (&acc)[2], int lds) {
#pragma unroll
    for (int i = 0; i < 4; i++) *(LAS f32x4*)(smem + lds + (wave * 4 + i) * 1024 + lane * 16) = f32x4{acc[1][4 * i], acc[1][4 * i + 1], acc[1][4 * i + 2], acc[1][4 * i + 3]};
    BARRIER();
#pragma unroll
    for (int i = 0; i < 4; i++) {
      const f32x4 v = *(LAS f32x4*)(smem + lds + ((wave ^ 1) * 4 + i) * 1024 + lane * 16);
      for (int j = 0; j < 4; j++) acc[0][4 * i + j] += v[j];
    }
  };
  {
    f32x16 acc[2];
#pragma unroll
    for (int i = 0; i < 16; i++) { acc[0][i] = 0.f; acc[1][i] = 0.f; }
    gemm_direct<16, 2, 0, Run4>(rs, lane * 16, [&](int pos) { return WS_O + (tile * 32 + kh * 16 + ((pos + off3) & 15)) * 1024; },
                          [&](int pos, int u) { return tr_frag(bsq[u] + kh * 32768 + pos * 2048, 1024); }, (const u32x4(*)[2]) nullptr, acc,
                                [&]() { BARRIER(); STAMP(10); },
                                nohook);
    STAMP(11);
    exchange(acc, L_Q);
    f32x2 t[8];
#pragma unroll
    for (int i = 0; i < 8; i++) t[i] = f32x2{acc[0][2 * i] + bflo(res[i >> 2][i & 3]), acc[0][2 * i + 1] + bfhi(res[i >> 2][i & 3])};
    FENCE();
#pragma unroll
    for (int i = 0; i < 8; i++) res[i >> 2][i & 3] = pack2v(t[i]);
    FENCE();
    st16(rs, lane * 16, WS_H + (tile * 32 + cs * 4 + kh * 2) * 1024, res[0]);
    st16(rs, lane * 16, WS_H + (tile * 32 + cs * 4 + kh * 2 + 1) * 1024, res[1]);
#pragma unroll
    for (int i = 0; i < 8; i++) t[i] = f32x2{bflo(res[i >> 2][i & 3]), bfhi(res[i >> 2][i & 3])};
    FENCE();
    f32x2 s2[4], q2[4];
#pragma unroll
    for (int i = 0; i < 4; i++) { s2[i] = t[i] + t[i + 4]; q2[i] = pfma(t[i], t[i], t[i + 4] * t[i + 4]); }
    const f32x2 sa = (s2[0] + s2[1]) + (s2[2] + s2[3]), qa = (q2[0] + q2[1]) + (q2[2] + q2[3]);
    const float sx = xhalf_sum(sa[0] + sa[1]), sxx = xhalf_sum(qa[0] + qa[1]);
    if (h == 0) __builtin_amdgcn_raw_buffer_store_b64(u32x2{asu(sx), asu(sxx)}, rs, r32 * 8, WS_ST + (tile * 16 + cs * 2 + kh) * 256, 16);
    STAMP(12);
    BARRIER();
#pragma unroll
    for (int i = 0; i < 12; i++) *(LAS u32x4*)(smem + L_B1 + lw * 12288 + i * 1024 + lane * 16) = stg(i);
    DRAIN(4);
    BARRIER();
#pragma unroll
    for (int i = 0; i < 8; i++) {
      LAS char* dst = smem + (2 * ((lw >> 1) * 7 + i) + (lw & 1)) * 2048 + lane * 16;
      *(LAS u32x4*)dst = stg(12 + 2 * i); *(LAS u32x4*)(dst + 1024) = stg(13 + 2 * i);
    }
    STAMP(13);
  }
  u32x4 pre[4][2];
  {
    const int t4 = rp * 4 + wave;
    f32x16 acc[3];
#pragma unroll
    for (int u = 0; u < 3; u++) for (int i = 0; i < 16; i++) acc[u][i] = 0.f;
    u32x4 wx[3];
    const int b4[2] = {opq(bb1), opq(bb1 + 49152)};
    unsigned sto = WS_ST + t4 * 4096; asm volatile("" : "+s"(sto));
    u32x2 st[8];
    float rs2 = 0.f, sx = 0.f, sxx = 0.f, nm = 0.f, sd = 0.f;
    u32x4 ax = {0u, 0u, 0u, 0u};
    gemm_direct<32, 3, 0, Run4>(rs, lane * 16, [&](int pos) { return WS_H + (t4 * 32 + ((pos + off4) & 31)) * 1024; },
        [&](int pos, int u) { return tr_frag(smem + b4[pos >> 4] + (pos & 15) * 3072 + u * 64, 768); }, (const u32x4(*)[3]) nullptr, acc,
        [&]() { BARRIER(); STAMP(16); },
        [&](auto pp) {
          constexpr int pos = pp.value;
          if constexpr (pos < 8) st[pos] = __builtin_amdgcn_raw_buffer_load_b64(rs, h * 2048 + r32 * 8 + pos * 256, sto, 16);
          if constexpr (pos >= 12 && pos < 20) { sx = add1(asf(st[pos - 12][0]), sx); sxx = add1(asf(st[pos - 12][1]), sxx); }
          if constexpr (pos == 20) sx = xhalf_sum(sx);
          if constexpr (pos == 21) sxx = xhalf_sum(sxx);
          if constexpr (pos == 22) { nm = sx * (-1.f / D_); sd = fmaxf(fma1(sxx, 1.f / D_, -nm * nm), 0.f) + 1e-5f; }
          if constexpr (pos == 23) { rs2 = __builtin_amdgcn_rsqf(sd); asm("" : "+v"(rs2)); }
          if constexpr (pos == 24) { sd *= rs2; rs2 *= LOG2E; nm = h ? 0.f : nm; sd = h ? 0.f : sd; asm("" : "+v"(nm), "+v"(sd), "+v"(rs2)); }
          if constexpr (pos == 25) { ax[0] = pack2(nm, nm); ax[3] = pack2(sd, sd); }
          if constexpr (pos == 26) { nm = add1(nm, -bflo(ax[0])); sd = add1(sd, -bflo(ax[3])); }
          if constexpr (pos >= 28 && pos < 31) wx[pos - 28] = *(LAS u32x4*)(smem + L_RES + 256 + (pos - 28) * 512 + r32 * 16);
          if constexpr (pos == 27) { ax[1] = (pack2(nm, 0.f) & 0xffffu) | (ax[3] & 0xffff0000u); ax[2] = pack2(sd, 0.f); ax[3] = 0u; }
        });
#pragma unroll
    for (int u = 0; u < 3; u++) acc[u] = mfma(wx[u], ax, acc[u]);
    STAMP(17);
#pragma unroll
    for (int i = 0; i < 4; i++) for (int u = 0; u < 2; u++) pre[i][u] = tr_frag(bsq[u] + (2 * i + kh) * 2048, 1024);
    FENCE();
    prog[wave] = 34;
    FENCE();
#pragma unroll
    for (int u = 0; u < 3; u++) {
      if (u < nunit) {
        f32x2 w[8], t[8];
#pragma unroll
        for (int i = 0; i < 8; i++) w[i] = f32x2{acc[u][2 * i], acc[u][2 * i + 1]} * pk(rs2);
        FENCE();
#pragma unroll
        for (int i = 0; i < 8; i++) t[i] = f32x2{__builtin_amdgcn_exp2f(w[i][0]), __builtin_amdgcn_exp2f(w[i][1])};
        FENCE();
        f32x2 a2[8];
#pragma unroll
        for (int i = 0; i < 8; i++) a2[i] = t[i] + pk(2.f);
        FENCE();
#pragma unroll
        for (int i = 0; i < 8; i++) t[i] = pfma(t[i], a2[i], pk(2.f));
        FENCE();
#pragma unroll
        for (int i = 0; i < 8; i++) t[i] = f32x2{__builtin_amdgcn_rcpf(t[i][0]), __builtin_amdgcn_rcpf(t[i][1])};
        FENCE();
#pragma unroll
        for (int i = 0; i < 8; i++) t[i] = pfma(t[i], pk(-2.f / LOG2E), pk(1.f / LOG2E));
        FENCE();
#pragma unroll
        for (int i = 0; i < 8; i++) t[i] = t[i] * w[i];
        FENCE();
        unsigned o[8];
#pragma unroll
        for (int i = 0; i < 8; i++) o[i] = pack2v(t[i]);
        FENCE();
        const unsigned so = WS_ACT + (t4 * 86 + (unit0 + u) * 2) * 1024;
        st16(rs, lane * 16, so, u32x4{o[0], o[1], o[2], o[3]});
        st16(rs, lane * 16, so + 1024, u32x4{o[4], o[5], o[6], o[7]});
      }
    }
    STAMP(18);
    DRAIN(8);
    STAMP(19);
  }
  {
    f32x16 acc[2];
#pragma unroll
    for (int i = 0; i < 16; i++) { acc[0][i] = 0.f; acc[1][i] = 0.f; }
    int b5[2][3];
#pragma unroll
    for (int i = 0; i < 6; i++) b5[i / 3][i % 3] = opq((int)(bsq[i / 3] - smem) + kh * 2048 + (i % 3) * 13 * 4096);
    BARRIER();
    gemm_direct<43, 2, 4, Run5>(rs, lane * 16, [&](int pos) { int ks = pos + off5; ks = ks >= 43 ? ks - 43 : ks; return WS_ACT + (tile * 86 + kh * 43 + ks) * 1024; },
                          [&](int pos, int u) { const int sp = pos >= 39 ? pos - 39 : pos; return tr_frag(smem + b5[u][sp / 13] + (sp % 13) * 4096, 1024); }, pre, acc,
                                [&]() { BARRIER(); STAMP(20); },
                                nohook);
    STAMP(21);
    BARRIER();
    exchange(acc, 0);
#pragma unroll
    for (int s = 0; s < 2; s++) {
      u32x4 o;
#pragma unroll
      for (int j = 0; j < 4; j++) o[j] = pack2(acc[0][8 * s + 2 * j] + bflo(res[s][j]), acc[0][8 * s + 2 * j + 1] + bfhi(res[s][j]));
      __builtin_amdgcn_raw_buffer_store_b128(o, __builtin_amdgcn_make_buffer_rsrc(out, 0, 2048 * D_ * 2, 0x00020000), (unsigned)rowoff * 2 + 32 * s, 0, OUTAUX);
    }
    STAMP(22);
    STAMP_FLUSH();
  }
  } else {
    const unsigned bv = b1[(unit0 + max(lw - 1, 0)) * 32 + r32];
    const unsigned selv = ((lane & 15) == 4 * blk) ? 0x3f803f80u : 0u;
    const u32x4 sel = {selv, selv, selv, selv};
    f32x4 c4 = {0.f, 0.f, 0.f, 0.f};
    auto colsum = [&](int k0) {
      u32x4 f[16];
      const int bc = opq(bb1 + k0 * 3072 + max(lw - 1, 0) * 64);
#pragma unroll
      for (int i = 0; i < 16; i++) f[i] = tr_frag(smem + bc + i * 3072, 768);
#pragma unroll
      for (int i = 0; i < 16; i++) c4 = mfma16(sel, f[i], c4);
    };
    if (wave == 4) wait_flags(ws, WS_F2 + (rg * 8 + (lane & 7)) * 128, tag); else colsum(16);
    BARRIER();
    BARRIER();
    BARRIER();
    BARRIER();
    STAMP(14);
    if (wave == 4) { publish(ws, WS_F3 + (rg * 8 + cs) * 128, tag, lane); wait_flags(ws, WS_F3 + (rp * 16 + (lane & 15)) * 128, tag); }
    else colsum(0);
    if (lw > 0 && h == 0) {
      const unsigned ch = pack2(c4[0], 0.f), cl = pack2(c4[0] - bflo(ch), 0.f);
      *(LAS u32x4*)(smem + L_RES + 256 + (lw - 1) * 512 + r32 * 16) = u32x4{ch | (cl << 16), ch | (bv << 16), bv, 0u};
    }
    STAMP(15);
    BARRIER();
    lds_spin<SL4>(L_RES + p * 4, 34);
    LAS char* dst = smem + (2 * (15 + (lw >> 1) * 14) + (lw & 1)) * 2048 + lane * 16;
    LAS char* dst2 = (lw >> 1) ? dst - 78 * 2048 : dst;
#pragma unroll
    for (int i = 0; i < 14; i++) { LAS char* d = (i >= 10 ? dst2 : dst) + i * 4096; *(LAS u32x4*)d = stg(2 * i); *(LAS u32x4*)(d + 1024) = stg(2 * i + 1); }
    STAMP(19);
    BARRIER();
    if (wave == 4) {
      publish(ws, WS_F4 + (rp * 16 + sl) * 128, tag, lane); wait_flags(ws, WS_F4 + ((rg >> 1) * 16 + (lane & 15)) * 128, tag);
      if (b == 0 && lane == 0) *(u64*)(ws + WS_EP) = tag;
    }
    BARRIER();
    STAMP_FLUSH();
  }
}

extern "C" void kernel_launch(void* const* d_in, const int* in_sizes, int n_in, void* d_out, int out_size, void* d_ws, size_t ws_size, hipStream_t stream) {
  fused<<<256, 512, 0, stream>>>((const u16*)d_in[0], (const u16*)d_in[1], (const u16*)d_in[2], (const u16*)d_in[4], (const u16*)d_in[5], (const u16*)d_in[6], (char*)d_ws, (u16*)d_out);
}
```
